```python
import torch

def _softmax(x, axis=-1):
    return torch.softmax(x, dim=axis)
def _gelu(x):
    return torch.nn.functional.gelu(x, approximate="tanh")
def _silu(x):
    return torch.nn.functional.silu(x)
def _sigmoid(x):
    return torch.sigmoid(x)
def _layernorm(x, axis=-1, eps=1e-5):
    m = x.mean(dim=axis, keepdim=True)
    v = x.var(dim=axis, keepdim=True, unbiased=False)
    return (x - m) / torch.sqrt(v + eps)
def _scatter_add(tgt, idx, src, axis=0):
    return tgt.clone().index_add_(axis, idx, src)
def _segment_sum(src, seg, n):
    out = torch.zeros((n,) + src.shape[1:], dtype=src.dtype)
    return out.index_add_(0, seg, src)
def _pad(x, pw):
    flat = tuple(v for pair in reversed(pw) for v in pair)
    return torch.nn.functional.pad(x, flat)

kernel_name = 'parallel_sum__linear_kernel_hedgehog__dense_activation_gelu__prompt'

def setup_inputs():

    _w = (lambda a: (lambda t: t.to(torch.bfloat16) if t.is_floating_point() else t)(torch.as_tensor(a)))
    def _A_setup():
        B, S, D, H = 1, 2048, 1024, 32
        Dh = D // H
        return {
            "x": _w((lambda *s: torch.randn(*s, dtype=torch.bfloat16) * 0.05)(B, S, D)),
            "wqkv": _w((lambda *s: torch.randn(*s, dtype=torch.bfloat16) * 0.2)(D, 3 * D)),
            "w_phi": _w((lambda *s: torch.randn(*s, dtype=torch.bfloat16) * 0.2)(H, Dh, Dh) * (Dh ** -0.5)),
            "wo": _w((lambda *s: torch.randn(*s, dtype=torch.bfloat16) * 0.05)(D, D)),
        }
    _A_in = _A_setup()

    def _B_setup():
        B, S, D, F = 1, 2048, 1024, 3584
        return {
            "x": _w((lambda *s: torch.randn(*s, dtype=torch.bfloat16) * 0.05)(B, S, D)),
            "w1": _w((lambda *s: torch.randn(*s, dtype=torch.bfloat16) * 0.8)(D, F) * (D ** -0.5)),
            "b1": _w((lambda *s: torch.randn(*s, dtype=torch.bfloat16) * 0.8)(F) * 0.01),
            "w2": _w((lambda *s: torch.randn(*s, dtype=torch.bfloat16) * 0.2)(F, D) * (F ** -0.5)),
            "b2": _w((lambda *s: torch.randn(*s, dtype=torch.bfloat16) * 0.2)(D) * 0.01),
        }
    _B_in = _B_setup()

    return {
        "A_x": _A_in["x"],
        "A_wqkv": _A_in["wqkv"],
        "A_w_phi": _A_in["w_phi"],
        "A_wo": _A_in["wo"],
        "B_x": _B_in["x"],
        "B_w1": _B_in["w1"],
        "B_b1": _B_in["b1"],
        "B_w2": _B_in["w2"],
        "B_b2": _B_in["b2"],
    }

def reference(A_x, A_wqkv, A_w_phi, A_wo, B_x, B_w1, B_b1, B_w2, B_b2):
    def _A_ref(x, wqkv, w_phi, wo):
        B, S, D = x.shape
        H = 32; Dh = D // H
        qkv = (lambda x, *d: x.permute(*d))((x @ wqkv).reshape(B, S, 3, H, Dh), 2, 0, 3, 1, 4)
        q, k, v = qkv[0], qkv[1], qkv[2]
        pq = torch.einsum("bhsd,hdf->bhsf", q, w_phi)
        pk = torch.einsum("bhsd,hdf->bhsf", k, w_phi)
        phi_q = torch.concatenate([_softmax(pq, axis=-1), _softmax(-pq, axis=-1)], axis=-1)
        phi_k = torch.concatenate([_softmax(pk, axis=-1), _softmax(-pk, axis=-1)], axis=-1)
        kv = torch.einsum("bhsf,bhsd->bhfd", phi_k, v)
        z  = torch.einsum("bhsf->bhf", phi_k)
        num = torch.einsum("bhqf,bhfd->bhqd", phi_q, kv)
        den = torch.einsum("bhqf,bhf->bhq", phi_q, z)[..., None] + 1e-6
        o = num / den
        return (lambda x, *d: x.permute(*d))(o, 0, 2, 1, 3).reshape(B, S, D) @ wo

    def _B_ref(x, w1, b1, w2, b2):
        h = torch.einsum("bsd,df->bsf", x, w1) + b1
        h = _gelu(h)
        return torch.einsum("bsf,fd->bsd", h, w2) + b2

    x = A_x
    xn = _layernorm(x, axis=-1)
    return x + _A_ref(xn, A_wqkv, A_w_phi, A_wo) + _B_ref(xn, B_w1, B_b1, B_w2, B_b2)
```

```cpp
#include <hip/hip_runtime.h>
#include <hip/hip_bf16.h>
#include <stdint.h>

typedef __bf16 bf16x8 __attribute__((ext_vector_type(8)));
typedef __bf16 bf16x4 __attribute__((ext_vector_type(4)));
typedef __bf16 bf16x2 __attribute__((ext_vector_type(2)));
typedef float f32x4 __attribute__((ext_vector_type(4)));
typedef float f32x2 __attribute__((ext_vector_type(2)));
typedef unsigned u32x4 __attribute__((ext_vector_type(4)));
typedef unsigned u32x2 __attribute__((ext_vector_type(2)));
typedef unsigned short u16;
#define LAS __attribute__((address_space(3)))
#define GAS __attribute__((address_space(1)))
#define DI __device__ __forceinline__

constexpr int S_ = 2048, D_ = 1024, F_ = 3584, H_ = 32;
constexpr int KCAT = F_ + D_;

constexpr size_t WS_XN   = 0;
constexpr size_t WS_ACT  = WS_XN + (size_t)S_ * D_ * 2;
constexpr size_t WS_PQ   = WS_ACT + (size_t)S_ * KCAT * 2;
constexpr size_t WS_KVP  = WS_PQ + (size_t)S_ * D_ * 2;
constexpr size_t WS_PART = WS_KVP + (size_t)8 * 32 * 3072 * 4;

DI float bf2f(u16 v) { return __uint_as_float((unsigned)v << 16); }
DI float rbf(float x) { return (float)(__bf16)x; }
DI unsigned pk2(float lo, float hi) { bf16x2 v; v[0] = (__bf16)lo; v[1] = (__bf16)hi; return __builtin_bit_cast(unsigned, v); }

#define MFMA16(a, b, c) __builtin_amdgcn_mfma_f32_16x16x32_bf16((a), (b), (c), 0, 0, 0)
#define WAIT_V(n) asm volatile("s_waitcnt vmcnt(%0)" ::"n"(n) : "memory")
#define WAIT_L(n) asm volatile("s_waitcnt lgkmcnt(%0)" ::"n"(n) : "memory")
#define BAR() do { asm volatile("" ::: "memory"); __builtin_amdgcn_s_barrier(); asm volatile("" ::: "memory"); } while (0)

DI void glds16(const void* g, LAS void* l) {
  __builtin_amdgcn_global_load_lds((const GAS void*)g, l, 16, 0, 0);
}
DI bf16x8 lds_b128(LAS char* p) { return *(LAS bf16x8*)p; }
DI bf16x4 lds_tr(LAS char* p) { return __builtin_amdgcn_ds_read_tr16_b64_v4bf16((LAS bf16x4*)p); }
DI bf16x8 cat8(bf16x4 a, bf16x4 b) { return __builtin_shufflevector(a, b, 0, 1, 2, 3, 4, 5, 6, 7); }
DI bf16x8 pack8(f32x4 a, f32x4 b) {
  bf16x8 v;
  v[0] = (__bf16)a[0]; v[1] = (__bf16)a[1]; v[2] = (__bf16)a[2]; v[3] = (__bf16)a[3];
  v[4] = (__bf16)b[0]; v[5] = (__bf16)b[1]; v[6] = (__bf16)b[2]; v[7] = (__bf16)b[3];
  return v;
}

__global__ __launch_bounds__(256) void k_ln(const u16* __restrict__ x, u16* __restrict__ xn) {
  const int lane = threadIdx.x & 63, row = blockIdx.x * 4 + (threadIdx.x >> 6);
  const u32x4* src = (const u32x4*)(x + (size_t)row * D_);
  u32x4 v0 = src[lane], v1 = src[64 + lane];
  float f[16];
#pragma unroll
  for (int i = 0; i < 4; ++i) {
    f[2 * i] = __uint_as_float(v0[i] << 16); f[2 * i + 1] = __uint_as_float(v0[i] & 0xffff0000u);
    f[8 + 2 * i] = __uint_as_float(v1[i] << 16); f[8 + 2 * i + 1] = __uint_as_float(v1[i] & 0xffff0000u);
  }
  float s = 0.f;
#pragma unroll
  for (int i = 0; i < 16; ++i) s += f[i];
#pragma unroll
  for (int o = 32; o > 0; o >>= 1) s += __shfl_xor(s, o);
  const float mean = s * (1.0f / D_);
  float q = 0.f;
#pragma unroll
  for (int i = 0; i < 16; ++i) { const float d = f[i] - mean; q += d * d; }
#pragma unroll
  for (int o = 32; o > 0; o >>= 1) q += __shfl_xor(q, o);
  const float mb = rbf(mean), vb = rbf(q * (1.0f / D_));
  const float sd = rbf(sqrtf(rbf(vb + 1e-5f)));
  u32x4 o0, o1;
#pragma unroll
  for (int i = 0; i < 4; ++i) {
    o0[i] = pk2(rbf(f[2 * i] - mb) / sd, rbf(f[2 * i + 1] - mb) / sd);
    o1[i] = pk2(rbf(f[8 + 2 * i] - mb) / sd, rbf(f[8 + 2 * i + 1] - mb) / sd);
  }
  u32x4* dst = (u32x4*)(xn + (size_t)row * D_);
  dst[lane] = o0; dst[64 + lane] = o1;
}

constexpr int G1_STAGE = 65536;
constexpr int G1_LDS = 2 * G1_STAGE + 4096;

DI float gelu_tanh(float x) {
  const float t = x * x;
  const float a = x * __builtin_fmaf(t, -0.10294324f, -2.3022082f);
  const float e = __builtin_amdgcn_exp2f(a);
  return x * __builtin_amdgcn_rcpf(1.0f + e);
}

template <bool QKV>
DI void gemm1_body(const u16* __restrict__ xn, const u16* __restrict__ W, int ldw, int wcol0  ,
                   int woff, int panel, int tcol, const u16* __restrict__ b1, const u16* __restrict__ wphi,
                   u16* __restrict__ act, u16* __restrict__ pqf, float* __restrict__ kvp, LAS char* lds) {
  constexpr int NG = QKV ? 3 : 4;
  constexpr int NT = QKV ? 6 : 7;
  constexpr int NLB = QKV ? 3 : 4;
  constexpr int NL = 4 + NLB;
  const int tid = threadIdx.x, lane = tid & 63;
  const int wid = __builtin_amdgcn_readfirstlane(tid >> 6);
  const int wr = wid >> 1, wc = wid & 1;
  const int g = lane >> 4, i16 = lane & 15, q4 = i16 >> 2, p4 = lane & 3;

  const int arow = wid * 8 + (lane >> 3);
  const u16* asrc = xn + (size_t)(panel * 256 + arow) * D_ + (((lane & 7) ^ ((arow >> 1) & 7)) << 3);
  const u16* bsrc[NLB];
#pragma unroll
  for (int j = 0; j < NLB; ++j) {
    const int pb = wid * NLB + j, kg = pb / NG, cg = pb % NG;
    const int r = lane >> 3, sw = ((r >> 1) & 1) | ((kg & 1) << 1);
    const int cc = (lane & 7) ^ (sw << 1);
    const int col = QKV ? (cg * D_ + wcol0) : (wcol0 + cg * 64);
    bsrc[j] = W + (size_t)(kg * 8 + r) * ldw + col + cc * 8;
  }
  auto stage = [&](int kt, int buf) {
    LAS char* base = lds + buf * G1_STAGE;
#pragma unroll
    for (int j = 0; j < 4; ++j) glds16(asrc + (size_t)j * 64 * D_ + kt * 64, base + (wid + 8 * j) * 1024);
#pragma unroll
    for (int j = 0; j < NLB; ++j) glds16(bsrc[j] + (size_t)kt * 64 * ldw, base + 32768 + (wid * NLB + j) * 1024);
  };

  int aoff[2];
#pragma unroll
  for (int ks = 0; ks < 2; ++ks) aoff[ks] = (wr * 64 + i16) * 128 + (((4 * ks + g) ^ (i16 >> 1)) << 4);
  int boff[NT];
#pragma unroll
  for (int j = 0; j < NT; ++j) {
    const int n0 = QKV ? ((j >> 1) * 64 + wc * 32 + (j & 1) * 16) : (woff + wc * 112 + j * 16);
    const int sw = (q4 >> 1) | ((g & 1) << 1);
    boff[j] = 32768 + (g * NG + (n0 >> 6)) * 1024 + q4 * 128 + (((((n0 & 63) >> 3) ^ (sw << 1)) + (p4 >> 1)) << 4) + (p4 & 1) * 8;
  }

  if (QKV) {
    if (tid < 256) {
      const u32x4 w = *(const u32x4*)(wphi + (size_t)tcol * 2 * 1024 + tid * 8);
      *(LAS u32x4*)(lds + 2 * G1_STAGE + tid * 16) = w;
    }
  }

  f32x4 acc[4][NT];
#pragma unroll
  for (int m = 0; m < 4; ++m)
#pragma unroll
    for (int n = 0; n < NT; ++n) acc[m][n] = (f32x4){0.f, 0.f, 0.f, 0.f};

  bf16x8 xa[4], xb[NT], ya[4], yb[NT];
  auto rdfr = [&](bf16x8 (&fa)[4], bf16x8 (&fb)[NT], int buf, int ks) {
    LAS char* base = lds + buf * G1_STAGE;
#pragma unroll
    for (int m = 0; m < 4; ++m) fa[m] = lds_b128(base + aoff[ks] + m * 2048);
#pragma unroll
    for (int n = 0; n < NT; ++n) {
      LAS char* p = base + boff[n] + ks * 4 * NG * 1024;
      fb[n] = cat8(lds_tr(p), lds_tr(p + 512));
    }
  };
  auto mma = [&](bf16x8 (&fa)[4], bf16x8 (&fb)[NT]) {
#pragma unroll
    for (int n = 0; n < NT; ++n)
#pragma unroll
      for (int m = 0; m < 4; ++m) {
        if (QKV && n >= 4) acc[m][n] = MFMA16(fa[m], fb[n], acc[m][n]);
        else               acc[m][n] = MFMA16(fb[n], fa[m], acc[m][n]);
      }
  };

  stage(0, 0);
  stage(1, 1);
  WAIT_V(NL); WAIT_L(0); BAR();
  rdfr(xa, xb, 0, 0);
  constexpr int NKT = D_ / 64;
#pragma unroll 1
  for (int t = 0; t < NKT; ++t) {
    const int buf = t & 1;
    rdfr(ya, yb, buf, 1);
    mma(xa, xb);
    WAIT_L(0); WAIT_V(0); BAR();
    if (t + 2 < NKT) stage(t + 2, buf);
    if (t + 1 < NKT) rdfr(xa, xb, buf ^ 1, 0);
    mma(ya, yb);
  }

  if (!QKV) {
    const int ncol0 = tcol * 224 + wc * 112 + 4 * g;
#pragma unroll
    for (int n = 0; n < NT; ++n) {
      const u32x2 bb = *(const u32x2*)(b1 + ncol0 + n * 16);
      const float b0 = __uint_as_float(bb[0] << 16), b1f = __uint_as_float(bb[0] & 0xffff0000u);
      const float b2f = __uint_as_float(bb[1] << 16), b3 = __uint_as_float(bb[1] & 0xffff0000u);
#pragma unroll
      for (int m = 0; m < 4; ++m) {
        const int row = panel * 256 + wr * 64 + m * 16 + i16;
        u32x2 o;
        o[0] = pk2(gelu_tanh(acc[m][n][0] + b0), gelu_tanh(acc[m][n][1] + b1f));
        o[1] = pk2(gelu_tanh(acc[m][n][2] + b2f), gelu_tanh(acc[m][n][3] + b3));
        *(u32x2*)(act + (size_t)row * KCAT + ncol0 + n * 16) = o;
      }
    }
  } else {
    const int head = tcol * 2 + wc;
    LAS char* wp = lds + 2 * G1_STAGE + wc * 2048;
    bf16x8 wf[2];
#pragma unroll
    for (int ft = 0; ft < 2; ++ft) {
      LAS char* p = wp + (4 * g + q4) * 64 + (16 * ft + 4 * p4) * 2;
      wf[ft] = cat8(lds_tr(p), lds_tr(p + 16 * 64));
    }
    const f32x4 z4 = {0.f, 0.f, 0.f, 0.f};
#pragma unroll
    for (int m = 0; m < 4; ++m) {
      const bf16x8 bq = pack8(acc[m][0], acc[m][1]);
      const f32x4 p0 = MFMA16(wf[0], bq, z4), p1 = MFMA16(wf[1], bq, z4);
      const bf16x8 o = pack8(p0, p1);
      const int stile = panel * 16 + wr * 4 + m;
      *(bf16x8*)(pqf + ((size_t)(stile * H_ + head) * 64 + lane) * 8) = o;
    }
    BAR();
    LAS char* sc = lds + wid * 8704;
#pragma unroll
    for (int m = 0; m < 4; ++m) {
      const bf16x8 bk = pack8(acc[m][2], acc[m][3]);
      const f32x4 p0 = MFMA16(wf[0], bk, z4), p1 = MFMA16(wf[1], bk, z4);
      float v[8];
#pragma unroll
      for (int r = 0; r < 4; ++r) { v[r] = rbf(p0[r]); v[4 + r] = rbf(p1[r]); }
      float mx = v[0], mn = v[0];
#pragma unroll
      for (int r = 1; r < 8; ++r) { mx = fmaxf(mx, v[r]); mn = fminf(mn, v[r]); }
      mx = fmaxf(mx, __shfl_xor(mx, 16)); mx = fmaxf(mx, __shfl_xor(mx, 32));
      mn = fminf(mn, __shfl_xor(mn, 16)); mn = fminf(mn, __shfl_xor(mn, 32));
      float ep[8], en[8], sp = 0.f, sn = 0.f;
#pragma unroll
      for (int r = 0; r < 8; ++r) {
        ep[r] = __builtin_amdgcn_exp2f((v[r] - mx) * 1.44269504f); sp += ep[r];
        en[r] = __builtin_amdgcn_exp2f((mn - v[r]) * 1.44269504f); sn += en[r];
      }
      sp += __shfl_xor(sp, 16); sp += __shfl_xor(sp, 32);
      sn += __shfl_xor(sn, 16); sn += __shfl_xor(sn, 32);
      const float rp = 1.0f / sp, rn = 1.0f / sn;
      LAS char* rowp = sc + (16 * m + i16) * 136 + 8 * g;
      u32x2 w;
      w[0] = pk2(ep[0] * rp, ep[1] * rp); w[1] = pk2(ep[2] * rp, ep[3] * rp); *(LAS u32x2*)(rowp) = w;
      w[0] = pk2(ep[4] * rp, ep[5] * rp); w[1] = pk2(ep[6] * rp, ep[7] * rp); *(LAS u32x2*)(rowp + 32) = w;
      w[0] = pk2(en[0] * rn, en[1] * rn); w[1] = pk2(en[2] * rn, en[3] * rn); *(LAS u32x2*)(rowp + 64) = w;
      w[0] = pk2(en[4] * rn, en[5] * rn); w[1] = pk2(en[6] * rn, en[7] * rn); *(LAS u32x2*)(rowp + 96) = w;
    }
    WAIT_L(0);
    f32x4 kv[4][3];
#pragma unroll
    for (int ft = 0; ft < 4; ++ft)
#pragma unroll
      for (int d = 0; d < 3; ++d) kv[ft][d] = z4;
    bf16x8 ones;
#pragma unroll
    for (int j = 0; j < 8; ++j) ones[j] = (__bf16)1.0f;
#pragma unroll
    for (int ks = 0; ks < 2; ++ks) {
      const bf16x8 bv0 = pack8(acc[2 * ks][4], acc[2 * ks + 1][4]);
      const bf16x8 bv1 = pack8(acc[2 * ks][5], acc[2 * ks + 1][5]);
#pragma unroll
      for (int ft = 0; ft < 4; ++ft) {
        LAS char* p = sc + (32 * ks + 4 * g + q4) * 136 + (16 * ft + 4 * p4) * 2;
        const bf16x8 a = cat8(lds_tr(p), lds_tr(p + 16 * 136));
        kv[ft][0] = MFMA16(a, bv0, kv[ft][0]);
        kv[ft][1] = MFMA16(a, bv1, kv[ft][1]);
        kv[ft][2] = MFMA16(a, ones, kv[ft][2]);
      }
    }
    WAIT_L(0); BAR();
    {
      LAS char* pp = lds + wid * 12288 + lane * 32;
#pragma unroll
      for (int sg = 0; sg < 2; ++sg)
#pragma unroll
        for (int d = 0; d < 3; ++d) {
          *(LAS f32x4*)(pp + (sg * 3 + d) * 2048) = kv[2 * sg][d];
          *(LAS f32x4*)(pp + (sg * 3 + d) * 2048 + 16) = kv[2 * sg + 1][d];
        }
    }
    WAIT_L(0); BAR();
#pragma unroll
    for (int j = 0; j < 3; ++j) {
      const int idx = tid + 512 * j;
      const int hc = idx / 768, e = idx % 768;
      f32x4 s = *(LAS f32x4*)(lds + (0 * 2 + hc) * 12288 + e * 16);
      s += *(LAS f32x4*)(lds + (1 * 2 + hc) * 12288 + e * 16);
      s += *(LAS f32x4*)(lds + (2 * 2 + hc) * 12288 + e * 16);
      s += *(LAS f32x4*)(lds + (3 * 2 + hc) * 12288 + e * 16);
      *(f32x4*)(kvp + ((size_t)(panel * H_ + tcol * 2 + hc) * 3072) + e * 4) = s;
    }
  }
}

__global__ __launch_bounds__(512, 2) void k_gemm1(const u16* __restrict__ xn, const u16* __restrict__ wqkv, const u16* __restrict__ w1,
                                                  const u16* __restrict__ b1, const u16* __restrict__ wphi,
                                                  u16* __restrict__ act, u16* __restrict__ pqf, float* __restrict__ kvp) {
  extern __shared__ __attribute__((aligned(16))) char smem[];
  LAS char* lds = (LAS char*)smem;
  const int b = blockIdx.x, xcd = b & 7, idx = b >> 3;
  const int panel = idx & 7, ct = xcd * 4 + (idx >> 3);
  const int kind = ct & 1, tcol = ct >> 1;
  if (kind == 0) {
    gemm1_body<true>(xn, wqkv, 3 * D_, tcol * 64, 0, panel, tcol, b1, wphi, act, pqf, kvp, lds);
  } else {
    const int c0 = tcol * 224, woff = c0 & 63;
    gemm1_body<false>(xn, w1, F_, c0 - woff, woff, panel, tcol, b1, wphi, act, pqf, kvp, lds);
  }
}

__global__ __launch_bounds__(256) void k_attn_o(const u16* __restrict__ pqf, const float* __restrict__ kvp, u16* __restrict__ act) {
  __shared__ __attribute__((aligned(16))) u16 kvs[3072];
  const int tid = threadIdx.x, lane = tid & 63, wid = tid >> 6;
  const int head = blockIdx.x & 31, rp = blockIdx.x >> 5;
  const int g = lane >> 4, i16 = lane & 15;
  bf16x8 pq[4];
#pragma unroll
  for (int m = 0; m < 4; ++m) pq[m] = *(const bf16x8*)(pqf + ((size_t)((rp * 16 + wid * 4 + m) * H_ + head) * 64 + lane) * 8);
#pragma unroll
  for (int j = 0; j < 3; ++j) {
    const int e = tid + 256 * j;
    f32x4 s = {0.f, 0.f, 0.f, 0.f};
#pragma unroll
    for (int p = 0; p < 8; ++p) s += *(const f32x4*)(kvp + (size_t)(p * H_ + head) * 3072 + e * 4);
    u32x2 w; w[0] = pk2(s[0], s[1]); w[1] = pk2(s[2], s[3]);
    *(u32x2*)(kvs + e * 4) = w;
  }
  __syncthreads();
  bf16x8 a[2][3];
#pragma unroll
  for (int sg = 0; sg < 2; ++sg)
#pragma unroll
    for (int d = 0; d < 3; ++d) a[sg][d] = *(const bf16x8*)(kvs + ((sg * 3 + d) * 64 + lane) * 8);
  const f32x4 z4 = {0.f, 0.f, 0.f, 0.f};
#pragma unroll
  for (int m = 0; m < 4; ++m) {
    float v[8];
#pragma unroll
    for (int r = 0; r < 8; ++r) v[r] = (float)pq[m][r];
    float mx = v[0], mn = v[0];
#pragma unroll
    for (int r = 1; r < 8; ++r) { mx = fmaxf(mx, v[r]); mn = fminf(mn, v[r]); }
    mx = fmaxf(mx, __shfl_xor(mx, 16)); mx = fmaxf(mx, __shfl_xor(mx, 32));
    mn = fminf(mn, __shfl_xor(mn, 16)); mn = fminf(mn, __shfl_xor(mn, 32));
    float ep[8], en[8], sp = 0.f, sn = 0.f;
#pragma unroll
    for (int r = 0; r < 8; ++r) {
      ep[r] = __builtin_amdgcn_exp2f((v[r] - mx) * 1.44269504f); sp += ep[r];
      en[r] = __builtin_amdgcn_exp2f((mn - v[r]) * 1.44269504f); sn += en[r];
    }
    sp += __shfl_xor(sp, 16); sp += __shfl_xor(sp, 32);
    sn += __shfl_xor(sn, 16); sn += __shfl_xor(sn, 32);
    const float rpv = 1.0f / sp, rnv = 1.0f / sn;
    bf16x8 bp, bn;
#pragma unroll
    for (int r = 0; r < 8; ++r) { bp[r] = (__bf16)(ep[r] * rpv); bn[r] = (__bf16)(en[r] * rnv); }
    f32x4 o0 = MFMA16(a[0][0], bp, z4), o1 = MFMA16(a[0][1], bp, z4), dn = MFMA16(a[0][2], bp, z4);
    o0 = MFMA16(a[1][0], bn, o0); o1 = MFMA16(a[1][1], bn, o1); dn = MFMA16(a[1][2], bn, dn);
    const float rd = 1.0f / (dn[0] + 1e-6f);
    const int row = rp * 256 + wid * 64 + m * 16 + i16;
    u16* dst = act + (size_t)row * KCAT + F_ + head * 32 + 4 * g;
    u32x2 w;
    w[0] = pk2(o0[0] * rd, o0[1] * rd); w[1] = pk2(o0[2] * rd, o0[3] * rd); *(u32x2*)(dst) = w;
    w[0] = pk2(o1[0] * rd, o1[1] * rd); w[1] = pk2(o1[2] * rd, o1[3] * rd); *(u32x2*)(dst + 16) = w;
  }
}

constexpr int G2_STAGE = 32768, G2_NST = 4;
constexpr int G2_LDS = G2_STAGE * G2_NST;

__global__ __launch_bounds__(512, 2) void k_gemm2(const u16* __restrict__ act, const u16* __restrict__ w2, const u16* __restrict__ wo,
                                                  float* __restrict__ part) {
  extern __shared__ __attribute__((aligned(16))) char smem[];
  LAS char* lds = (LAS char*)smem;
  const int tid = threadIdx.x, lane = tid & 63;
  const int wid = __builtin_amdgcn_readfirstlane(tid >> 6);
  const int kh = wid >> 2, wr = (wid >> 1) & 1, wc = wid & 1;
  const int g = lane >> 4, i16 = lane & 15, q4 = i16 >> 2, p4 = lane & 3;
  const int b = blockIdx.x, grp = b & 7, idx = b >> 3;
  const int split = grp & 1, pm = (grp >> 1) * 4 + (idx & 3), pn = idx >> 2;
  constexpr int NKT = 36;
  const int kt0 = split * NKT;
  const int arow = wid * 8 + (lane >> 3);
  const u16* asrc = act + (size_t)(pm * 128 + arow) * KCAT + (size_t)kt0 * 64 + (((lane & 7) ^ ((arow >> 1) & 7)) << 3);
  int boffs[2];
#pragma unroll
  for (int j = 0; j < 2; ++j) {
    const int kg = wid, cg = j;
    const int r = lane >> 3, sw = ((r >> 1) & 1) | ((kg & 1) << 1);
    boffs[j] = (kg * 8 + r) * D_ + pn * 128 + cg * 64 + (((lane & 7) ^ (sw << 1)) << 3);
  }
  auto stage = [&](int t, int buf) {
    LAS char* base = lds + buf * G2_STAGE;
    const int kt = kt0 + t;
    const u16* wb = kt < 56 ? w2 + (size_t)kt * 64 * D_ : wo + (size_t)(kt - 56) * 64 * D_;
#pragma unroll
    for (int j = 0; j < 2; ++j) glds16(asrc + (size_t)j * 64 * KCAT + t * 64, base + (wid + 8 * j) * 1024);
#pragma unroll
    for (int j = 0; j < 2; ++j) glds16(wb + boffs[j], base + 16384 + (wid * 2 + j) * 1024);
  };
  const int aoff = (wr * 64 + i16) * 128 + (((4 * kh + g) ^ (i16 >> 1)) << 4);
  int boff[4];
#pragma unroll
  for (int j = 0; j < 4; ++j) {
    const int sw = (q4 >> 1) | ((g & 1) << 1);
    boff[j] = 16384 + ((kh * 4 + g) * 2 + wc) * 1024 + q4 * 128 + ((((2 * j) ^ (sw << 1)) + (p4 >> 1)) << 4) + (p4 & 1) * 8;
  }
  f32x4 acc[4][4];
#pragma unroll
  for (int m = 0; m < 4; ++m)
#pragma unroll
    for (int n = 0; n < 4; ++n) acc[m][n] = (f32x4){0.f, 0.f, 0.f, 0.f};
  stage(0, 0); stage(1, 1); stage(2, 2);
#pragma unroll 1
  for (int t = 0; t < NKT; ++t) {
    if (t + 2 < NKT) WAIT_V(8); else if (t + 1 < NKT) WAIT_V(4); else WAIT_V(0);
    BAR();
    if (t + 3 < NKT) stage(t + 3, (t + 3) & 3);
    LAS char* base = lds + (t & 3) * G2_STAGE;
    bf16x8 fa[4], fb[4];
#pragma unroll
    for (int m = 0; m < 4; ++m) fa[m] = lds_b128(base + aoff + m * 2048);
#pragma unroll
    for (int n = 0; n < 4; ++n) fb[n] = cat8(lds_tr(base + boff[n]), lds_tr(base + boff[n] + 512));
#pragma unroll
    for (int n = 0; n < 4; ++n)
#pragma unroll
      for (int m = 0; m < 4; ++m) acc[m][n] = MFMA16(fb[n], fa[m], acc[m][n]);
    WAIT_L(0);
  }
  BAR();
  if (kh == 1) {
    LAS char* pp = lds + (wid & 3) * 16384 + lane * 16;
#pragma unroll
    for (int m = 0; m < 4; ++m)
#pragma unroll
      for (int n = 0; n < 4; ++n) *(LAS f32x4*)(pp + (m * 4 + n) * 1024) = acc[m][n];
  }
  WAIT_L(0); BAR();
  if (kh == 0) {
    LAS char* pp = lds + (wid & 3) * 16384 + lane * 16;
    float* dst = part + (size_t)split * S_ * D_;
#pragma unroll
    for (int m = 0; m < 4; ++m)
#pragma unroll
      for (int n = 0; n < 4; ++n) {
        const f32x4 v = acc[m][n] + *(LAS f32x4*)(pp + (m * 4 + n) * 1024);
        const int row = pm * 128 + wr * 64 + m * 16 + i16, col = pn * 128 + wc * 64 + n * 16 + 4 * g;
        *(f32x4*)(dst + (size_t)row * D_ + col) = v;
      }
  }
}

__global__ __launch_bounds__(256) void k_final(const u16* __restrict__ x, const u16* __restrict__ b2, const float* __restrict__ part, u16* __restrict__ out) {
  const size_t e = ((size_t)blockIdx.x * 256 + threadIdx.x) * 8;
  const int col = (int)(e & (D_ - 1));
  const u32x4 xv = *(const u32x4*)(x + e), bv = *(const u32x4*)(b2 + col);
  const f32x4 a0 = *(const f32x4*)(part + e), a1 = *(const f32x4*)(part + e + 4);
  const f32x4 c0 = *(const f32x4*)(part + (size_t)S_ * D_ + e), c1 = *(const f32x4*)(part + (size_t)S_ * D_ + e + 4);
  float r[8];
#pragma unroll
  for (int i = 0; i < 4; ++i) { r[i] = a0[i] + c0[i]; r[4 + i] = a1[i] + c1[i]; }
  u32x4 o;
#pragma unroll
  for (int i = 0; i < 4; ++i) {
    const float lo = r[2 * i] + __uint_as_float(xv[i] << 16) + __uint_as_float(bv[i] << 16);
    const float hi = r[2 * i + 1] + __uint_as_float(xv[i] & 0xffff0000u) + __uint_as_float(bv[i] & 0xffff0000u);
    o[i] = pk2(lo, hi);
  }
  *(u32x4*)(out + e) = o;
}

extern "C" void kernel_launch(void* const* d_in, const int* in_sizes, int n_in, void* d_out, int out_size, void* d_ws, size_t ws_size,
                              hipStream_t stream) {
  const u16* x = (const u16*)d_in[0];
  const u16* wqkv = (const u16*)d_in[1];
  const u16* wphi = (const u16*)d_in[2];
  const u16* wo = (const u16*)d_in[3];
  const u16* w1 = (const u16*)d_in[5];
  const u16* b1 = (const u16*)d_in[6];
  const u16* w2 = (const u16*)d_in[7];
  const u16* b2 = (const u16*)d_in[8];
  char* ws = (char*)d_ws;
  u16* xn = (u16*)(ws + WS_XN);
  u16* act = (u16*)(ws + WS_ACT);
  u16* pqf = (u16*)(ws + WS_PQ);
  float* kvp = (float*)(ws + WS_KVP);
  float* part = (float*)(ws + WS_PART);
  hipFuncSetAttribute((const void*)k_gemm1, hipFuncAttributeMaxDynamicSharedMemorySize, G1_LDS);
  hipFuncSetAttribute((const void*)k_gemm2, hipFuncAttributeMaxDynamicSharedMemorySize, G2_LDS);
  k_ln<<<S_ / 4, 256, 0, stream>>>(x, xn);
  k_gemm1<<<256, 512, G1_LDS, stream>>>(xn, wqkv, w1, b1, wphi, act, pqf, kvp);
  k_attn_o<<<256, 256, 0, stream>>>(pqf, kvp, act);
  k_gemm2<<<256, 512, G2_LDS, stream>>>(act, w2, wo, part);
  k_final<<<S_ * D_ / 8 / 256, 256, 0, stream>>>(x, b2, part, (u16*)d_out);
}
```

```cpp
#include <hip/hip_runtime.h>
#include <hip/hip_bf16.h>
#include <stdint.h>

typedef __bf16 bf16x8 __attribute__((ext_vector_type(8)));
typedef __bf16 bf16x4 __attribute__((ext_vector_type(4)));
typedef __bf16 bf16x2 __attribute__((ext_vector_type(2)));
typedef float f32x4 __attribute__((ext_vector_type(4)));
typedef float f32x2 __attribute__((ext_vector_type(2)));
typedef unsigned u32x4 __attribute__((ext_vector_type(4)));
typedef unsigned u32x2 __attribute__((ext_vector_type(2)));
typedef unsigned short u16;
#define LAS __attribute__((address_space(3)))
#define DI __device__ __forceinline__

constexpr int S_ = 2048, D_ = 1024, F_ = 3584, H_ = 32;
constexpr int KCAT = F_ + D_;
constexpr int NKG1 = D_ / 32;
constexpr int NKG2 = KCAT / 32;

constexpr size_t WS_XN   = 0;
constexpr size_t WS_ACT  = WS_XN + (size_t)S_ * D_ * 2;
constexpr size_t WS_KVX  = WS_ACT + (size_t)S_ * KCAT * 2;
constexpr int KVX_TILE = 8192 + 256;
constexpr size_t KVX_BYTES = (size_t)8 * 16 * KVX_TILE;
constexpr size_t WS_XCH  = WS_KVX + KVX_BYTES;

DI float rbf(float x) { return (float)(__bf16)x; }
DI unsigned pk2(float lo, float hi) { bf16x2 v; v[0] = (__bf16)lo; v[1] = (__bf16)hi; return __builtin_bit_cast(unsigned, v); }
DI float blo(unsigned u) { return __uint_as_float(u << 16); }
DI float bhi(unsigned u) { return __uint_as_float(u & 0xffff0000u); }

#define MFMA16(a, b, c) __builtin_amdgcn_mfma_f32_16x16x32_bf16((a), (b), (c), 0, 0, 0)
#define WAIT_V(n) asm volatile("s_waitcnt vmcnt(%0)" ::"n"(n) : "memory")
#define WAIT_L(n) do { static_assert((n) == 0, ""); asm volatile("" ::: "memory"); __builtin_amdgcn_s_waitcnt(0xC07F); asm volatile("" ::: "memory"); } while (0)
#define BAR() do { asm volatile("" ::: "memory"); __builtin_amdgcn_s_barrier(); asm volatile("" ::: "memory"); } while (0)
#define SCHED() __builtin_amdgcn_sched_barrier(0)
#define STAMP(K, I) do { } while (0)
template <int N> struct IC { static constexpr int v = N; };

DI void glds16(unsigned voff, const void* sbase, unsigned ldsaddr) {
  asm volatile("s_mov_b32 m0, %2\n\ts_nop 0\n\tglobal_load_lds_dwordx4 %0, %1" ::"v"(voff), "s"(sbase), "s"(ldsaddr) : "memory");
}
DI const char* uniform_ptr(const char* p) {
  const unsigned long long a = (unsigned long long)p;
  unsigned lo = __builtin_amdgcn_readfirstlane((unsigned)a), hi = __builtin_amdgcn_readfirstlane((unsigned)(a >> 32));
  asm volatile("s_nop 4" : "+s"(lo), "+s"(hi));
  return (const char*)(((unsigned long long)hi << 32) | lo);
}
DI LAS char* opaque(LAS char* p) { asm volatile("" : "+v"(p)); return p; }
DI bf16x8 lds_b128(LAS char* p) { return *(LAS bf16x8*)p; }
DI bf16x4 lds_tr(LAS char* p) { return __builtin_amdgcn_ds_read_tr16_b64_v4bf16((LAS bf16x4*)p); }
DI bf16x8 cat8(bf16x4 a, bf16x4 b) { return __builtin_shufflevector(a, b, 0, 1, 2, 3, 4, 5, 6, 7); }
DI bf16x8 pack8(f32x4 a, f32x4 b) {
  bf16x8 v;
  v[0] = (__bf16)a[0]; v[1] = (__bf16)a[1]; v[2] = (__bf16)a[2]; v[3] = (__bf16)a[3];
  v[4] = (__bf16)b[0]; v[5] = (__bf16)b[1]; v[6] = (__bf16)b[2]; v[7] = (__bf16)b[3];
  return v;
}

typedef __attribute__((address_space(1))) unsigned gu32;
DI void st_sc1(void* p, u32x4 v) { asm volatile("global_store_dwordx4 %0, %1, off sc1\n\ts_nop 1" ::"v"(p), "v"(v) : "memory"); }
DI unsigned umax4(u32x4 v, unsigned m) { return max(max(max(v[0], v[1]), v[2]), max(v[3], m)); }
DI bool fresh4(u32x4 v) { return umax4(v, 0u) != 0xffffffffu; }
#define XRED4(OP, a1, b1, a2, b2) asm volatile( \
    "s_nop 1\n\tv_permlane16_swap_b32 %0, %1\n\tv_permlane16_swap_b32 %2, %3\n\t" \
    OP " %0, %0, %1\n\t" OP " %2, %2, %3\n\tv_mov_b32 %1, %0\n\tv_mov_b32 %3, %2\n\ts_nop 0\n\t" \
    "v_permlane32_swap_b32 %0, %1\n\tv_permlane32_swap_b32 %2, %3\n\t" \
    OP " %0, %0, %1\n\t" OP " %2, %2, %3\n\tv_mov_b32 %1, %0\n\tv_mov_b32 %3, %2\n\ts_nop 0\n\t" \
    "v_permlane16_swap_b32 %0, %1\n\tv_permlane16_swap_b32 %2, %3" \
    : "+v"(a1), "+v"(b1), "+v"(a2), "+v"(b2))
DI float max8(const float* v) {
  const float t1 = fmaxf(fmaxf(v[0], v[1]), v[2]), t2 = fmaxf(fmaxf(v[3], v[4]), v[5]), t3 = fmaxf(fmaxf(v[6], v[7]), t1);
  return fmaxf(t2, t3);
}
DI float min8(const float* v) {
  const float t1 = fminf(fminf(v[0], v[1]), v[2]), t2 = fminf(fminf(v[3], v[4]), v[5]), t3 = fminf(fminf(v[6], v[7]), t1);
  return fminf(t2, t3);
}
DI void dual_softmax2(const f32x4 (&p)[2][2], u32x4 (&pos)[2], u32x4 (&neg)[2]) {
  float v[2][8];
#pragma unroll
  for (int t = 0; t < 2; ++t)
#pragma unroll
    for (int h = 0; h < 2; ++h)
#pragma unroll
      for (int r = 0; r < 2; ++r) {
        unsigned u = pk2(p[t][h][2 * r], p[t][h][2 * r + 1]);
        asm("" : "+v"(u));
        v[t][4 * h + 2 * r] = blo(u); v[t][4 * h + 2 * r + 1] = bhi(u);
      }
  float mx0 = max8(v[0]), nm0 = -min8(v[0]), mx1 = max8(v[1]), nm1 = -min8(v[1]);
  XRED4("v_max_f32", mx0, nm0, mx1, nm1);
  constexpr float L2E = 1.44269504f;
  const f32x2 kp = {L2E, L2E}, kn = {-L2E, -L2E};
  f32x2 ep[2][4], en[2][4];
  float sp[2], sn[2];
#pragma unroll
  for (int t = 0; t < 2; ++t) {
    const float cpv = -(t ? mx1 : mx0) * L2E, cnv = -(t ? nm1 : nm0) * L2E;
    const f32x2 cp = {cpv, cpv}, cn = {cnv, cnv};
#pragma unroll
    for (int r = 0; r < 4; ++r) {
      const f32x2 x = {v[t][2 * r], v[t][2 * r + 1]};
      const f32x2 ap = __builtin_elementwise_fma(x, kp, cp), an = __builtin_elementwise_fma(x, kn, cn);
      ep[t][r] = (f32x2){__builtin_amdgcn_exp2f(ap[0]), __builtin_amdgcn_exp2f(ap[1])};
      en[t][r] = (f32x2){__builtin_amdgcn_exp2f(an[0]), __builtin_amdgcn_exp2f(an[1])};
    }
    const f32x2 s2p = (ep[t][0] + ep[t][1]) + (ep[t][2] + ep[t][3]), s2n = (en[t][0] + en[t][1]) + (en[t][2] + en[t][3]);
    sp[t] = s2p[0] + s2p[1]; sn[t] = s2n[0] + s2n[1];
  }
  XRED4("v_add_f32", sp[0], sn[0], sp[1], sn[1]);
#pragma unroll
  for (int t = 0; t < 2; ++t) {
    const float rp = __builtin_amdgcn_rcpf(sp[t]), rn = __builtin_amdgcn_rcpf(sn[t]);
    const f32x2 rp2 = {rp, rp}, rn2 = {rn, rn};
#pragma unroll
    for (int r = 0; r < 4; ++r) {
      const f32x2 a = ep[t][r] * rp2, b = en[t][r] * rn2;
      pos[t][r] = pk2(a[0], a[1]); neg[t][r] = pk2(b[0], b[1]);
    }
  }
}

__global__ __launch_bounds__(512) void k_ln(const u16* __restrict__ x, u16* __restrict__ xn, char* __restrict__ kvx) {
  __shared__ f32x2 st[8][8];
  {
    const unsigned e = blockIdx.x * 512 + threadIdx.x;
    if (e < KVX_BYTES / 16) *(u32x4*)(kvx + (size_t)e * 16) = (u32x4){~0u, ~0u, ~0u, ~0u};
  }
  const int lane = threadIdx.x & 63, wid = threadIdx.x >> 6;
  const int r8 = lane >> 3, c8 = lane & 7;
  const int row = blockIdx.x * 8 + r8;
  const u16* src = x + (size_t)row * D_ + wid * 128 + c8 * 8;
  const u32x4 v0 = *(const u32x4*)src, v1 = *(const u32x4*)(src + 64);
  float f[16];
#pragma unroll
  for (int i = 0; i < 4; ++i) { f[2 * i] = blo(v0[i]); f[2 * i + 1] = bhi(v0[i]); f[8 + 2 * i] = blo(v1[i]); f[8 + 2 * i + 1] = bhi(v1[i]); }
  float s = 0.f;
#pragma unroll
  for (int i = 0; i < 16; ++i) s += f[i];
  s += __shfl_xor(s, 1); s += __shfl_xor(s, 2); s += __shfl_xor(s, 4);
  const float mw = s * (1.0f / 128.f);
  float q = 0.f;
#pragma unroll
  for (int i = 0; i < 16; ++i) { const float d = f[i] - mw; q += d * d; }
  q += __shfl_xor(q, 1); q += __shfl_xor(q, 2); q += __shfl_xor(q, 4);
  if (c8 == 0) st[wid][r8] = (f32x2){mw, q};
  __syncthreads();
  float mean = 0.f, m2 = 0.f;
  f32x2 p[8];
#pragma unroll
  for (int w = 0; w < 8; ++w) { p[w] = st[w][r8]; mean += p[w][0]; m2 += p[w][1]; }
  mean *= 0.125f;
#pragma unroll
  for (int w = 0; w < 8; ++w) { const float d = p[w][0] - mean; m2 += 128.f * d * d; }
  const float mb = rbf(mean), vb = rbf(m2 * (1.0f / D_));
  const float sd = rbf(sqrtf(rbf(vb + 1e-5f)));
  u32x4 o0, o1;
#pragma unroll
  for (int i = 0; i < 4; ++i) {
    o0[i] = pk2(rbf(f[2 * i] - mb) / sd, rbf(f[2 * i + 1] - mb) / sd);
    o1[i] = pk2(rbf(f[8 + 2 * i] - mb) / sd, rbf(f[8 + 2 * i + 1] - mb) / sd);
  }
  const int rt = row >> 4, i16 = row & 15;
  const int kgA = wid * 4 + (c8 >> 2), g = c8 & 3;
  u16* dst = xn + ((size_t)(rt * NKG1 + kgA) * 64 + g * 16 + i16) * 8;
  *(u32x4*)dst = o0;
  *(u32x4*)(dst + 2 * 512) = o1;
}

constexpr int G1_SLOT = 32768;
constexpr int G1_LDS = 4 * G1_SLOT + 4096 + 2048;

DI float gelu_tanh(float x) {
  const float t = x * x;
  const float a = x * __builtin_fmaf(t, -0.10294324f, -2.3022082f);
  const float e = __builtin_amdgcn_exp2f(a);
  return x * __builtin_amdgcn_rcpf(1.0f + e);
}

template <bool QKV>
DI void gemm1_body(const u16* __restrict__ xn, const u16* __restrict__ W, int wcol0, int woff, int panel, int tcol,
                   const u16* __restrict__ b1, const u16* __restrict__ wphi,
                   u16* __restrict__ act, char* __restrict__ kvx, char* __restrict__ xch,
                   const u16* __restrict__ w2, const u16* __restrict__ wo, LAS char* lds) {
  constexpr int LDW = QKV ? 3 * D_ : F_;
  constexpr int NG = QKV ? 3 : 4;
  constexpr int NT = QKV ? 6 : 7;
  const int tid = threadIdx.x, lane = tid & 63;
  const int wid = __builtin_amdgcn_readfirstlane(tid >> 6);
  const int wr = wid >> 1, wc = wid & 1;
  const int g = lane >> 4, i16 = lane & 15, q4 = i16 >> 2, p4 = lane & 3;
  const unsigned ldsb = (unsigned)(size_t)lds;

  STAMP(0, 0);
  const char* abase = (const char*)xn + (size_t)panel * 16 * NKG1 * 1024;
  const unsigned avoff = (unsigned)(wid * 2) * NKG1 * 1024 + lane * 16;
  const char* bbase = (const char*)W;
  unsigned bvoff[2]; unsigned bdst[2];
#pragma unroll
  for (int j = 0; j < 2; ++j) {
    int pb = QKV ? (wid + 8 * j) : (wid * 2 + j);
    if (QKV && pb >= 12) pb -= 8;
    const int kg = pb / NG, cg = pb % NG;
    const int r = lane >> 3, sw = ((r >> 1) & 1) | ((kg & 1) << 1);
    const int cc = (lane & 7) ^ (sw << 1);
    const int col = QKV ? (cg * D_ + wcol0) : (wcol0 + cg * 64);
    bvoff[j] = (unsigned)(((kg * 8 + r) * LDW + col + cc * 8) * 2);
    bdst[j] = ldsb + 16384 + pb * 1024;
  }
  const unsigned adst = ldsb + wid * 2048;

  LAS char* ard[2];
  ard[0] = opaque(lds + wr * 4096 + lane * 16); ard[1] = opaque(ard[0] + 65536);
  constexpr int NB = QKV ? 2 : 4;
  LAS char* brd[2][NB];
#pragma unroll
  for (int j = 0; j < NB; ++j) {
    const int n0 = QKV ? (wc * 32 + j * 16) : (woff + wc * 112 + j * 16);
    const int sw = (q4 >> 1) | ((g & 1) << 1);
    brd[0][j] = opaque(lds + 16384 + (g * NG + (n0 >> 6)) * 1024 + q4 * 128 + (((((n0 & 63) >> 3) ^ (sw << 1)) + (p4 >> 1)) << 4) + (p4 & 1) * 8);
    brd[1][j] = opaque(brd[0][j] + 65536);
  }

  if (QKV) {
    if (tid < 256) {
      const u32x4 w = *(const u32x4*)(wphi + (size_t)tcol * 2 * 1024 + tid * 8);
      *(LAS u32x4*)(lds + 4 * G1_SLOT + tid * 16) = w;
    }
  }

  f32x4 acc[4][NT];
#pragma unroll
  for (int m = 0; m < 4; ++m)
#pragma unroll
    for (int n = 0; n < NT; ++n) acc[m][n] = (f32x4){0.f, 0.f, 0.f, 0.f};

  bf16x8 fa[2][4], fb[2][NT];

  auto issue = [&](int q, int stage, int slot) {
    if (q < 2) glds16(avoff + q * NKG1 * 1024, abase + (size_t)stage * 1024, adst + q * 1024 + slot * G1_SLOT);
    else       glds16(bvoff[q - 2], bbase + (size_t)stage * 64 * LDW, bdst[q - 2] + slot * G1_SLOT);
  };
  auto rdA = [&](auto P, auto SL, int m) { fa[P.v][m] = lds_b128(ard[SL.v >> 1] + (SL.v & 1) * G1_SLOT + m * 1024); };
  auto rdB = [&](auto P, auto SL, int n) { LAS char* p = brd[SL.v >> 1][n % NB] + (n / NB) * 1024 + (SL.v & 1) * G1_SLOT; fb[P.v][n] = cat8(lds_tr(p), lds_tr(p + 512)); };
  auto mma4 = [&](auto P, int n) {
#pragma unroll
    for (int m = 0; m < 4; ++m) {
      if (QKV && n >= 4) acc[m][n] = MFMA16(fa[P.v][m], fb[P.v][n], acc[m][n]);
      else               acc[m][n] = MFMA16(fb[P.v][n], fa[P.v][m], acc[m][n]);
    }
  };
  auto step = [&](auto SC, auto VM, auto GL, auto RD, int s) {
    constexpr int P = SC.v & 1, SNv = (SC.v + 1) & 3;
    const IC<P> Pc; const IC<P ^ 1> Pn; const IC<SNv> SN;
    if constexpr (RD.v) { WAIT_V(VM.v); WAIT_L(0); BAR(); }
    SCHED();
    if constexpr (GL.v) issue(0, s + 4, SC.v);
    if constexpr (RD.v) { rdA(Pn, SN, 0); rdA(Pn, SN, 1); rdB(Pn, SN, 0); }
    mma4(Pc, 0); SCHED();
    if constexpr (GL.v) issue(1, s + 4, SC.v);
    if constexpr (RD.v) { rdA(Pn, SN, 2); rdA(Pn, SN, 3); rdB(Pn, SN, 1); }
    mma4(Pc, 1); SCHED();
    if constexpr (GL.v) issue(2, s + 4, SC.v);
    if constexpr (RD.v) { rdB(Pn, SN, 2); rdB(Pn, SN, 3); }
    mma4(Pc, 2); SCHED();
    if constexpr (GL.v) issue(3, s + 4, SC.v);
    if constexpr (RD.v) { rdB(Pn, SN, 4); rdB(Pn, SN, 5); }
    mma4(Pc, 3); SCHED();
    if constexpr (RD.v && NT == 7) rdB(Pn, SN, 6);
    mma4(Pc, 4); SCHED();
    mma4(Pc, 5);
    if constexpr (NT == 7) mma4(Pc, 6);
    SCHED();
  };

#pragma unroll
  for (int st = 0; st < 4; ++st)
#pragma unroll
    for (int q = 0; q < 4; ++q) issue(q, st, st);
  WAIT_V(12); WAIT_L(0); BAR();
  STAMP(0, 1);
  {
    const IC<0> P0; const IC<0> S0;
#pragma unroll
    for (int m = 0; m < 4; ++m) rdA(P0, S0, m);
#pragma unroll
    for (int n = 0; n < NT; ++n) rdB(P0, S0, n);
  }
#pragma unroll 1
  for (int s = 0; s < 28; s += 4) {
    step(IC<0>{}, IC<8>{}, IC<1>{}, IC<1>{}, s);
    step(IC<1>{}, IC<8>{}, IC<1>{}, IC<1>{}, s + 1);
    step(IC<2>{}, IC<8>{}, IC<1>{}, IC<1>{}, s + 2);
    step(IC<3>{}, IC<8>{}, IC<1>{}, IC<1>{}, s + 3);
  }
  step(IC<0>{}, IC<8>{}, IC<0>{}, IC<1>{}, 28);
  {
    int wv = blockIdx.x * 8 + wid;
    if (wv >= 1152) wv -= 1152;
    const char* src = uniform_ptr(wv < 896 ? (const char*)w2 + (size_t)wv * 8192 : (const char*)wo + (size_t)(wv - 896) * 8192);
    asm volatile("s_mov_b32 m0, %2\n\ts_nop 0\n\tglobal_load_lds_dword %0, %1" ::"v"(lane * 128), "s"(src), "s"(ldsb + 4 * G1_SLOT + 4096 + wid * 256) : "memory");
    char* xp = xch + (size_t)blockIdx.x * 16384 + tid * 16;
    const u32x4 ones = {~0u, ~0u, ~0u, ~0u};
    asm volatile("global_store_dwordx4 %0, %2, off\n\tglobal_store_dwordx4 %1, %2, off\n\ts_nop 1" ::"v"(xp), "v"(xp + 8192), "v"(ones) : "memory");
  }
  step(IC<1>{}, IC<4 + 3>{}, IC<0>{}, IC<1>{}, 29);
  step(IC<2>{}, IC<0 + 3>{}, IC<0>{}, IC<1>{}, 30);
  step(IC<3>{}, IC<0>{}, IC<0>{}, IC<0>{}, 31);
  STAMP(0, 2);

  if (!QKV) {
    const int cbase = wc * 112;
    const int ncol0 = tcol * 224 + cbase + 4 * g;
    unsigned hv[4][NT][2];
#pragma unroll
    for (int n = 0; n < NT; ++n) {
      const u32x2 bb = *(const u32x2*)(b1 + ncol0 + n * 16);
      const float b0 = blo(bb[0]), b1f = bhi(bb[0]), b2f = blo(bb[1]), b3 = bhi(bb[1]);
#pragma unroll
      for (int m = 0; m < 4; ++m) {
        hv[m][n][0] = pk2(gelu_tanh(acc[m][n][0] + b0), gelu_tanh(acc[m][n][1] + b1f));
        hv[m][n][1] = pk2(gelu_tanh(acc[m][n][2] + b2f), gelu_tanh(acc[m][n][3] + b3));
      }
    }
    const int rt0 = panel * 16 + wr * 4;
    char* obase = (char*)act + ((size_t)rt0 * NKG2 + tcol * 7) * 1024 + lane * 16;
    if (wc == 0) {
#pragma unroll
      for (int m = 0; m < 4; ++m) {
        char* rb = obase + (size_t)m * NKG2 * 1024;
#pragma unroll
        for (int p = 0; p < 3; ++p) *(u32x4*)(rb + p * 1024) = (u32x4){hv[m][2 * p][0], hv[m][2 * p][1], hv[m][2 * p + 1][0], hv[m][2 * p + 1][1]};
        *(u32x2*)(rb + 3 * 1024) = (u32x2){hv[m][6 % NT][0], hv[m][6 % NT][1]};
      }
    } else {
#pragma unroll
      for (int m = 0; m < 4; ++m) {
        char* rb = obase + (size_t)m * NKG2 * 1024;
        *(u32x2*)(rb + 3 * 1024 + 8) = (u32x2){hv[m][0][0], hv[m][0][1]};
#pragma unroll
        for (int p = 0; p < 3; ++p) *(u32x4*)(rb + (4 + p) * 1024) = (u32x4){hv[m][2 * p + 1][0], hv[m][2 * p + 1][1], hv[m][(2 * p + 2) % NT][0], hv[m][(2 * p + 2) % NT][1]};
      }
    }
  } else {
    const int head = tcol * 2 + wc;
    LAS char* wp = lds + 4 * G1_SLOT + wc * 2048;
    bf16x8 wf[2];
#pragma unroll
    for (int ft = 0; ft < 2; ++ft) {
      LAS char* p = wp + (4 * g + q4) * 64 + (16 * ft + 4 * p4) * 2;
      wf[ft] = cat8(lds_tr(p), lds_tr(p + 16 * 64));
    }
    const f32x4 z4 = {0.f, 0.f, 0.f, 0.f};
    LAS char* sc = lds + wid * 8704;
#pragma unroll
    for (int mp = 0; mp < 2; ++mp) {
      f32x4 pp[2][2]; u32x4 pos[2], neg[2];
#pragma unroll
      for (int t = 0; t < 2; ++t) {
        const bf16x8 bk = pack8(acc[2 * mp + t][2], acc[2 * mp + t][3]);
        pp[t][0] = MFMA16(wf[0], bk, z4); pp[t][1] = MFMA16(wf[1], bk, z4);
      }
      dual_softmax2(pp, pos, neg);
#pragma unroll
      for (int t = 0; t < 2; ++t) {
        LAS char* rowp = sc + (16 * (2 * mp + t) + i16) * 136 + 8 * g;
        *(LAS u32x2*)(rowp) = (u32x2){pos[t][0], pos[t][1]};      *(LAS u32x2*)(rowp + 32) = (u32x2){pos[t][2], pos[t][3]};
        *(LAS u32x2*)(rowp + 64) = (u32x2){neg[t][0], neg[t][1]}; *(LAS u32x2*)(rowp + 96) = (u32x2){neg[t][2], neg[t][3]};
      }
    }
    WAIT_L(0);
    STAMP(0, 5);
    f32x4 kv[4][3];
#pragma unroll
    for (int ft = 0; ft < 4; ++ft)
#pragma unroll
      for (int d = 0; d < 3; ++d) kv[ft][d] = z4;
    bf16x8 ones;
#pragma unroll
    for (int j = 0; j < 8; ++j) ones[j] = (__bf16)1.0f;
#pragma unroll
    for (int ks = 0; ks < 2; ++ks) {
      const bf16x8 bv0 = pack8(acc[2 * ks][4], acc[2 * ks + 1][4]);
      const bf16x8 bv1 = pack8(acc[2 * ks][5], acc[2 * ks + 1][5]);
#pragma unroll
      for (int ft = 0; ft < 4; ++ft) {
        LAS char* p = sc + (32 * ks + 4 * g + q4) * 136 + (16 * ft + 4 * p4) * 2;
        const bf16x8 a = cat8(lds_tr(p), lds_tr(p + 16 * 136));
        kv[ft][0] = MFMA16(a, bv0, kv[ft][0]);
        kv[ft][1] = MFMA16(a, bv1, kv[ft][1]);
        kv[ft][2] = MFMA16(a, ones, kv[ft][2]);
      }
    }
    STAMP(0, 6);
    WAIT_L(0); BAR();
    STAMP(0, 7);
    {
      LAS char* pp = lds + wid * 8448;
#pragma unroll
      for (int sg = 0; sg < 2; ++sg) {
#pragma unroll
        for (int d = 0; d < 2; ++d) {
          *(LAS f32x4*)(pp + (sg * 2 + d) * 2048 + lane * 16) = kv[2 * sg][d];
          *(LAS f32x4*)(pp + (sg * 2 + d) * 2048 + 1024 + lane * 16) = kv[2 * sg + 1][d];
        }
        if (i16 == 0) {
          *(LAS f32x4*)(pp + 8192 + sg * 128 + g * 16) = kv[2 * sg][2];
          *(LAS f32x4*)(pp + 8192 + sg * 128 + 64 + g * 16) = kv[2 * sg + 1][2];
        }
      }
    }
    WAIT_L(0); BAR();
    char* kvt = kvx + (size_t)tcol * KVX_TILE;
    char* kvmine = kvt + (size_t)panel * 16 * KVX_TILE;
    {
      LAS char* pp = lds + (tid >> 8) * 8448 + ((tid >> 6) & 3) * 2048 + lane * 16;
      f32x4 lo = *(LAS f32x4*)(pp), hi = *(LAS f32x4*)(pp + 1024);
#pragma unroll
      for (int w = 1; w < 4; ++w) { lo += *(LAS f32x4*)(pp + w * 16896); hi += *(LAS f32x4*)(pp + w * 16896 + 1024); }
      st_sc1(kvmine + tid * 16, (u32x4){pk2(lo[0], lo[1]), pk2(lo[2], lo[3]), pk2(hi[0], hi[1]), pk2(hi[2], hi[3])});
    }
    if (tid < 16) {
      LAS char* pp = lds + (tid >> 3) * 8448 + 8192 + ((tid >> 2) & 1) * 128 + (tid & 3) * 16;
      f32x4 lo = *(LAS f32x4*)(pp), hi = *(LAS f32x4*)(pp + 64);
#pragma unroll
      for (int w = 1; w < 4; ++w) { lo += *(LAS f32x4*)(pp + w * 16896); hi += *(LAS f32x4*)(pp + w * 16896 + 64); }
      st_sc1(kvmine + 8192 + tid * 16, (u32x4){pk2(lo[0], lo[1]), pk2(lo[2], lo[3]), pk2(hi[0], hi[1]), pk2(hi[2], hi[3])});
    }
    STAMP(0, 8);
    u32x4 qp[2][2], qn[2][2];
    auto qsm = [&](int mp) {
      f32x4 pp[2][2];
#pragma unroll
      for (int t = 0; t < 2; ++t) {
        const bf16x8 bq = pack8(acc[2 * mp + t][0], acc[2 * mp + t][1]);
        pp[t][0] = MFMA16(wf[0], bq, z4); pp[t][1] = MFMA16(wf[1], bq, z4);
      }
      dual_softmax2(pp, qp[mp], qn[mp]);
    };
    qsm(0);
    const char* kb0 = uniform_ptr(kvt), * kb1 = uniform_ptr(kvt + (size_t)32 * KVX_TILE);
    const char* kb2 = uniform_ptr(kvt + (size_t)64 * KVX_TILE), * kb3 = uniform_ptr(kvt + (size_t)96 * KVX_TILE);
    u32x4 pv[8], pz[8];
#define KV_GATHER(pv, OFF, TAIL) asm volatile( \
          "global_load_dwordx4 %0, %8, %10 sc1\n\tglobal_load_dwordx4 %1, %9, %10 sc1\n\t" \
          "global_load_dwordx4 %2, %8, %11 sc1\n\tglobal_load_dwordx4 %3, %9, %11 sc1\n\t" \
          "global_load_dwordx4 %4, %8, %12 sc1\n\tglobal_load_dwordx4 %5, %9, %12 sc1\n\t" \
          "global_load_dwordx4 %6, %8, %13 sc1\n\tglobal_load_dwordx4 %7, %9, %13 sc1" TAIL \
          : "=&v"(pv[0]), "=&v"(pv[1]), "=&v"(pv[2]), "=&v"(pv[3]), "=&v"(pv[4]), "=&v"(pv[5]), "=&v"(pv[6]), "=&v"(pv[7]) \
          : "v"(OFF), "v"((OFF) + 16 * KVX_TILE), "s"(kb0), "s"(kb1), "s"(kb2), "s"(kb3) : "memory")
#define KV_WAIT(pv) asm volatile("s_waitcnt vmcnt(0)" \
          : "+v"(pv[0]), "+v"(pv[1]), "+v"(pv[2]), "+v"(pv[3]), "+v"(pv[4]), "+v"(pv[5]), "+v"(pv[6]), "+v"(pv[7]) :: "memory")
    LAS char* kvs = lds + 3 * G1_SLOT;
    auto finish = [&](u32x4 (&pv)[8], unsigned off) {
      for (unsigned spins = 0;; ++spins) {
        unsigned m = 0;
#pragma unroll
        for (int p = 0; p < 8; ++p) m = umax4(pv[p], m);
        if (__all(m != 0xffffffffu) || spins > (1u << 14)) break;
        __builtin_amdgcn_s_sleep(2);
        KV_GATHER(pv, off, "\n\ts_waitcnt vmcnt(0)");
      }
      f32x2 t[4];
#pragma unroll
      for (int r = 0; r < 4; ++r) t[r] = (f32x2){blo(pv[0][r]), bhi(pv[0][r])};
#pragma unroll
      for (int p = 1; p < 8; ++p)
#pragma unroll
        for (int r = 0; r < 4; ++r) t[r] += (f32x2){blo(pv[p][r]), bhi(pv[p][r])};
      *(LAS u32x4*)(kvs + off) = (u32x4){pk2(t[0][0], t[0][1]), pk2(t[1][0], t[1][1]), pk2(t[2][0], t[2][1]), pk2(t[3][0], t[3][1])};
    };
    const unsigned go = (unsigned)tid * 16, gz = 8192u + (unsigned)i16 * 16;
    STAMP(0, 9);
    SCHED();
    KV_GATHER(pv, go, "");
    KV_GATHER(pz, gz, "");
    SCHED();
    qsm(1);
    STAMP(0, 10);
    SCHED();
    KV_WAIT(pv);
    KV_WAIT(pz);
    SCHED();
    finish(pv, go);
    if (wid == 0) finish(pz, gz);
    STAMP(0, 11);
    WAIT_L(0); BAR();
    bf16x8 a[2][3];
#pragma unroll
    for (int sg = 0; sg < 2; ++sg) {
#pragma unroll
      for (int d = 0; d < 2; ++d) a[sg][d] = lds_b128(kvs + wc * 4096 + (sg * 2 + d) * 1024 + lane * 16);
      a[sg][2] = lds_b128(kvs + 8192 + wc * 128 + sg * 64 + g * 16);
    }
#pragma unroll
    for (int m = 0; m < 4; ++m) {
      const bf16x8 bp = __builtin_bit_cast(bf16x8, qp[m >> 1][m & 1]), bn = __builtin_bit_cast(bf16x8, qn[m >> 1][m & 1]);
      f32x4 o0 = MFMA16(a[0][0], bp, z4), o1 = MFMA16(a[0][1], bp, z4), dn = MFMA16(a[0][2], bp, z4);
      o0 = MFMA16(a[1][0], bn, o0); o1 = MFMA16(a[1][1], bn, o1); dn = MFMA16(a[1][2], bn, dn);
      const float rd = __builtin_amdgcn_rcpf(dn[0] + 1e-6f);
      const int rt = panel * 16 + wr * 4 + m;
      u32x4 w;
      w[0] = pk2(o0[0] * rd, o0[1] * rd); w[1] = pk2(o0[2] * rd, o0[3] * rd);
      w[2] = pk2(o1[0] * rd, o1[1] * rd); w[3] = pk2(o1[2] * rd, o1[3] * rd);
      *(u32x4*)((char*)act + ((size_t)rt * NKG2 + 112 + head) * 1024 + lane * 16) = w;
    }
  }
  STAMP(0, 3);
  STAMP(0, 4);
}

__global__ __launch_bounds__(512, 2) void k_gemm1(const u16* __restrict__ xn, const u16* __restrict__ wqkv, const u16* __restrict__ w1,
                                                  const u16* __restrict__ b1, const u16* __restrict__ wphi,
                                                  u16* __restrict__ act, char* __restrict__ kvx, char* __restrict__ xch,
                                                  const u16* __restrict__ w2, const u16* __restrict__ wo) {
  extern __shared__ __attribute__((aligned(16))) char smem[];
  LAS char* lds = (LAS char*)smem;
  const int b = blockIdx.x, xcd = b & 7, idx = b >> 3;
  const int panel = idx & 7, ct = xcd * 4 + (idx >> 3);
  const int kind = ct & 1, tcol = ct >> 1;
  if (kind == 0) {
    gemm1_body<true>(xn, wqkv, tcol * 64, 0, panel, tcol, b1, wphi, act, kvx, xch, w2, wo, lds);
  } else {
    const int c0 = tcol * 224, woff = c0 & 63;
    gemm1_body<false>(xn, w1, c0 - woff, woff, panel, tcol, b1, wphi, act, kvx, xch, w2, wo, lds);
  }
}

constexpr int G2_SLOT = 32768;
constexpr int G2_LDS = G2_SLOT * 5;

__global__ __launch_bounds__(512, 2) void k_gemm2(const u16* __restrict__ act, const u16* __restrict__ w2, const u16* __restrict__ wo,
                                                  const u16* __restrict__ x, const u16* __restrict__ b2,
                                                  char* __restrict__ xch, u16* __restrict__ out) {
  extern __shared__ __attribute__((aligned(16))) char smem[];
  LAS char* lds = (LAS char*)smem;
  const unsigned ldsb = (unsigned)(size_t)lds;
  const int tid = threadIdx.x, lane = tid & 63;
  const int wid = __builtin_amdgcn_readfirstlane(tid >> 6);
  const int kh = wid >> 2, wr = (wid >> 1) & 1, wc = wid & 1;
  const int g = lane >> 4, i16 = lane & 15, q4 = i16 >> 2, p4 = lane & 3;
  const int b = blockIdx.x, grp = b & 7, idx = b >> 3;
  const int split = grp & 1, pm = (grp >> 1) * 4 + (idx & 3), pn = idx >> 2;
  constexpr int NKT = 36;
  STAMP(1, 0);
  const int kt0 = split * NKT;
  const char* abase = (const char*)act + ((size_t)(pm * 8) * NKG2 + kt0 * 2) * 1024;
  const unsigned avoff = (unsigned)wid * NKG2 * 1024 + lane * 16;
  const unsigned adst = ldsb + wid * 1024;
  const int r8 = lane >> 3;
  const unsigned bvoff = (unsigned)((wid * 8 + r8) * D_ + pn * 128 + (((lane & 7) ^ (((r8 >> 1) & 3) << 1)) << 3)) * 2;
  const unsigned bdst = ldsb + 16384 + wid * 2048;
  const char* wb = uniform_ptr((const char*)w2 + (size_t)kt0 * 64 * D_ * 2);
  auto issue = [&](int q, int t, int slot) {
    if (q < 2) glds16(avoff + q * 1024, abase + (size_t)t * 2048, adst + q * 8192 + slot * G2_SLOT);
    else       glds16(bvoff + (q - 2) * 128, wb + (size_t)t * 64 * D_ * 2, bdst + (q - 2) * 1024 + slot * G2_SLOT);
  };
#pragma unroll
  for (int st = 0; st < 5; ++st)
#pragma unroll
    for (int q = 0; q < 4; ++q) issue(q, st, st);

  LAS char* ard[3];
  ard[0] = opaque(lds + kh * 8192 + wr * 4096 + lane * 16); ard[1] = opaque(ard[0] + 65536); ard[2] = opaque(ard[0] + 131072);
  LAS char* brd[3][4];
#pragma unroll
  for (int j = 0; j < 4; ++j) {
    const int r = 4 * (g & 1) + q4, sw = (r >> 1) & 3;
    brd[0][j] = opaque(lds + 16384 + ((kh * 4 + (g >> 1)) * 2 + wc) * 1024 + r * 128 + ((((2 * j) ^ (sw << 1)) + (p4 >> 1)) << 4) + (p4 & 1) * 8);
    brd[1][j] = opaque(brd[0][j] + 65536); brd[2][j] = opaque(brd[0][j] + 131072);
  }
  f32x4 acc[4][4];
#pragma unroll
  for (int m = 0; m < 4; ++m)
#pragma unroll
    for (int n = 0; n < 4; ++n) acc[m][n] = (f32x4){0.f, 0.f, 0.f, 0.f};
  bf16x8 fa[2][4], fb[2][4];
  auto rdA = [&](auto P, auto SL, int m) { fa[P.v][m] = lds_b128(ard[SL.v >> 1] + (SL.v & 1) * G2_SLOT + m * 1024); };
  auto rdB = [&](auto P, auto SL, int n) { LAS char* p = brd[SL.v >> 1][n] + (SL.v & 1) * G2_SLOT; fb[P.v][n] = cat8(lds_tr(p), lds_tr(p + 4096)); };
  auto mma4 = [&](auto P, int n) {
#pragma unroll
    for (int m = 0; m < 4; ++m) acc[m][n] = MFMA16(fb[P.v][n], fa[P.v][m], acc[m][n]);
  };
  auto step = [&](auto J, auto VM, auto GL, auto RD, int t) {
    constexpr int P = J.v & 1, SCv = J.v % 5, SNv = (J.v + 1) % 5;
    const IC<P> Pc; const IC<P ^ 1> Pn; const IC<SNv> SN;
    if constexpr (RD.v) { WAIT_V(VM.v); WAIT_L(0); BAR(); }
    SCHED();
    if constexpr (GL.v) { issue(0, t + 5, SCv); issue(1, t + 5, SCv); }
    if constexpr (RD.v) { rdA(Pn, SN, 0); rdA(Pn, SN, 1); rdB(Pn, SN, 0); }
    mma4(Pc, 0); SCHED();
    if constexpr (GL.v) { issue(2, t + 5, SCv); issue(3, t + 5, SCv); }
    if constexpr (RD.v) { rdA(Pn, SN, 2); rdA(Pn, SN, 3); rdB(Pn, SN, 1); }
    mma4(Pc, 1); SCHED();
    if constexpr (RD.v) { rdB(Pn, SN, 2); rdB(Pn, SN, 3); }
    mma4(Pc, 2); SCHED();
    mma4(Pc, 3); SCHED();
  };
  WAIT_V(16); BAR();
  STAMP(1, 1);
  {
    const IC<0> P0; const IC<0> S0;
#pragma unroll
    for (int m = 0; m < 4; ++m) rdA(P0, S0, m);
#pragma unroll
    for (int n = 0; n < 4; ++n) rdB(P0, S0, n);
  }
  const IC<1> Y; const IC<0> N_; const IC<12> V12;
#pragma unroll 1
  for (int t = 0; t < 30; t += 10) {
    step(IC<0>{}, V12, Y, Y, t);     step(IC<1>{}, V12, Y, Y, t + 1); step(IC<2>{}, V12, Y, Y, t + 2);
    step(IC<3>{}, V12, Y, Y, t + 3); step(IC<4>{}, V12, Y, Y, t + 4);
    if (t == 10 && split == 1) wb = uniform_ptr((const char*)wo - (size_t)20 * 64 * D_ * 2);
    step(IC<5>{}, V12, Y, Y, t + 5); step(IC<6>{}, V12, Y, Y, t + 6); step(IC<7>{}, V12, Y, Y, t + 7);
    step(IC<8>{}, V12, Y, Y, t + 8); step(IC<9>{}, V12, Y, Y, t + 9);
  }
  step(IC<0>{}, V12, Y, Y, 30);
  step(IC<1>{}, V12, N_, Y, 31);
  step(IC<2>{}, IC<8>{}, N_, Y, 32);
  step(IC<3>{}, IC<4>{}, N_, Y, 33);
  step(IC<4>{}, IC<0>{}, N_, Y, 34);
  step(IC<5>{}, IC<0>{}, N_, N_, 35);
  STAMP(1, 2);
  const int quad = wid & 3;
  {
    LAS char* pp = lds + G2_SLOT + (quad * 2 + kh) * 8192 + lane * 16;
#pragma unroll
    for (int mm = 0; mm < 2; ++mm)
#pragma unroll
      for (int n = 0; n < 4; ++n) *(LAS f32x4*)(pp + (mm * 4 + n) * 1024) = kh ? acc[mm][n] : acc[2 + mm][n];
  }
  const bool keep = (wr == split);
  const int row0 = pm * 128 + wr * 64 + kh * 32 + i16, col0 = pn * 128 + wc * 64 + 4 * g;
  u32x2 xv[2][4], bv[4];
  if (keep) {
#pragma unroll
    for (int n = 0; n < 4; ++n) bv[n] = *(const u32x2*)(b2 + col0 + n * 16);
#pragma unroll
    for (int mm = 0; mm < 2; ++mm)
#pragma unroll
      for (int n = 0; n < 4; ++n) xv[mm][n] = *(const u32x2*)(x + (size_t)(row0 + mm * 16) * D_ + col0 + n * 16);
  }
  WAIT_L(0); BAR();
  f32x4 sum[2][4];
  {
    LAS char* pp = lds + G2_SLOT + (quad * 2 + (kh ^ 1)) * 8192 + lane * 16;
#pragma unroll
    for (int mm = 0; mm < 2; ++mm)
#pragma unroll
      for (int n = 0; n < 4; ++n) sum[mm][n] = (kh ? acc[2 + mm][n] : acc[mm][n]) + *(LAS f32x4*)(pp + (mm * 4 + n) * 1024);
  }
  const int piece = kh * 2 + wc;
  if (!keep) {
    char* dst = xch + (size_t)b * 16384 + piece * 4096 + lane * 16;
#pragma unroll
    for (int mm = 0; mm < 2; ++mm)
#pragma unroll
      for (int h = 0; h < 2; ++h)
        st_sc1(dst + (mm * 2 + h) * 1024, (u32x4){pk2(sum[mm][2 * h][0], sum[mm][2 * h][1]), pk2(sum[mm][2 * h][2], sum[mm][2 * h][3]),
                                                  pk2(sum[mm][2 * h + 1][0], sum[mm][2 * h + 1][1]), pk2(sum[mm][2 * h + 1][2], sum[mm][2 * h + 1][3])});
  } else {
    const char* src = xch + (size_t)(b ^ 1) * 16384 + piece * 4096 + lane * 16;
    u32x4 pv[4];
    for (unsigned spins = 0;; ++spins) {
      asm volatile("global_load_dwordx4 %0, %1, off offset:3072 sc1\n\ts_waitcnt vmcnt(0)" : "=&v"(pv[3]) : "v"(src) : "memory");
      if (__all(fresh4(pv[3])) || spins > (1u << 14)) break;
      __builtin_amdgcn_s_sleep(1);
    }
    for (unsigned spins = 0;; ++spins) {
      asm volatile(
          "global_load_dwordx4 %0, %3, off sc1\n\tglobal_load_dwordx4 %1, %3, off offset:1024 sc1\n\t"
          "global_load_dwordx4 %2, %3, off offset:2048 sc1\n\ts_waitcnt vmcnt(0)"
          : "=&v"(pv[0]), "=&v"(pv[1]), "=&v"(pv[2]) : "v"(src) : "memory");
      if (__all(fresh4(pv[0]) && fresh4(pv[1]) && fresh4(pv[2])) || spins > (1u << 14)) break;
      __builtin_amdgcn_s_sleep(1);
    }
#pragma unroll
    for (int mm = 0; mm < 2; ++mm)
#pragma unroll
      for (int n = 0; n < 4; ++n) {
        const unsigned q0 = pv[mm * 2 + (n >> 1)][2 * (n & 1)], q1 = pv[mm * 2 + (n >> 1)][2 * (n & 1) + 1];
        const f32x4 v = sum[mm][n];
        u32x2 o;
        o[0] = pk2(v[0] + blo(q0) + blo(xv[mm][n][0]) + blo(bv[n][0]), v[1] + bhi(q0) + bhi(xv[mm][n][0]) + bhi(bv[n][0]));
        o[1] = pk2(v[2] + blo(q1) + blo(xv[mm][n][1]) + blo(bv[n][1]), v[3] + bhi(q1) + bhi(xv[mm][n][1]) + bhi(bv[n][1]));
        *(u32x2*)(out + (size_t)(row0 + mm * 16) * D_ + col0 + n * 16) = o;
      }
  }
  STAMP(1, 3);
  STAMP(1, 4);
}

extern "C" void kernel_launch(void* const* d_in, const int* in_sizes, int n_in, void* d_out, int out_size, void* d_ws, size_t ws_size,
                              hipStream_t stream) {
  const u16* x = (const u16*)d_in[0];
  const u16* wqkv = (const u16*)d_in[1];
  const u16* wphi = (const u16*)d_in[2];
  const u16* wo = (const u16*)d_in[3];
  const u16* w1 = (const u16*)d_in[5];
  const u16* b1 = (const u16*)d_in[6];
  const u16* w2 = (const u16*)d_in[7];
  const u16* b2 = (const u16*)d_in[8];
  char* ws = (char*)d_ws;
  u16* xn = (u16*)(ws + WS_XN);
  u16* act = (u16*)(ws + WS_ACT);
  char* kvx = ws + WS_KVX;
  char* xch = ws + WS_XCH;
  (void)hipFuncSetAttribute((const void*)k_gemm1, hipFuncAttributeMaxDynamicSharedMemorySize, G1_LDS);
  (void)hipFuncSetAttribute((const void*)k_gemm2, hipFuncAttributeMaxDynamicSharedMemorySize, G2_LDS);
  k_ln<<<S_ / 8, 512, 0, stream>>>(x, xn, kvx);
  k_gemm1<<<256, 512, G1_LDS, stream>>>(xn, wqkv, w1, b1, wphi, act, kvx, xch, w2, wo);
  k_gemm2<<<256, 512, G2_LDS, stream>>>(act, w2, wo, x, b2, xch, (u16*)d_out);
}
```

```cpp
#include <hip/hip_runtime.h>
#include <hip/hip_bf16.h>
#include <stdint.h>

typedef __bf16 bf16x8 __attribute__((ext_vector_type(8)));
typedef __bf16 bf16x4 __attribute__((ext_vector_type(4)));
typedef __bf16 bf16x2 __attribute__((ext_vector_type(2)));
typedef float f32x4 __attribute__((ext_vector_type(4)));
typedef float f32x2 __attribute__((ext_vector_type(2)));
typedef unsigned u32x4 __attribute__((ext_vector_type(4)));
typedef unsigned u32x2 __attribute__((ext_vector_type(2)));
typedef unsigned short u16;
#define LAS __attribute__((address_space(3)))
#define DI __device__ __forceinline__

constexpr int S_ = 2048, D_ = 1024, F_ = 3584, H_ = 32;
constexpr int KCAT = F_ + D_;
constexpr int NKG1 = D_ / 32;
constexpr int NKG2 = KCAT / 32;

constexpr size_t WS_XN   = 0;
constexpr size_t WS_ACT  = WS_XN + (size_t)S_ * D_ * 2;
constexpr size_t WS_KVX  = WS_ACT + (size_t)S_ * KCAT * 2;
constexpr int KVX_TILE = 8192 + 256;
constexpr size_t KVX_BYTES = (size_t)8 * 16 * KVX_TILE;
constexpr size_t WS_XCH  = WS_KVX + KVX_BYTES;
constexpr size_t WS_CNT  = WS_XCH + (size_t)256 * 16384;

DI float rbf(float x) { return (float)(__bf16)x; }
DI unsigned pk2(float lo, float hi) { bf16x2 v; v[0] = (__bf16)lo; v[1] = (__bf16)hi; return __builtin_bit_cast(unsigned, v); }
DI float blo(unsigned u) { return __uint_as_float(u << 16); }
DI float bhi(unsigned u) { return __uint_as_float(u & 0xffff0000u); }

#define MFMA16(a, b, c) __builtin_amdgcn_mfma_f32_16x16x32_bf16((a), (b), (c), 0, 0, 0)
#define WAIT_V(n) asm volatile("s_waitcnt vmcnt(%0)" ::"n"(n) : "memory")
#define WAIT_L(n) do { static_assert((n) == 0, ""); asm volatile("" ::: "memory"); __builtin_amdgcn_s_waitcnt(0xC07F); asm volatile("" ::: "memory"); } while (0)
#define BAR() do { asm volatile("" ::: "memory"); __builtin_amdgcn_s_barrier(); asm volatile("" ::: "memory"); } while (0)
#define SCHED() __builtin_amdgcn_sched_barrier(0)
#define STAMP(K, I) do { } while (0)
template <int N> struct IC { static constexpr int v = N; };

DI void glds16_sc1(unsigned voff, const void* sbase, unsigned ldsaddr) {
  asm volatile("s_mov_b32 m0, %2\n\ts_nop 0\n\tglobal_load_lds_dwordx4 %0, %1 sc1" ::"v"(voff), "s"(sbase), "s"(ldsaddr) : "memory");
}
DI void glds16(unsigned voff, const void* sbase, unsigned ldsaddr) {
  asm volatile("s_mov_b32 m0, %2\n\ts_nop 0\n\tglobal_load_lds_dwordx4 %0, %1" ::"v"(voff), "s"(sbase), "s"(ldsaddr) : "memory");
}
DI const char* uniform_ptr(const char* p) {
  const unsigned long long a = (unsigned long long)p;
  unsigned lo = __builtin_amdgcn_readfirstlane((unsigned)a), hi = __builtin_amdgcn_readfirstlane((unsigned)(a >> 32));
  asm volatile("s_nop 4" : "+s"(lo), "+s"(hi));
  return (const char*)(((unsigned long long)hi << 32) | lo);
}
DI LAS char* opaque(LAS char* p) { asm volatile("" : "+v"(p)); return p; }
DI bf16x8 lds_b128(LAS char* p) { return *(LAS bf16x8*)p; }
DI bf16x4 lds_tr(LAS char* p) { return __builtin_amdgcn_ds_read_tr16_b64_v4bf16((LAS bf16x4*)p); }
DI bf16x8 cat8(bf16x4 a, bf16x4 b) { return __builtin_shufflevector(a, b, 0, 1, 2, 3, 4, 5, 6, 7); }
DI bf16x8 pack8(f32x4 a, f32x4 b) {
  bf16x8 v;
  v[0] = (__bf16)a[0]; v[1] = (__bf16)a[1]; v[2] = (__bf16)a[2]; v[3] = (__bf16)a[3];
  v[4] = (__bf16)b[0]; v[5] = (__bf16)b[1]; v[6] = (__bf16)b[2]; v[7] = (__bf16)b[3];
  return v;
}

typedef __attribute__((address_space(1))) unsigned gu32;
DI void st_sc1(void* p, u32x4 v) { asm volatile("global_store_dwordx4 %0, %1, off sc1\n\ts_nop 1" ::"v"(p), "v"(v) : "memory"); }
DI unsigned umax4(u32x4 v, unsigned m) { return max(max(max(v[0], v[1]), v[2]), max(v[3], m)); }
DI void st8_sc1(void* p, u32x2 v) { asm volatile("global_store_dwordx2 %0, %1, off sc1\n\ts_nop 1" ::"v"(p), "v"(v) : "memory"); }
DI bool fresh4(u32x4 v) { return umax4(v, 0u) != 0xffffffffu; }
#define XRED4(OP, a1, b1, a2, b2) asm volatile( \
    "s_nop 1\n\tv_permlane16_swap_b32 %0, %1\n\tv_permlane16_swap_b32 %2, %3\n\t" \
    OP " %0, %0, %1\n\t" OP " %2, %2, %3\n\tv_mov_b32 %1, %0\n\tv_mov_b32 %3, %2\n\ts_nop 0\n\t" \
    "v_permlane32_swap_b32 %0, %1\n\tv_permlane32_swap_b32 %2, %3\n\t" \
    OP " %0, %0, %1\n\t" OP " %2, %2, %3\n\tv_mov_b32 %1, %0\n\tv_mov_b32 %3, %2\n\ts_nop 0\n\t" \
    "v_permlane16_swap_b32 %0, %1\n\tv_permlane16_swap_b32 %2, %3" \
    : "+v"(a1), "+v"(b1), "+v"(a2), "+v"(b2))
DI float max8(const float* v) {
  const float t1 = fmaxf(fmaxf(v[0], v[1]), v[2]), t2 = fmaxf(fmaxf(v[3], v[4]), v[5]), t3 = fmaxf(fmaxf(v[6], v[7]), t1);
  return fmaxf(t2, t3);
}
DI float min8(const float* v) {
  const float t1 = fminf(fminf(v[0], v[1]), v[2]), t2 = fminf(fminf(v[3], v[4]), v[5]), t3 = fminf(fminf(v[6], v[7]), t1);
  return fminf(t2, t3);
}
DI void dual_softmax2(const f32x4 (&p)[2][2], u32x4 (&pos)[2], u32x4 (&neg)[2]) {
  float v[2][8];
#pragma unroll
  for (int t = 0; t < 2; ++t)
#pragma unroll
    for (int h = 0; h < 2; ++h)
#pragma unroll
      for (int r = 0; r < 2; ++r) {
        unsigned u = pk2(p[t][h][2 * r], p[t][h][2 * r + 1]);
        asm("" : "+v"(u));
        v[t][4 * h + 2 * r] = blo(u); v[t][4 * h + 2 * r + 1] = bhi(u);
      }
  float mx0 = max8(v[0]), nm0 = -min8(v[0]), mx1 = max8(v[1]), nm1 = -min8(v[1]);
  XRED4("v_max_f32", mx0, nm0, mx1, nm1);
  constexpr float L2E = 1.44269504f;
  const f32x2 kp = {L2E, L2E}, kn = {-L2E, -L2E};
  f32x2 ep[2][4], en[2][4];
  float sp[2], sn[2];
#pragma unroll
  for (int t = 0; t < 2; ++t) {
    const float cpv = -(t ? mx1 : mx0) * L2E, cnv = -(t ? nm1 : nm0) * L2E;
    const f32x2 cp = {cpv, cpv}, cn = {cnv, cnv};
#pragma unroll
    for (int r = 0; r < 4; ++r) {
      const f32x2 x = {v[t][2 * r], v[t][2 * r + 1]};
      const f32x2 ap = __builtin_elementwise_fma(x, kp, cp), an = __builtin_elementwise_fma(x, kn, cn);
      ep[t][r] = (f32x2){__builtin_amdgcn_exp2f(ap[0]), __builtin_amdgcn_exp2f(ap[1])};
      en[t][r] = (f32x2){__builtin_amdgcn_exp2f(an[0]), __builtin_amdgcn_exp2f(an[1])};
    }
    const f32x2 s2p = (ep[t][0] + ep[t][1]) + (ep[t][2] + ep[t][3]), s2n = (en[t][0] + en[t][1]) + (en[t][2] + en[t][3]);
    sp[t] = s2p[0] + s2p[1]; sn[t] = s2n[0] + s2n[1];
  }
  XRED4("v_add_f32", sp[0], sn[0], sp[1], sn[1]);
#pragma unroll
  for (int t = 0; t < 2; ++t) {
    const float rp = __builtin_amdgcn_rcpf(sp[t]), rn = __builtin_amdgcn_rcpf(sn[t]);
    const f32x2 rp2 = {rp, rp}, rn2 = {rn, rn};
#pragma unroll
    for (int r = 0; r < 4; ++r) {
      const f32x2 a = ep[t][r] * rp2, b = en[t][r] * rn2;
      pos[t][r] = pk2(a[0], a[1]); neg[t][r] = pk2(b[0], b[1]);
    }
  }
}

__global__ __launch_bounds__(512) void k_ln(const u16* __restrict__ x, u16* __restrict__ xn, char* __restrict__ kvx, unsigned* __restrict__ cnt) {
  __shared__ f32x2 st[8][8];
  {
    if (blockIdx.x == 255 && threadIdx.x < 16) cnt[threadIdx.x * 32] = 0u;
    const unsigned e = blockIdx.x * 512 + threadIdx.x;
    if (e < KVX_BYTES / 16) *(u32x4*)(kvx + (size_t)e * 16) = (u32x4){~0u, ~0u, ~0u, ~0u};
  }
  const int lane = threadIdx.x & 63, wid = threadIdx.x >> 6;
  const int r8 = lane >> 3, c8 = lane & 7;
  const int row = blockIdx.x * 8 + r8;
  const u16* src = x + (size_t)row * D_ + wid * 128 + c8 * 8;
  const u32x4 v0 = *(const u32x4*)src, v1 = *(const u32x4*)(src + 64);
  float f[16];
#pragma unroll
  for (int i = 0; i < 4; ++i) { f[2 * i] = blo(v0[i]); f[2 * i + 1] = bhi(v0[i]); f[8 + 2 * i] = blo(v1[i]); f[8 + 2 * i + 1] = bhi(v1[i]); }
  float s = 0.f;
#pragma unroll
  for (int i = 0; i < 16; ++i) s += f[i];
  s += __shfl_xor(s, 1); s += __shfl_xor(s, 2); s += __shfl_xor(s, 4);
  const float mw = s * (1.0f / 128.f);
  float q = 0.f;
#pragma unroll
  for (int i = 0; i < 16; ++i) { const float d = f[i] - mw; q += d * d; }
  q += __shfl_xor(q, 1); q += __shfl_xor(q, 2); q += __shfl_xor(q, 4);
  if (c8 == 0) st[wid][r8] = (f32x2){mw, q};
  __syncthreads();
  float mean = 0.f, m2 = 0.f;
  f32x2 p[8];
#pragma unroll
  for (int w = 0; w < 8; ++w) { p[w] = st[w][r8]; mean += p[w][0]; m2 += p[w][1]; }
  mean *= 0.125f;
#pragma unroll
  for (int w = 0; w < 8; ++w) { const float d = p[w][0] - mean; m2 += 128.f * d * d; }
  const float mb = rbf(mean), vb = rbf(m2 * (1.0f / D_));
  const float sd = rbf(sqrtf(rbf(vb + 1e-5f)));
  u32x4 o0, o1;
#pragma unroll
  for (int i = 0; i < 4; ++i) {
    o0[i] = pk2(rbf(f[2 * i] - mb) / sd, rbf(f[2 * i + 1] - mb) / sd);
    o1[i] = pk2(rbf(f[8 + 2 * i] - mb) / sd, rbf(f[8 + 2 * i + 1] - mb) / sd);
  }
  const int rt = row >> 4, i16 = row & 15;
  const int kgA = wid * 4 + (c8 >> 2), g = c8 & 3;
  u16* dst = xn + ((size_t)(rt * NKG1 + kgA) * 64 + g * 16 + i16) * 8;
  *(u32x4*)dst = o0;
  *(u32x4*)(dst + 2 * 512) = o1;
}

constexpr int G1_SLOT = 32768;
constexpr int G1_LDS = 4 * G1_SLOT + 4096 + 2048;

DI float gelu_tanh(float x) {
  const float t = x * x;
  const float a = x * __builtin_fmaf(t, -0.10294324f, -2.3022082f);
  const float e = __builtin_amdgcn_exp2f(a);
  return x * __builtin_amdgcn_rcpf(1.0f + e);
}

DI f32x2 gelu2(f32x2 x) {
  const f32x2 c1 = {-0.10294324f, -0.10294324f}, c0 = {-2.3022082f, -2.3022082f}, one = {1.0f, 1.0f};
  const f32x2 a = x * __builtin_elementwise_fma(x * x, c1, c0);
  const f32x2 d = one + (f32x2){__builtin_amdgcn_exp2f(a[0]), __builtin_amdgcn_exp2f(a[1])};
  return x * (f32x2){__builtin_amdgcn_rcpf(d[0]), __builtin_amdgcn_rcpf(d[1])};
}

template <bool QKV>
DI void gemm1_body(const u16* __restrict__ xn, const u16* __restrict__ W, int wcol0, int woff, int panel, int tcol,
                   const u16* __restrict__ b1, const u16* __restrict__ wphi,
                   u16* __restrict__ act, char* __restrict__ kvx, char* __restrict__ xch,
                   const u16* __restrict__ w2, const u16* __restrict__ wo, LAS char* lds) {
  constexpr int LDW = QKV ? 3 * D_ : F_;
  constexpr int NG = QKV ? 3 : 4;
  constexpr int NT = QKV ? 6 : 7;
  const int tid = threadIdx.x, lane = tid & 63;
  const int wid = __builtin_amdgcn_readfirstlane(tid >> 6);
  const int wr = wid >> 1, wc = wid & 1;
  const int g = lane >> 4, i16 = lane & 15, q4 = i16 >> 2, p4 = lane & 3;
  const unsigned ldsb = (unsigned)(size_t)lds;

  STAMP(0, 0);
  const char* abase = (const char*)xn + (size_t)panel * 16 * NKG1 * 1024;
  const unsigned avoff = (unsigned)(wid * 2) * NKG1 * 1024 + lane * 16;
  const char* bbase = (const char*)W;
  unsigned bvoff[2]; unsigned bdst[2];
#pragma unroll
  for (int j = 0; j < 2; ++j) {
    int pb = QKV ? (wid + 8 * j) : (wid * 2 + j);
    if (QKV && pb >= 12) pb -= 8;
    const int kg = pb / NG, cg = pb % NG;
    const int r = lane >> 3, sw = ((r >> 1) & 1) | ((kg & 1) << 1);
    const int cc = (lane & 7) ^ (sw << 1);
    const int col = QKV ? (cg * D_ + wcol0) : (wcol0 + cg * 64);
    bvoff[j] = (unsigned)(((kg * 8 + r) * LDW + col + cc * 8) * 2);
    bdst[j] = ldsb + 16384 + pb * 1024;
  }
  const unsigned adst = ldsb + wid * 2048;

  LAS char* ard[2];
  ard[0] = opaque(lds + wr * 4096 + lane * 16); ard[1] = opaque(ard[0] + 65536);
  constexpr int NB = QKV ? 2 : 4;
  LAS char* brd[2][NB];
#pragma unroll
  for (int j = 0; j < NB; ++j) {
    const int n0 = QKV ? (wc * 32 + j * 16) : (woff + wc * 112 + j * 16);
    const int sw = (q4 >> 1) | ((g & 1) << 1);
    brd[0][j] = opaque(lds + 16384 + (g * NG + (n0 >> 6)) * 1024 + q4 * 128 + (((((n0 & 63) >> 3) ^ (sw << 1)) + (p4 >> 1)) << 4) + (p4 & 1) * 8);
    brd[1][j] = opaque(brd[0][j] + 65536);
  }

  if (QKV) {
    if (tid < 256) {
      const u32x4 w = *(const u32x4*)(wphi + (size_t)tcol * 2 * 1024 + tid * 8);
      *(LAS u32x4*)(lds + 4 * G1_SLOT + tid * 16) = w;
    }
  }

  f32x4 acc[4][NT];
#pragma unroll
  for (int m = 0; m < 4; ++m)
#pragma unroll
    for (int n = 0; n < NT; ++n) acc[m][n] = (f32x4){0.f, 0.f, 0.f, 0.f};

  bf16x8 fa[2][4], fb[2][NT];

  auto issue = [&](int q, int stage, int slot) {
    if (q < 2) glds16(avoff + q * NKG1 * 1024, abase + (size_t)stage * 1024, adst + q * 1024 + slot * G1_SLOT);
    else       glds16(bvoff[q - 2], bbase + (size_t)stage * 64 * LDW, bdst[q - 2] + slot * G1_SLOT);
  };
  auto rdA = [&](auto P, auto SL, int m) { fa[P.v][m] = lds_b128(ard[SL.v >> 1] + (SL.v & 1) * G1_SLOT + m * 1024); };
  auto rdB = [&](auto P, auto SL, int n) { LAS char* p = brd[SL.v >> 1][n % NB] + (n / NB) * 1024 + (SL.v & 1) * G1_SLOT; fb[P.v][n] = cat8(lds_tr(p), lds_tr(p + 512)); };
  auto mma4 = [&](auto P, int n) {
#pragma unroll
    for (int m = 0; m < 4; ++m) {
      if (QKV && n >= 4) acc[m][n] = MFMA16(fa[P.v][m], fb[P.v][n], acc[m][n]);
      else               acc[m][n] = MFMA16(fb[P.v][n], fa[P.v][m], acc[m][n]);
    }
  };
  auto step = [&](auto SC, auto VM, auto GL, auto RD, int s) {
    constexpr int P = SC.v & 1, SNv = (SC.v + 1) & 3;
    const IC<P> Pc; const IC<P ^ 1> Pn; const IC<SNv> SN;
    if constexpr (RD.v) { WAIT_V(VM.v); WAIT_L(0); BAR(); }
    SCHED();
    if constexpr (GL.v) issue(0, s + 4, SC.v);
    if constexpr (RD.v) { rdA(Pn, SN, 0); rdA(Pn, SN, 1); rdB(Pn, SN, 0); }
    mma4(Pc, 0); SCHED();
    if constexpr (GL.v) issue(1, s + 4, SC.v);
    if constexpr (RD.v) { rdA(Pn, SN, 2); rdA(Pn, SN, 3); rdB(Pn, SN, 1); }
    mma4(Pc, 1); SCHED();
    if constexpr (GL.v) issue(2, s + 4, SC.v);
    if constexpr (RD.v) { rdB(Pn, SN, 2); rdB(Pn, SN, 3); }
    mma4(Pc, 2); SCHED();
    if constexpr (GL.v) issue(3, s + 4, SC.v);
    if constexpr (RD.v) { rdB(Pn, SN, 4); rdB(Pn, SN, 5); }
    mma4(Pc, 3); SCHED();
    if constexpr (RD.v && NT == 7) rdB(Pn, SN, 6);
    mma4(Pc, 4); SCHED();
    mma4(Pc, 5);
    if constexpr (NT == 7) mma4(Pc, 6);
    SCHED();
  };

#pragma unroll
  for (int st = 0; st < 4; ++st)
#pragma unroll
    for (int q = 0; q < 4; ++q) issue(q, st, st);
  WAIT_V(12); WAIT_L(0); BAR();
  STAMP(0, 1);
  {
    const IC<0> P0; const IC<0> S0;
#pragma unroll
    for (int m = 0; m < 4; ++m) rdA(P0, S0, m);
#pragma unroll
    for (int n = 0; n < NT; ++n) rdB(P0, S0, n);
  }
#pragma unroll 1
  for (int s = 0; s < 28; s += 4) {
    step(IC<0>{}, IC<8>{}, IC<1>{}, IC<1>{}, s);
    step(IC<1>{}, IC<8>{}, IC<1>{}, IC<1>{}, s + 1);
    step(IC<2>{}, IC<8>{}, IC<1>{}, IC<1>{}, s + 2);
    step(IC<3>{}, IC<8>{}, IC<1>{}, IC<1>{}, s + 3);
  }
  step(IC<0>{}, IC<8>{}, IC<0>{}, IC<1>{}, 28);
  {
    int wv = blockIdx.x * 8 + wid;
    if (wv >= 1152) wv -= 1152;
    const char* src = uniform_ptr(wv < 896 ? (const char*)w2 + (size_t)wv * 8192 : (const char*)wo + (size_t)(wv - 896) * 8192);
    asm volatile("s_mov_b32 m0, %2\n\ts_nop 0\n\tglobal_load_lds_dword %0, %1" ::"v"(lane * 128), "s"(src), "s"(ldsb + 4 * G1_SLOT + 4096 + wid * 256) : "memory");
    char* xp = xch + (size_t)blockIdx.x * 16384 + tid * 16;
    const u32x4 ones = {~0u, ~0u, ~0u, ~0u};
    asm volatile("global_store_dwordx4 %0, %2, off sc1\n\tglobal_store_dwordx4 %1, %2, off sc1\n\ts_nop 1" ::"v"(xp), "v"(xp + 8192), "v"(ones) : "memory");
  }
  step(IC<1>{}, IC<4 + 3>{}, IC<0>{}, IC<1>{}, 29);
  step(IC<2>{}, IC<0 + 3>{}, IC<0>{}, IC<1>{}, 30);
  step(IC<3>{}, IC<0>{}, IC<0>{}, IC<0>{}, 31);
  STAMP(0, 2);

  if (!QKV) {
    const int ncol0 = tcol * 224 + wc * 112 + 4 * g;
    u32x2 bb[NT];
#pragma unroll
    for (int n = 0; n < NT; ++n) bb[n] = *(const u32x2*)(b1 + ncol0 + n * 16);
    auto act4 = [&](int m, int n) {
      const f32x2 yA = gelu2((f32x2){acc[m][n][0] + blo(bb[n][0]), acc[m][n][1] + bhi(bb[n][0])});
      const f32x2 yB = gelu2((f32x2){acc[m][n][2] + blo(bb[n][1]), acc[m][n][3] + bhi(bb[n][1])});
      return (u32x2){pk2(yA[0], yA[1]), pk2(yB[0], yB[1])};
    };
    const int rt0 = panel * 16 + wr * 4;
    char* obase = (char*)act + ((size_t)rt0 * NKG2 + tcol * 7) * 1024 + lane * 16;
    if (wc == 0) {
#pragma unroll
      for (int m = 0; m < 4; ++m) {
        char* rb = obase + (size_t)m * NKG2 * 1024;
#pragma unroll
        for (int p = 0; p < 3; ++p) { const u32x2 lo = act4(m, 2 * p), hi = act4(m, 2 * p + 1); st_sc1(rb + p * 1024, (u32x4){lo[0], lo[1], hi[0], hi[1]}); }
        st8_sc1(rb + 3 * 1024, act4(m, 6 % NT));
      }
    } else {
#pragma unroll
      for (int m = 0; m < 4; ++m) {
        char* rb = obase + (size_t)m * NKG2 * 1024;
        st8_sc1(rb + 3 * 1024 + 8, act4(m, 0));
#pragma unroll
        for (int p = 0; p < 3; ++p) { const u32x2 lo = act4(m, 2 * p + 1), hi = act4(m, (2 * p + 2) % NT); st_sc1(rb + (4 + p) * 1024, (u32x4){lo[0], lo[1], hi[0], hi[1]}); }
      }
    }
  } else {
    const int head = tcol * 2 + wc;
    LAS char* wp = lds + 4 * G1_SLOT + wc * 2048;
    bf16x8 wf[2];
#pragma unroll
    for (int ft = 0; ft < 2; ++ft) {
      LAS char* p = wp + (4 * g + q4) * 64 + (16 * ft + 4 * p4) * 2;
      wf[ft] = cat8(lds_tr(p), lds_tr(p + 16 * 64));
    }
    const f32x4 z4 = {0.f, 0.f, 0.f, 0.f};
    LAS char* sc = lds + wid * 8704;
#pragma unroll
    for (int mp = 0; mp < 2; ++mp) {
      f32x4 pp[2][2]; u32x4 pos[2], neg[2];
#pragma unroll
      for (int t = 0; t < 2; ++t) {
        const bf16x8 bk = pack8(acc[2 * mp + t][2], acc[2 * mp + t][3]);
        pp[t][0] = MFMA16(wf[0], bk, z4); pp[t][1] = MFMA16(wf[1], bk, z4);
      }
      dual_softmax2(pp, pos, neg);
#pragma unroll
      for (int t = 0; t < 2; ++t) {
        LAS char* rowp = sc + (16 * (2 * mp + t) + i16) * 136 + 8 * g;
        *(LAS u32x2*)(rowp) = (u32x2){pos[t][0], pos[t][1]};      *(LAS u32x2*)(rowp + 32) = (u32x2){pos[t][2], pos[t][3]};
        *(LAS u32x2*)(rowp + 64) = (u32x2){neg[t][0], neg[t][1]}; *(LAS u32x2*)(rowp + 96) = (u32x2){neg[t][2], neg[t][3]};
      }
    }
    WAIT_L(0);
    STAMP(0, 5);
    f32x4 kv[4][3];
#pragma unroll
    for (int ft = 0; ft < 4; ++ft)
#pragma unroll
      for (int d = 0; d < 3; ++d) kv[ft][d] = z4;
    bf16x8 ones;
#pragma unroll
    for (int j = 0; j < 8; ++j) ones[j] = (__bf16)1.0f;
#pragma unroll
    for (int ks = 0; ks < 2; ++ks) {
      const bf16x8 bv0 = pack8(acc[2 * ks][4], acc[2 * ks + 1][4]);
      const bf16x8 bv1 = pack8(acc[2 * ks][5], acc[2 * ks + 1][5]);
#pragma unroll
      for (int ft = 0; ft < 4; ++ft) {
        LAS char* p = sc + (32 * ks + 4 * g + q4) * 136 + (16 * ft + 4 * p4) * 2;
        const bf16x8 a = cat8(lds_tr(p), lds_tr(p + 16 * 136));
        kv[ft][0] = MFMA16(a, bv0, kv[ft][0]);
        kv[ft][1] = MFMA16(a, bv1, kv[ft][1]);
        kv[ft][2] = MFMA16(a, ones, kv[ft][2]);
      }
    }
    STAMP(0, 6);
    WAIT_L(0); BAR();
    STAMP(0, 7);
    {
      LAS char* pp = lds + wid * 8448;
#pragma unroll
      for (int sg = 0; sg < 2; ++sg) {
#pragma unroll
        for (int d = 0; d < 2; ++d) {
          *(LAS f32x4*)(pp + (sg * 2 + d) * 2048 + lane * 16) = kv[2 * sg][d];
          *(LAS f32x4*)(pp + (sg * 2 + d) * 2048 + 1024 + lane * 16) = kv[2 * sg + 1][d];
        }
        if (i16 == 0) {
          *(LAS f32x4*)(pp + 8192 + sg * 128 + g * 16) = kv[2 * sg][2];
          *(LAS f32x4*)(pp + 8192 + sg * 128 + 64 + g * 16) = kv[2 * sg + 1][2];
        }
      }
    }
    WAIT_L(0); BAR();
    char* kvt = kvx + (size_t)tcol * KVX_TILE;
    char* kvmine = kvt + (size_t)panel * 16 * KVX_TILE;
    {
      LAS char* pp = lds + (tid >> 8) * 8448 + ((tid >> 6) & 3) * 2048 + lane * 16;
      f32x4 lo = *(LAS f32x4*)(pp), hi = *(LAS f32x4*)(pp + 1024);
#pragma unroll
      for (int w = 1; w < 4; ++w) { lo += *(LAS f32x4*)(pp + w * 16896); hi += *(LAS f32x4*)(pp + w * 16896 + 1024); }
      st_sc1(kvmine + tid * 16, (u32x4){pk2(lo[0], lo[1]), pk2(lo[2], lo[3]), pk2(hi[0], hi[1]), pk2(hi[2], hi[3])});
    }
    if (tid < 16) {
      LAS char* pp = lds + (tid >> 3) * 8448 + 8192 + ((tid >> 2) & 1) * 128 + (tid & 3) * 16;
      f32x4 lo = *(LAS f32x4*)(pp), hi = *(LAS f32x4*)(pp + 64);
#pragma unroll
      for (int w = 1; w < 4; ++w) { lo += *(LAS f32x4*)(pp + w * 16896); hi += *(LAS f32x4*)(pp + w * 16896 + 64); }
      st_sc1(kvmine + 8192 + tid * 16, (u32x4){pk2(lo[0], lo[1]), pk2(lo[2], lo[3]), pk2(hi[0], hi[1]), pk2(hi[2], hi[3])});
    }
    STAMP(0, 8);
    u32x4 qp[2][2], qn[2][2];
    auto qsm = [&](int mp) {
      f32x4 pp[2][2];
#pragma unroll
      for (int t = 0; t < 2; ++t) {
        const bf16x8 bq = pack8(acc[2 * mp + t][0], acc[2 * mp + t][1]);
        pp[t][0] = MFMA16(wf[0], bq, z4); pp[t][1] = MFMA16(wf[1], bq, z4);
      }
      dual_softmax2(pp, qp[mp], qn[mp]);
    };
    qsm(0);
    const char* kb0 = uniform_ptr(kvt), * kb1 = uniform_ptr(kvt + (size_t)32 * KVX_TILE);
    const char* kb2 = uniform_ptr(kvt + (size_t)64 * KVX_TILE), * kb3 = uniform_ptr(kvt + (size_t)96 * KVX_TILE);
    u32x4 pv[8], pz[8];
#define KV_GATHER(pv, OFF, TAIL) asm volatile( \
          "global_load_dwordx4 %0, %8, %10 sc1\n\tglobal_load_dwordx4 %1, %9, %10 sc1\n\t" \
          "global_load_dwordx4 %2, %8, %11 sc1\n\tglobal_load_dwordx4 %3, %9, %11 sc1\n\t" \
          "global_load_dwordx4 %4, %8, %12 sc1\n\tglobal_load_dwordx4 %5, %9, %12 sc1\n\t" \
          "global_load_dwordx4 %6, %8, %13 sc1\n\tglobal_load_dwordx4 %7, %9, %13 sc1" TAIL \
          : "=&v"(pv[0]), "=&v"(pv[1]), "=&v"(pv[2]), "=&v"(pv[3]), "=&v"(pv[4]), "=&v"(pv[5]), "=&v"(pv[6]), "=&v"(pv[7]) \
          : "v"(OFF), "v"((OFF) + 16 * KVX_TILE), "s"(kb0), "s"(kb1), "s"(kb2), "s"(kb3) : "memory")
#define KV_WAIT(pv) asm volatile("s_waitcnt vmcnt(0)" \
          : "+v"(pv[0]), "+v"(pv[1]), "+v"(pv[2]), "+v"(pv[3]), "+v"(pv[4]), "+v"(pv[5]), "+v"(pv[6]), "+v"(pv[7]) :: "memory")
    LAS char* kvs = lds + 3 * G1_SLOT;
    auto finish = [&](u32x4 (&pv)[8], unsigned off) {
      for (unsigned spins = 0;; ++spins) {
        unsigned m = 0;
#pragma unroll
        for (int p = 0; p < 8; ++p) m = umax4(pv[p], m);
        if (__all(m != 0xffffffffu) || spins > (1u << 14)) break;
        __builtin_amdgcn_s_sleep(2);
        KV_GATHER(pv, off, "\n\ts_waitcnt vmcnt(0)");
      }
      f32x2 t[4];
#pragma unroll
      for (int r = 0; r < 4; ++r) t[r] = (f32x2){blo(pv[0][r]), bhi(pv[0][r])};
#pragma unroll
      for (int p = 1; p < 8; ++p)
#pragma unroll
        for (int r = 0; r < 4; ++r) t[r] += (f32x2){blo(pv[p][r]), bhi(pv[p][r])};
      *(LAS u32x4*)(kvs + off) = (u32x4){pk2(t[0][0], t[0][1]), pk2(t[1][0], t[1][1]), pk2(t[2][0], t[2][1]), pk2(t[3][0], t[3][1])};
    };
    const unsigned go = (unsigned)tid * 16, gz = 8192u + (unsigned)i16 * 16;
    STAMP(0, 9);
    SCHED();
    KV_GATHER(pv, go, "");
    KV_GATHER(pz, gz, "");
    SCHED();
    qsm(1);
    STAMP(0, 10);
    SCHED();
    KV_WAIT(pv);
    KV_WAIT(pz);
    SCHED();
    finish(pv, go);
    if (wid == 0) finish(pz, gz);
    STAMP(0, 11);
    WAIT_L(0); BAR();
    bf16x8 a[2][3];
#pragma unroll
    for (int sg = 0; sg < 2; ++sg) {
#pragma unroll
      for (int d = 0; d < 2; ++d) a[sg][d] = lds_b128(kvs + wc * 4096 + (sg * 2 + d) * 1024 + lane * 16);
      a[sg][2] = lds_b128(kvs + 8192 + wc * 128 + sg * 64 + g * 16);
    }
#pragma unroll
    for (int m = 0; m < 4; ++m) {
      const bf16x8 bp = __builtin_bit_cast(bf16x8, qp[m >> 1][m & 1]), bn = __builtin_bit_cast(bf16x8, qn[m >> 1][m & 1]);
      f32x4 o0 = MFMA16(a[0][0], bp, z4), o1 = MFMA16(a[0][1], bp, z4), dn = MFMA16(a[0][2], bp, z4);
      o0 = MFMA16(a[1][0], bn, o0); o1 = MFMA16(a[1][1], bn, o1); dn = MFMA16(a[1][2], bn, dn);
      const float rd = __builtin_amdgcn_rcpf(dn[0] + 1e-6f);
      const int rt = panel * 16 + wr * 4 + m;
      u32x4 w;
      w[0] = pk2(o0[0] * rd, o0[1] * rd); w[1] = pk2(o0[2] * rd, o0[3] * rd);
      w[2] = pk2(o1[0] * rd, o1[1] * rd); w[3] = pk2(o1[2] * rd, o1[3] * rd);
      st_sc1((char*)act + ((size_t)rt * NKG2 + 112 + head) * 1024 + lane * 16, w);
    }
  }
  STAMP(0, 3);
}

constexpr int G2_SLOT = 32768;
constexpr int G2_POLL = 4 * G2_SLOT + 4096;
constexpr int KSPLIT = 40;

DI void gemm2_body(const char* __restrict__ act, const u16* __restrict__ w2, const u16* __restrict__ wo,
                   const u16* __restrict__ x, const u16* __restrict__ b2, char* __restrict__ xch, unsigned* cntp, u16* __restrict__ out,
                   int split, int panel, int pm, int pn, LAS char* lds) {
  const unsigned ldsb = (unsigned)(size_t)lds;
  int lane = __builtin_amdgcn_mbcnt_hi(~0u, __builtin_amdgcn_mbcnt_lo(~0u, 0u));
  asm volatile("" : "+v"(lane));
  const int wid = __builtin_amdgcn_readfirstlane(threadIdx.x >> 6);
  const int kh = wid >> 2, wr = (wid >> 1) & 1, wc = wid & 1;
  const int g = lane >> 4, i16 = lane & 15, q4 = i16 >> 2, p4 = lane & 3;
  const int b = blockIdx.x;
  gu32* cnt = (gu32*)cntp;
  const int kt0 = split ? KSPLIT : 0, nst = split ? 72 - KSPLIT : KSPLIT;
  const char* abase = uniform_ptr(act + ((size_t)(pm * 8) * NKG2 + kt0 * 2) * 1024);
  const unsigned avoff = (unsigned)wid * NKG2 * 1024 + lane * 16;
  const unsigned adst = ldsb + wid * 1024;
  const int r8 = lane >> 3;
  const unsigned bvoff = (unsigned)((wid * 8 + r8) * D_ + pn * 128 + (((lane & 7) ^ (((r8 >> 1) & 3) << 1)) << 3)) * 2;
  const unsigned bdst = ldsb + 16384 + wid * 2048;
  const char* wb = uniform_ptr((const char*)w2 + (size_t)kt0 * 64 * D_ * 2);
  auto issue = [&](int q, int t, int slot) {
    if (q < 2) glds16_sc1(avoff + q * 1024, abase + (size_t)t * 2048, adst + q * 8192 + slot * G2_SLOT);
    else       glds16(bvoff + (q - 2) * 128, wb + (size_t)t * 64 * D_ * 2, bdst + (q - 2) * 1024 + slot * G2_SLOT);
  };
  WAIT_V(0); WAIT_L(0); BAR();
  if (threadIdx.x == 0) __hip_atomic_fetch_add(cnt + (split * 8 + panel) * 32, 1u, __ATOMIC_RELAXED, __HIP_MEMORY_SCOPE_AGENT);
#pragma unroll
  for (int st = 0; st < 4; ++st) { issue(2, st, st); issue(3, st, st); }
  if (wid == 0) {
    unsigned spins = 0;
    while (__builtin_amdgcn_readfirstlane(__hip_atomic_load(cnt + panel * 32, __ATOMIC_RELAXED, __HIP_MEMORY_SCOPE_AGENT)) < 16u && ++spins < (1u << 16))
      __builtin_amdgcn_s_sleep(1);
  }
  BAR();
  STAMP(1, 0);
#pragma unroll
  for (int st = 0; st < 4; ++st) { issue(0, st, st); issue(1, st, st); }

  LAS char* ard[2];
  ard[0] = opaque(lds + kh * 8192 + wr * 4096 + lane * 16); ard[1] = opaque(ard[0] + 65536);
  LAS char* brd[2][4];
#pragma unroll
  for (int j = 0; j < 4; ++j) {
    const int r = 4 * (g & 1) + q4, sw = (r >> 1) & 3;
    brd[0][j] = opaque(lds + 16384 + ((kh * 4 + (g >> 1)) * 2 + wc) * 1024 + r * 128 + ((((2 * j) ^ (sw << 1)) + (p4 >> 1)) << 4) + (p4 & 1) * 8);
    brd[1][j] = opaque(brd[0][j] + 65536);
  }
  f32x4 acc[4][4];
#pragma unroll
  for (int m = 0; m < 4; ++m)
#pragma unroll
    for (int n = 0; n < 4; ++n) acc[m][n] = (f32x4){0.f, 0.f, 0.f, 0.f};
  bf16x8 fa[2][4], fb[2][4];
  auto rdA = [&](auto P, auto SL, int m) { fa[P.v][m] = lds_b128(ard[SL.v >> 1] + (SL.v & 1) * G2_SLOT + m * 1024); };
  auto rdB = [&](auto P, auto SL, int n) { LAS char* p = brd[SL.v >> 1][n] + (SL.v & 1) * G2_SLOT; fb[P.v][n] = cat8(lds_tr(p), lds_tr(p + 4096)); };
  auto mma4 = [&](auto P, int n) {
#pragma unroll
    for (int m = 0; m < 4; ++m) acc[m][n] = MFMA16(fb[P.v][n], fa[P.v][m], acc[m][n]);
  };
  auto step = [&](auto SC, auto VM, auto GL, auto RD, int t) {
    constexpr int P = SC.v & 1, SNv = (SC.v + 1) & 3;
    const IC<P> Pc; const IC<P ^ 1> Pn; const IC<SNv> SN;
    if constexpr (RD.v) { WAIT_V(VM.v); WAIT_L(0); BAR(); }
    SCHED();
    if constexpr (GL.v) { issue(0, t + 4, SC.v); issue(1, t + 4, SC.v); }
    if constexpr (RD.v) { rdA(Pn, SN, 0); rdA(Pn, SN, 1); rdB(Pn, SN, 0); }
    mma4(Pc, 0); SCHED();
    if constexpr (GL.v) { issue(2, t + 4, SC.v); issue(3, t + 4, SC.v); }
    if constexpr (RD.v) { rdA(Pn, SN, 2); rdA(Pn, SN, 3); rdB(Pn, SN, 1); }
    mma4(Pc, 1); SCHED();
    if constexpr (RD.v) { rdB(Pn, SN, 2); rdB(Pn, SN, 3); }
    mma4(Pc, 2); SCHED();
    mma4(Pc, 3); SCHED();
  };
  WAIT_V(6); BAR();
  STAMP(1, 1);
  {
    const IC<0> P0; const IC<0> S0;
#pragma unroll
    for (int m = 0; m < 4; ++m) rdA(P0, S0, m);
#pragma unroll
    for (int n = 0; n < 4; ++n) rdB(P0, S0, n);
  }
  const IC<1> Y; const IC<0> N_; const IC<8> V8;
  step(IC<0>{}, IC<4>{}, Y, Y, 0); step(IC<1>{}, IC<6>{}, Y, Y, 1); step(IC<2>{}, V8, Y, Y, 2); step(IC<3>{}, V8, Y, Y, 3);
  const int tp = split ? 4 : 28;
#pragma unroll 1
  for (int t = 4; t < nst - 4; t += 4) {
    if (wid == 0) {
      if (t == tp) {
        asm volatile("s_mov_b32 m0, %2\n\ts_nop 0\n\tglobal_load_lds_dword %0, %1 sc1" ::"v"(0u), "s"(uniform_ptr((const char*)(cntp + (8 + panel) * 32))), "s"(ldsb + G2_POLL) : "memory");
      } else if (t == tp + 4) {
        if (__builtin_amdgcn_readfirstlane(*(LAS unsigned*)(lds + G2_POLL)) < 16u) {
          unsigned spins = 0;
          while (__builtin_amdgcn_readfirstlane(__hip_atomic_load(cnt + (8 + panel) * 32, __ATOMIC_RELAXED, __HIP_MEMORY_SCOPE_AGENT)) < 16u && ++spins < (1u << 16))
            __builtin_amdgcn_s_sleep(1);
        }
      }
    }
    if (t == 12 && split == 1) wb = uniform_ptr((const char*)wo - (size_t)(56 - KSPLIT) * 64 * D_ * 2);
    step(IC<0>{}, V8, Y, Y, t); step(IC<1>{}, V8, Y, Y, t + 1); step(IC<2>{}, V8, Y, Y, t + 2); step(IC<3>{}, V8, Y, Y, t + 3);
  }
  step(IC<0>{}, V8, N_, Y, 0);
  step(IC<1>{}, IC<4>{}, N_, Y, 0);
  step(IC<2>{}, IC<0>{}, N_, Y, 0);
  step(IC<3>{}, IC<0>{}, N_, N_, 0);
  STAMP(1, 2);
  const int quad = wid & 3;
  {
    LAS char* pp = lds + (quad * 2 + kh) * 8192 + lane * 16;
#pragma unroll
    for (int mm = 0; mm < 2; ++mm)
#pragma unroll
      for (int n = 0; n < 4; ++n) *(LAS f32x4*)(pp + (mm * 4 + n) * 1024) = kh ? acc[mm][n] : acc[2 + mm][n];
  }
  const bool keep = (wr == split);
  const int row0 = pm * 128 + wr * 64 + kh * 32 + i16, col0 = pn * 128 + wc * 64 + 4 * g;
  u32x2 xv[2][4], bv[4];
  if (keep) {
#pragma unroll
    for (int n = 0; n < 4; ++n) bv[n] = *(const u32x2*)(b2 + col0 + n * 16);
#pragma unroll
    for (int mm = 0; mm < 2; ++mm)
#pragma unroll
      for (int n = 0; n < 4; ++n) xv[mm][n] = *(const u32x2*)(x + (size_t)(row0 + mm * 16) * D_ + col0 + n * 16);
  }
  WAIT_L(0); BAR();
  f32x4 sum[2][4];
  {
    LAS char* pp = lds + (quad * 2 + (kh ^ 1)) * 8192 + lane * 16;
#pragma unroll
    for (int mm = 0; mm < 2; ++mm)
#pragma unroll
      for (int n = 0; n < 4; ++n) sum[mm][n] = (kh ? acc[2 + mm][n] : acc[mm][n]) + *(LAS f32x4*)(pp + (mm * 4 + n) * 1024);
  }
  const int piece = kh * 2 + wc;
  if (!keep) {
    char* dst = xch + (size_t)b * 16384 + piece * 4096 + lane * 16;
#pragma unroll
    for (int mm = 0; mm < 2; ++mm)
#pragma unroll
      for (int h = 0; h < 2; ++h)
        st_sc1(dst + (mm * 2 + h) * 1024, (u32x4){pk2(sum[mm][2 * h][0], sum[mm][2 * h][1]), pk2(sum[mm][2 * h][2], sum[mm][2 * h][3]),
                                                  pk2(sum[mm][2 * h + 1][0], sum[mm][2 * h + 1][1]), pk2(sum[mm][2 * h + 1][2], sum[mm][2 * h + 1][3])});
  } else {
    const char* src = xch + (size_t)(b ^ 1) * 16384 + piece * 4096 + lane * 16;
    u32x4 pv[4];
    for (unsigned spins = 0;; ++spins) {
      asm volatile("global_load_dwordx4 %0, %1, off offset:3072 sc1\n\ts_waitcnt vmcnt(0)" : "=&v"(pv[3]) : "v"(src) : "memory");
      if (__all(fresh4(pv[3])) || spins > (1u << 14)) break;
      __builtin_amdgcn_s_sleep(1);
    }
    for (unsigned spins = 0;; ++spins) {
      asm volatile(
          "global_load_dwordx4 %0, %3, off sc1\n\tglobal_load_dwordx4 %1, %3, off offset:1024 sc1\n\t"
          "global_load_dwordx4 %2, %3, off offset:2048 sc1\n\ts_waitcnt vmcnt(0)"
          : "=&v"(pv[0]), "=&v"(pv[1]), "=&v"(pv[2]) : "v"(src) : "memory");
      if (__all(fresh4(pv[0]) && fresh4(pv[1]) && fresh4(pv[2])) || spins > (1u << 14)) break;
      __builtin_amdgcn_s_sleep(1);
    }
#pragma unroll
    for (int mm = 0; mm < 2; ++mm)
#pragma unroll
      for (int n = 0; n < 4; ++n) {
        const unsigned q0 = pv[mm * 2 + (n >> 1)][2 * (n & 1)], q1 = pv[mm * 2 + (n >> 1)][2 * (n & 1) + 1];
        const f32x4 v = sum[mm][n];
        u32x2 o;
        o[0] = pk2(v[0] + blo(q0) + blo(xv[mm][n][0]) + blo(bv[n][0]), v[1] + bhi(q0) + bhi(xv[mm][n][0]) + bhi(bv[n][0]));
        o[1] = pk2(v[2] + blo(q1) + blo(xv[mm][n][1]) + blo(bv[n][1]), v[3] + bhi(q1) + bhi(xv[mm][n][1]) + bhi(bv[n][1]));
        *(u32x2*)(out + (size_t)(row0 + mm * 16) * D_ + col0 + n * 16) = o;
      }
  }
  STAMP(1, 3);
  STAMP(1, 4);
}

__global__ __launch_bounds__(512, 2) void k_main(const u16* __restrict__ xn, const u16* __restrict__ wqkv, const u16* __restrict__ w1,
                                                 const u16* __restrict__ b1, const u16* __restrict__ wphi,
                                                 u16* __restrict__ act, char* __restrict__ kvx, char* __restrict__ xch, unsigned* cnt,
                                                 const u16* __restrict__ w2, const u16* __restrict__ wo,
                                                 const u16* __restrict__ x, const u16* __restrict__ b2, u16* __restrict__ out) {
  extern __shared__ __attribute__((aligned(16))) char smem[];
  LAS char* lds = (LAS char*)smem;
  const int b = blockIdx.x, grp = b & 7, idx = b >> 3;
  const int panel = idx & 7, kind = grp & 1, tcol = (grp >> 1) * 4 + (idx >> 3);
  if (kind == 0) {
    gemm1_body<true>(xn, wqkv, tcol * 64, 0, panel, tcol, b1, wphi, act, kvx, xch, w2, wo, lds);
  } else {
    const int c0 = tcol * 224, woff = c0 & 63;
    gemm1_body<false>(xn, w1, c0 - woff, woff, panel, tcol, b1, wphi, act, kvx, xch, w2, wo, lds);
  }
  gemm2_body((const char*)act, w2, wo, x, b2, xch, cnt, out,   kind ^ 1, panel, panel * 2 + ((grp >> 1) & 1), (grp >> 2) * 4 + (idx >> 3), lds);
}

extern "C" void kernel_launch(void* const* d_in, const int* in_sizes, int n_in, void* d_out, int out_size, void* d_ws, size_t ws_size,
                              hipStream_t stream) {
  const u16* x = (const u16*)d_in[0];
  const u16* wqkv = (const u16*)d_in[1];
  const u16* wphi = (const u16*)d_in[2];
  const u16* wo = (const u16*)d_in[3];
  const u16* w1 = (const u16*)d_in[5];
  const u16* b1 = (const u16*)d_in[6];
  const u16* w2 = (const u16*)d_in[7];
  const u16* b2 = (const u16*)d_in[8];
  char* ws = (char*)d_ws;
  u16* xn = (u16*)(ws + WS_XN);
  u16* act = (u16*)(ws + WS_ACT);
  char* kvx = ws + WS_KVX;
  char* xch = ws + WS_XCH;
  unsigned* cnt = (unsigned*)(ws + WS_CNT);
  (void)hipFuncSetAttribute((const void*)k_main, hipFuncAttributeMaxDynamicSharedMemorySize, G1_LDS);
  k_ln<<<S_ / 8, 512, 0, stream>>>(x, xn, kvx, cnt);
  k_main<<<256, 512, G1_LDS, stream>>>(xn, wqkv, w1, b1, wphi, act, kvx, xch, cnt, w2, wo, x, b2, (u16*)d_out);
}
```

```cpp
#include <hip/hip_runtime.h>
#include <hip/hip_bf16.h>
#include <stdint.h>

typedef __bf16 bf16x8 __attribute__((ext_vector_type(8)));
typedef __bf16 bf16x4 __attribute__((ext_vector_type(4)));
typedef __bf16 bf16x2 __attribute__((ext_vector_type(2)));
typedef float f32x4 __attribute__((ext_vector_type(4)));
typedef float f32x2 __attribute__((ext_vector_type(2)));
typedef unsigned u32x4 __attribute__((ext_vector_type(4)));
typedef unsigned u32x2 __attribute__((ext_vector_type(2)));
typedef unsigned short u16;
#define LAS __attribute__((address_space(3)))
#define DI __device__ __forceinline__

constexpr int S_ = 2048, D_ = 1024, F_ = 3584, H_ = 32;
constexpr int KCAT = F_ + D_;
constexpr int NKG1 = D_ / 32;
constexpr int NKG2 = KCAT / 32;

constexpr size_t WS_XN   = 0;
constexpr size_t WS_ACT  = WS_XN + (size_t)S_ * D_ * 2;
constexpr size_t WS_KVX  = WS_ACT + (size_t)S_ * KCAT * 2;
constexpr int KVX_TILE = 8192 + 256;
constexpr size_t KVX_BYTES = (size_t)8 * 16 * KVX_TILE;
constexpr size_t WS_XCH  = WS_KVX + KVX_BYTES;
constexpr size_t WS_CNT  = WS_XCH + (size_t)256 * 16384;

DI float rbf(float x) { return (float)(__bf16)x; }
DI unsigned pk2(float lo, float hi) { bf16x2 v; v[0] = (__bf16)lo; v[1] = (__bf16)hi; return __builtin_bit_cast(unsigned, v); }
DI float blo(unsigned u) { return __uint_as_float(u << 16); }
DI float bhi(unsigned u) { return __uint_as_float(u & 0xffff0000u); }

#define MFMA16(a, b, c) __builtin_amdgcn_mfma_f32_16x16x32_bf16((a), (b), (c), 0, 0, 0)
#define WAIT_V(n) asm volatile("s_waitcnt vmcnt(%0)" ::"n"(n) : "memory")
#define WAIT_L(n) do { static_assert((n) == 0, ""); asm volatile("" ::: "memory"); __builtin_amdgcn_s_waitcnt(0xC07F); asm volatile("" ::: "memory"); } while (0)
#define BAR() do { asm volatile("" ::: "memory"); __builtin_amdgcn_s_barrier(); asm volatile("" ::: "memory"); } while (0)
#define SCHED() __builtin_amdgcn_sched_barrier(0)
#define STAMP(K, I) do { } while (0)
template <int N> struct IC { static constexpr int v = N; };

DI void glds16_sc1(unsigned voff, const void* sbase, unsigned ldsaddr) {
  asm volatile("s_mov_b32 m0, %2\n\ts_nop 0\n\tglobal_load_lds_dwordx4 %0, %1 sc1" ::"v"(voff), "s"(sbase), "s"(ldsaddr) : "memory");
}
DI void glds16(unsigned voff, const void* sbase, unsigned ldsaddr) {
  asm volatile("s_mov_b32 m0, %2\n\ts_nop 0\n\tglobal_load_lds_dwordx4 %0, %1" ::"v"(voff), "s"(sbase), "s"(ldsaddr) : "memory");
}
DI const char* uniform_ptr(const char* p) {
  const unsigned long long a = (unsigned long long)p;
  unsigned lo = __builtin_amdgcn_readfirstlane((unsigned)a), hi = __builtin_amdgcn_readfirstlane((unsigned)(a >> 32));
  asm volatile("s_nop 4" : "+s"(lo), "+s"(hi));
  return (const char*)(((unsigned long long)hi << 32) | lo);
}
DI LAS char* opaque(LAS char* p) { asm volatile("" : "+v"(p)); return p; }
DI bf16x8 lds_b128(LAS char* p) { return *(LAS bf16x8*)p; }
DI bf16x4 lds_tr(LAS char* p) { return __builtin_amdgcn_ds_read_tr16_b64_v4bf16((LAS bf16x4*)p); }
DI bf16x8 cat8(bf16x4 a, bf16x4 b) { return __builtin_shufflevector(a, b, 0, 1, 2, 3, 4, 5, 6, 7); }
DI bf16x8 pack8(f32x4 a, f32x4 b) {
  bf16x8 v;
  v[0] = (__bf16)a[0]; v[1] = (__bf16)a[1]; v[2] = (__bf16)a[2]; v[3] = (__bf16)a[3];
  v[4] = (__bf16)b[0]; v[5] = (__bf16)b[1]; v[6] = (__bf16)b[2]; v[7] = (__bf16)b[3];
  return v;
}

typedef __attribute__((address_space(1))) unsigned gu32;
DI void st_sc1(void* p, u32x4 v) { asm volatile("global_store_dwordx4 %0, %1, off sc1\n\ts_nop 1" ::"v"(p), "v"(v) : "memory"); }
DI unsigned umax4(u32x4 v, unsigned m) { return max(max(max(v[0], v[1]), v[2]), max(v[3], m)); }
DI void st8_sc1(void* p, u32x2 v) { asm volatile("global_store_dwordx2 %0, %1, off sc1\n\ts_nop 1" ::"v"(p), "v"(v) : "memory"); }
DI bool fresh4(u32x4 v) { return umax4(v, 0u) != 0xffffffffu; }
#define XRED4(OP, a1, b1, a2, b2) asm volatile( \
    "s_nop 1\n\tv_permlane16_swap_b32 %0, %1\n\tv_permlane16_swap_b32 %2, %3\n\t" \
    OP " %0, %0, %1\n\t" OP " %2, %2, %3\n\tv_mov_b32 %1, %0\n\tv_mov_b32 %3, %2\n\ts_nop 0\n\t" \
    "v_permlane32_swap_b32 %0, %1\n\tv_permlane32_swap_b32 %2, %3\n\t" \
    OP " %0, %0, %1\n\t" OP " %2, %2, %3\n\tv_mov_b32 %1, %0\n\tv_mov_b32 %3, %2\n\ts_nop 0\n\t" \
    "v_permlane16_swap_b32 %0, %1\n\tv_permlane16_swap_b32 %2, %3" \
    : "+v"(a1), "+v"(b1), "+v"(a2), "+v"(b2))
DI float max8(const float* v) {
  const float t1 = fmaxf(fmaxf(v[0], v[1]), v[2]), t2 = fmaxf(fmaxf(v[3], v[4]), v[5]), t3 = fmaxf(fmaxf(v[6], v[7]), t1);
  return fmaxf(t2, t3);
}
DI float min8(const float* v) {
  const float t1 = fminf(fminf(v[0], v[1]), v[2]), t2 = fminf(fminf(v[3], v[4]), v[5]), t3 = fminf(fminf(v[6], v[7]), t1);
  return fminf(t2, t3);
}
DI void dual_softmax2(const f32x4 (&p)[2][2], u32x4 (&pos)[2], u32x4 (&neg)[2]) {
  float v[2][8];
#pragma unroll
  for (int t = 0; t < 2; ++t)
#pragma unroll
    for (int h = 0; h < 2; ++h)
#pragma unroll
      for (int r = 0; r < 2; ++r) {
        unsigned u = pk2(p[t][h][2 * r], p[t][h][2 * r + 1]);
        asm("" : "+v"(u));
        v[t][4 * h + 2 * r] = blo(u); v[t][4 * h + 2 * r + 1] = bhi(u);
      }
  float mx0 = max8(v[0]), nm0 = -min8(v[0]), mx1 = max8(v[1]), nm1 = -min8(v[1]);
  XRED4("v_max_f32", mx0, nm0, mx1, nm1);
  constexpr float L2E = 1.44269504f;
  const f32x2 kp = {L2E, L2E}, kn = {-L2E, -L2E};
  f32x2 ep[2][4], en[2][4];
  float sp[2], sn[2];
#pragma unroll
  for (int t = 0; t < 2; ++t) {
    const float cpv = -(t ? mx1 : mx0) * L2E, cnv = -(t ? nm1 : nm0) * L2E;
    const f32x2 cp = {cpv, cpv}, cn = {cnv, cnv};
#pragma unroll
    for (int r = 0; r < 4; ++r) {
      const f32x2 x = {v[t][2 * r], v[t][2 * r + 1]};
      const f32x2 ap = __builtin_elementwise_fma(x, kp, cp), an = __builtin_elementwise_fma(x, kn, cn);
      ep[t][r] = (f32x2){__builtin_amdgcn_exp2f(ap[0]), __builtin_amdgcn_exp2f(ap[1])};
      en[t][r] = (f32x2){__builtin_amdgcn_exp2f(an[0]), __builtin_amdgcn_exp2f(an[1])};
    }
    const f32x2 s2p = (ep[t][0] + ep[t][1]) + (ep[t][2] + ep[t][3]), s2n = (en[t][0] + en[t][1]) + (en[t][2] + en[t][3]);
    sp[t] = s2p[0] + s2p[1]; sn[t] = s2n[0] + s2n[1];
  }
  XRED4("v_add_f32", sp[0], sn[0], sp[1], sn[1]);
#pragma unroll
  for (int t = 0; t < 2; ++t) {
    const float rp = __builtin_amdgcn_rcpf(sp[t]), rn = __builtin_amdgcn_rcpf(sn[t]);
    const f32x2 rp2 = {rp, rp}, rn2 = {rn, rn};
#pragma unroll
    for (int r = 0; r < 4; ++r) {
      const f32x2 a = ep[t][r] * rp2, b = en[t][r] * rn2;
      pos[t][r] = pk2(a[0], a[1]); neg[t][r] = pk2(b[0], b[1]);
    }
  }
}

__global__ __launch_bounds__(512) void k_ln(const u16* __restrict__ x, u16* __restrict__ xn, char* __restrict__ kvx, unsigned* __restrict__ cnt) {
  __shared__ f32x2 st[8][8];
  {
    if (blockIdx.x == 255 && threadIdx.x < 16) cnt[threadIdx.x * 32] = 0u;
    const unsigned e = blockIdx.x * 512 + threadIdx.x;
    const u32x4 fill = {~0u, ~0u, ~0u, ~0u};
    if (e < KVX_BYTES / 16) *(u32x4*)(kvx + (size_t)e * 16) = fill;
    char* xp = kvx + KVX_BYTES + (size_t)e * 16;
    *(u32x4*)xp = fill; *(u32x4*)(xp + 2097152) = fill;
  }
  const int lane = threadIdx.x & 63, wid = threadIdx.x >> 6;
  const int r8 = lane >> 3, c8 = lane & 7;
  const int row = blockIdx.x * 8 + r8;
  const u16* src = x + (size_t)row * D_ + wid * 128 + c8 * 8;
  const u32x4 v0 = *(const u32x4*)src, v1 = *(const u32x4*)(src + 64);
  float f[16];
#pragma unroll
  for (int i = 0; i < 4; ++i) { f[2 * i] = blo(v0[i]); f[2 * i + 1] = bhi(v0[i]); f[8 + 2 * i] = blo(v1[i]); f[8 + 2 * i + 1] = bhi(v1[i]); }
  float s = 0.f;
#pragma unroll
  for (int i = 0; i < 16; ++i) s += f[i];
  s += __shfl_xor(s, 1); s += __shfl_xor(s, 2); s += __shfl_xor(s, 4);
  const float mw = s * (1.0f / 128.f);
  float q = 0.f;
#pragma unroll
  for (int i = 0; i < 16; ++i) { const float d = f[i] - mw; q += d * d; }
  q += __shfl_xor(q, 1); q += __shfl_xor(q, 2); q += __shfl_xor(q, 4);
  if (c8 == 0) st[wid][r8] = (f32x2){mw, q};
  __syncthreads();
  float mean = 0.f, m2 = 0.f;
  f32x2 p[8];
#pragma unroll
  for (int w = 0; w < 8; ++w) { p[w] = st[w][r8]; mean += p[w][0]; m2 += p[w][1]; }
  mean *= 0.125f;
#pragma unroll
  for (int w = 0; w < 8; ++w) { const float d = p[w][0] - mean; m2 += 128.f * d * d; }
  const float mb = rbf(mean), vb = rbf(m2 * (1.0f / D_));
  const float sd = rbf(sqrtf(rbf(vb + 1e-5f)));
  u32x4 o0, o1;
#pragma unroll
  for (int i = 0; i < 4; ++i) {
    o0[i] = pk2(rbf(f[2 * i] - mb) / sd, rbf(f[2 * i + 1] - mb) / sd);
    o1[i] = pk2(rbf(f[8 + 2 * i] - mb) / sd, rbf(f[8 + 2 * i + 1] - mb) / sd);
  }
  const int rt = row >> 4, i16 = row & 15;
  const int kgA = wid * 4 + (c8 >> 2), g = c8 & 3;
  u16* dst = xn + ((size_t)(rt * NKG1 + kgA) * 64 + g * 16 + i16) * 8;
  *(u32x4*)dst = o0;
  *(u32x4*)(dst + 2 * 512) = o1;
}

constexpr int G1_SLOT = 32768;
constexpr int G1_LDS = 4 * G1_SLOT + 4096 + 2048;

DI float gelu_tanh(float x) {
  const float t = x * x;
  const float a = x * __builtin_fmaf(t, -0.10294324f, -2.3022082f);
  const float e = __builtin_amdgcn_exp2f(a);
  return x * __builtin_amdgcn_rcpf(1.0f + e);
}

DI f32x2 gelu2(f32x2 x) {
  const f32x2 c1 = {-0.10294324f, -0.10294324f}, c0 = {-2.3022082f, -2.3022082f}, one = {1.0f, 1.0f};
  const f32x2 a = x * __builtin_elementwise_fma(x * x, c1, c0);
  const f32x2 d = one + (f32x2){__builtin_amdgcn_exp2f(a[0]), __builtin_amdgcn_exp2f(a[1])};
  return x * (f32x2){__builtin_amdgcn_rcpf(d[0]), __builtin_amdgcn_rcpf(d[1])};
}

template <bool QKV>
DI void gemm1_body(const u16* __restrict__ xn, const u16* __restrict__ W, int wcol0, int woff, int panel, int tcol,
                   const u16* __restrict__ b1, const u16* __restrict__ wphi,
                   u16* __restrict__ act, char* __restrict__ kvx, char* __restrict__ xch,
                   const u16* __restrict__ w2, const u16* __restrict__ wo, LAS char* lds) {
  constexpr int LDW = QKV ? 3 * D_ : F_;
  constexpr int NG = QKV ? 3 : 4;
  constexpr int NT = QKV ? 6 : 7;
  const int tid = threadIdx.x, lane = tid & 63;
  const int wid = __builtin_amdgcn_readfirstlane(tid >> 6);
  const int wr = wid >> 1, wc = wid & 1;
  const int g = lane >> 4, i16 = lane & 15, q4 = i16 >> 2, p4 = lane & 3;
  const unsigned ldsb = (unsigned)(size_t)lds;

  STAMP(0, 0);
  const char* abase = (const char*)xn + (size_t)panel * 16 * NKG1 * 1024;
  const unsigned avoff = (unsigned)(wid * 2) * NKG1 * 1024 + lane * 16;
  const char* bbase = (const char*)W;
  unsigned bvoff[2]; unsigned bdst[2];
#pragma unroll
  for (int j = 0; j < 2; ++j) {
    int pb = QKV ? (wid + 8 * j) : (wid * 2 + j);
    if (QKV && pb >= 12) pb -= 8;
    const int kg = pb / NG, cg = pb % NG;
    const int r = lane >> 3, sw = ((r >> 1) & 1) | ((kg & 1) << 1);
    const int cc = (lane & 7) ^ (sw << 1);
    const int col = QKV ? (cg * D_ + wcol0) : (wcol0 + cg * 64);
    bvoff[j] = (unsigned)(((kg * 8 + r) * LDW + col + cc * 8) * 2);
    bdst[j] = ldsb + 16384 + pb * 1024;
  }
  const unsigned adst = ldsb + wid * 2048;

  LAS char* ard[2];
  ard[0] = opaque(lds + wr * 4096 + lane * 16); ard[1] = opaque(ard[0] + 65536);
  constexpr int NB = QKV ? 2 : 4;
  LAS char* brd[2][NB];
#pragma unroll
  for (int j = 0; j < NB; ++j) {
    const int n0 = QKV ? (wc * 32 + j * 16) : (woff + wc * 112 + j * 16);
    const int sw = (q4 >> 1) | ((g & 1) << 1);
    brd[0][j] = opaque(lds + 16384 + (g * NG + (n0 >> 6)) * 1024 + q4 * 128 + (((((n0 & 63) >> 3) ^ (sw << 1)) + (p4 >> 1)) << 4) + (p4 & 1) * 8);
    brd[1][j] = opaque(brd[0][j] + 65536);
  }

  if (QKV) {
    if (tid < 256) {
      const u32x4 w = *(const u32x4*)(wphi + (size_t)tcol * 2 * 1024 + tid * 8);
      *(LAS u32x4*)(lds + 4 * G1_SLOT + tid * 16) = w;
    }
  }

  f32x4 acc[4][NT];
#pragma unroll
  for (int m = 0; m < 4; ++m)
#pragma unroll
    for (int n = 0; n < NT; ++n) acc[m][n] = (f32x4){0.f, 0.f, 0.f, 0.f};

  bf16x8 fa[2][4], fb[2][NT];

  auto issue = [&](int q, int stage, int slot) {
    if (q < 2) glds16(avoff + q * NKG1 * 1024, abase + (size_t)stage * 1024, adst + q * 1024 + slot * G1_SLOT);
    else       glds16(bvoff[q - 2], bbase + (size_t)stage * 64 * LDW, bdst[q - 2] + slot * G1_SLOT);
  };
  auto rdA = [&](auto P, auto SL, int m) { fa[P.v][m] = lds_b128(ard[SL.v >> 1] + (SL.v & 1) * G1_SLOT + m * 1024); };
  auto rdB = [&](auto P, auto SL, int n) { LAS char* p = brd[SL.v >> 1][n % NB] + (n / NB) * 1024 + (SL.v & 1) * G1_SLOT; fb[P.v][n] = cat8(lds_tr(p), lds_tr(p + 512)); };
  auto mma4 = [&](auto P, int n) {
#pragma unroll
    for (int m = 0; m < 4; ++m) {
      if (QKV && n >= 4) acc[m][n] = MFMA16(fa[P.v][m], fb[P.v][n], acc[m][n]);
      else               acc[m][n] = MFMA16(fb[P.v][n], fa[P.v][m], acc[m][n]);
    }
  };
  auto step = [&](auto SC, auto VM, auto GL, auto RD, int s) {
    constexpr int P = SC.v & 1, SNv = (SC.v + 1) & 3;
    const IC<P> Pc; const IC<P ^ 1> Pn; const IC<SNv> SN;
    if constexpr (RD.v) { WAIT_V(VM.v); WAIT_L(0); BAR(); }
    SCHED();
    if constexpr (GL.v) issue(0, s + 4, SC.v);
    if constexpr (RD.v) { rdA(Pn, SN, 0); rdA(Pn, SN, 1); rdB(Pn, SN, 0); }
    mma4(Pc, 0); SCHED();
    if constexpr (GL.v) issue(1, s + 4, SC.v);
    if constexpr (RD.v) { rdA(Pn, SN, 2); rdA(Pn, SN, 3); rdB(Pn, SN, 1); }
    mma4(Pc, 1); SCHED();
    if constexpr (GL.v) issue(2, s + 4, SC.v);
    if constexpr (RD.v) { rdB(Pn, SN, 2); rdB(Pn, SN, 3); }
    mma4(Pc, 2); SCHED();
    if constexpr (GL.v) issue(3, s + 4, SC.v);
    if constexpr (RD.v) { rdB(Pn, SN, 4); rdB(Pn, SN, 5); }
    mma4(Pc, 3); SCHED();
    if constexpr (RD.v && NT == 7) rdB(Pn, SN, 6);
    mma4(Pc, 4); SCHED();
    mma4(Pc, 5);
    if constexpr (NT == 7) mma4(Pc, 6);
    SCHED();
  };

#pragma unroll
  for (int st = 0; st < 4; ++st)
#pragma unroll
    for (int q = 0; q < 4; ++q) issue(q, st, st);
  WAIT_V(12); WAIT_L(0); BAR();
  STAMP(0, 1);
  {
    const IC<0> P0; const IC<0> S0;
#pragma unroll
    for (int m = 0; m < 4; ++m) rdA(P0, S0, m);
#pragma unroll
    for (int n = 0; n < NT; ++n) rdB(P0, S0, n);
  }
#pragma unroll 1
  for (int s = 0; s < 28; s += 4) {
    step(IC<0>{}, IC<8>{}, IC<1>{}, IC<1>{}, s);
    step(IC<1>{}, IC<8>{}, IC<1>{}, IC<1>{}, s + 1);
    step(IC<2>{}, IC<8>{}, IC<1>{}, IC<1>{}, s + 2);
    step(IC<3>{}, IC<8>{}, IC<1>{}, IC<1>{}, s + 3);
  }
  step(IC<0>{}, IC<8>{}, IC<0>{}, IC<1>{}, 28);
  constexpr int NX = 1;
  {
    int wv = blockIdx.x * 8 + wid;
    if (wv >= 1152) wv -= 1152;
    const char* src = uniform_ptr(wv < 896 ? (const char*)w2 + (size_t)wv * 8192 : (const char*)wo + (size_t)(wv - 896) * 8192);
    asm volatile("s_mov_b32 m0, %2\n\ts_nop 0\n\tglobal_load_lds_dword %0, %1" ::"v"(lane * 128), "s"(src), "s"(ldsb + 4 * G1_SLOT + 4096 + wid * 256) : "memory");
  }
  step(IC<1>{}, IC<4 + NX>{}, IC<0>{}, IC<1>{}, 29);
  step(IC<2>{}, IC<0 + NX>{}, IC<0>{}, IC<1>{}, 30);
  step(IC<3>{}, IC<0>{}, IC<0>{}, IC<0>{}, 31);
  STAMP(0, 2);

  if (!QKV) {
    const int ncol0 = tcol * 224 + wc * 112 + 4 * g;
    u32x2 bb[NT];
#pragma unroll
    for (int n = 0; n < NT; ++n) bb[n] = *(const u32x2*)(b1 + ncol0 + n * 16);
    auto act4 = [&](int m, int n) {
      const f32x2 yA = gelu2((f32x2){acc[m][n][0] + blo(bb[n][0]), acc[m][n][1] + bhi(bb[n][0])});
      const f32x2 yB = gelu2((f32x2){acc[m][n][2] + blo(bb[n][1]), acc[m][n][3] + bhi(bb[n][1])});
      return (u32x2){pk2(yA[0], yA[1]), pk2(yB[0], yB[1])};
    };
    const int rt0 = panel * 16 + wr * 4;
    char* obase = (char*)act + ((size_t)rt0 * NKG2 + tcol * 7) * 1024 + lane * 16;
    if (wc == 0) {
#pragma unroll
      for (int m = 0; m < 4; ++m) {
        char* rb = obase + (size_t)m * NKG2 * 1024;
#pragma unroll
        for (int p = 0; p < 3; ++p) { const u32x2 lo = act4(m, 2 * p), hi = act4(m, 2 * p + 1); st_sc1(rb + p * 1024, (u32x4){lo[0], lo[1], hi[0], hi[1]}); }
        st8_sc1(rb + 3 * 1024, act4(m, 6 % NT));
      }
    } else {
#pragma unroll
      for (int m = 0; m < 4; ++m) {
        char* rb = obase + (size_t)m * NKG2 * 1024;
        st8_sc1(rb + 3 * 1024 + 8, act4(m, 0));
#pragma unroll
        for (int p = 0; p < 3; ++p) { const u32x2 lo = act4(m, 2 * p + 1), hi = act4(m, (2 * p + 2) % NT); st_sc1(rb + (4 + p) * 1024, (u32x4){lo[0], lo[1], hi[0], hi[1]}); }
      }
    }
  } else {
    const int head = tcol * 2 + wc;
    LAS char* wp = lds + 4 * G1_SLOT + wc * 2048;
    bf16x8 wf[2];
#pragma unroll
    for (int ft = 0; ft < 2; ++ft) {
      LAS char* p = wp + (4 * g + q4) * 64 + (16 * ft + 4 * p4) * 2;
      wf[ft] = cat8(lds_tr(p), lds_tr(p + 16 * 64));
    }
    const f32x4 z4 = {0.f, 0.f, 0.f, 0.f};
    LAS char* sc = lds + wid * 8704;
#pragma unroll
    for (int mp = 0; mp < 2; ++mp) {
      f32x4 pp[2][2]; u32x4 pos[2], neg[2];
#pragma unroll
      for (int t = 0; t < 2; ++t) {
        const bf16x8 bk = pack8(acc[2 * mp + t][2], acc[2 * mp + t][3]);
        pp[t][0] = MFMA16(wf[0], bk, z4); pp[t][1] = MFMA16(wf[1], bk, z4);
      }
      dual_softmax2(pp, pos, neg);
#pragma unroll
      for (int t = 0; t < 2; ++t) {
        LAS char* rowp = sc + (16 * (2 * mp + t) + i16) * 136 + 8 * g;
        *(LAS u32x2*)(rowp) = (u32x2){pos[t][0], pos[t][1]};      *(LAS u32x2*)(rowp + 32) = (u32x2){pos[t][2], pos[t][3]};
        *(LAS u32x2*)(rowp + 64) = (u32x2){neg[t][0], neg[t][1]}; *(LAS u32x2*)(rowp + 96) = (u32x2){neg[t][2], neg[t][3]};
      }
    }
    WAIT_L(0);
    STAMP(0, 5);
    f32x4 kv[4][3];
#pragma unroll
    for (int ft = 0; ft < 4; ++ft)
#pragma unroll
      for (int d = 0; d < 3; ++d) kv[ft][d] = z4;
    bf16x8 ones;
#pragma unroll
    for (int j = 0; j < 8; ++j) ones[j] = (__bf16)1.0f;
#pragma unroll
    for (int ks = 0; ks < 2; ++ks) {
      const bf16x8 bv0 = pack8(acc[2 * ks][4], acc[2 * ks + 1][4]);
      const bf16x8 bv1 = pack8(acc[2 * ks][5], acc[2 * ks + 1][5]);
#pragma unroll
      for (int ft = 0; ft < 4; ++ft) {
        LAS char* p = sc + (32 * ks + 4 * g + q4) * 136 + (16 * ft + 4 * p4) * 2;
        const bf16x8 a = cat8(lds_tr(p), lds_tr(p + 16 * 136));
        kv[ft][0] = MFMA16(a, bv0, kv[ft][0]);
        kv[ft][1] = MFMA16(a, bv1, kv[ft][1]);
        kv[ft][2] = MFMA16(a, ones, kv[ft][2]);
      }
    }
    STAMP(0, 6);
    WAIT_L(0); BAR();
    STAMP(0, 7);
    {
      LAS char* pp = lds + wid * 8448;
#pragma unroll
      for (int sg = 0; sg < 2; ++sg) {
#pragma unroll
        for (int d = 0; d < 2; ++d) {
          *(LAS f32x4*)(pp + (sg * 2 + d) * 2048 + lane * 16) = kv[2 * sg][d];
          *(LAS f32x4*)(pp + (sg * 2 + d) * 2048 + 1024 + lane * 16) = kv[2 * sg + 1][d];
        }
        if (i16 == 0) {
          *(LAS f32x4*)(pp + 8192 + sg * 128 + g * 16) = kv[2 * sg][2];
          *(LAS f32x4*)(pp + 8192 + sg * 128 + 64 + g * 16) = kv[2 * sg + 1][2];
        }
      }
    }
    WAIT_L(0); BAR();
    char* kvt = kvx + (size_t)tcol * KVX_TILE;
    char* kvmine = kvt + (size_t)panel * 16 * KVX_TILE;
    {
      LAS char* pp = lds + (tid >> 8) * 8448 + ((tid >> 6) & 3) * 2048 + lane * 16;
      f32x4 lo = *(LAS f32x4*)(pp), hi = *(LAS f32x4*)(pp + 1024);
#pragma unroll
      for (int w = 1; w < 4; ++w) { lo += *(LAS f32x4*)(pp + w * 16896); hi += *(LAS f32x4*)(pp + w * 16896 + 1024); }
      st_sc1(kvmine + tid * 16, (u32x4){pk2(lo[0], lo[1]), pk2(lo[2], lo[3]), pk2(hi[0], hi[1]), pk2(hi[2], hi[3])});
    }
    if (tid < 16) {
      LAS char* pp = lds + (tid >> 3) * 8448 + 8192 + ((tid >> 2) & 1) * 128 + (tid & 3) * 16;
      f32x4 lo = *(LAS f32x4*)(pp), hi = *(LAS f32x4*)(pp + 64);
#pragma unroll
      for (int w = 1; w < 4; ++w) { lo += *(LAS f32x4*)(pp + w * 16896); hi += *(LAS f32x4*)(pp + w * 16896 + 64); }
      st_sc1(kvmine + 8192 + tid * 16, (u32x4){pk2(lo[0], lo[1]), pk2(lo[2], lo[3]), pk2(hi[0], hi[1]), pk2(hi[2], hi[3])});
    }
    STAMP(0, 8);
    u32x4 qp[2][2], qn[2][2];
    auto qsm = [&](int mp) {
      f32x4 pp[2][2];
#pragma unroll
      for (int t = 0; t < 2; ++t) {
        const bf16x8 bq = pack8(acc[2 * mp + t][0], acc[2 * mp + t][1]);
        pp[t][0] = MFMA16(wf[0], bq, z4); pp[t][1] = MFMA16(wf[1], bq, z4);
      }
      dual_softmax2(pp, qp[mp], qn[mp]);
    };
    qsm(0);
    const char* kb0 = uniform_ptr(kvt), * kb1 = uniform_ptr(kvt + (size_t)32 * KVX_TILE);
    const char* kb2 = uniform_ptr(kvt + (size_t)64 * KVX_TILE), * kb3 = uniform_ptr(kvt + (size_t)96 * KVX_TILE);
    u32x4 pv[8], pz[8];
#define KV_GATHER(pv, OFF, TAIL) asm volatile( \
          "global_load_dwordx4 %0, %8, %10 sc1\n\tglobal_load_dwordx4 %1, %9, %10 sc1\n\t" \
          "global_load_dwordx4 %2, %8, %11 sc1\n\tglobal_load_dwordx4 %3, %9, %11 sc1\n\t" \
          "global_load_dwordx4 %4, %8, %12 sc1\n\tglobal_load_dwordx4 %5, %9, %12 sc1\n\t" \
          "global_load_dwordx4 %6, %8, %13 sc1\n\tglobal_load_dwordx4 %7, %9, %13 sc1" TAIL \
          : "=&v"(pv[0]), "=&v"(pv[1]), "=&v"(pv[2]), "=&v"(pv[3]), "=&v"(pv[4]), "=&v"(pv[5]), "=&v"(pv[6]), "=&v"(pv[7]) \
          : "v"(OFF), "v"((OFF) + 16 * KVX_TILE), "s"(kb0), "s"(kb1), "s"(kb2), "s"(kb3) : "memory")
#define KV_WAIT(pv) asm volatile("s_waitcnt vmcnt(0)" \
          : "+v"(pv[0]), "+v"(pv[1]), "+v"(pv[2]), "+v"(pv[3]), "+v"(pv[4]), "+v"(pv[5]), "+v"(pv[6]), "+v"(pv[7]) :: "memory")
    LAS char* kvs = lds + 3 * G1_SLOT;
    auto finish = [&](u32x4 (&pv)[8], unsigned off) {
      for (unsigned spins = 0;; ++spins) {
        unsigned m = 0;
#pragma unroll
        for (int p = 0; p < 8; ++p) m = umax4(pv[p], m);
        if (__all(m != 0xffffffffu) || spins > (1u << 14)) break;
        __builtin_amdgcn_s_sleep(2);
        KV_GATHER(pv, off, "\n\ts_waitcnt vmcnt(0)");
      }
      f32x2 t[4];
#pragma unroll
      for (int r = 0; r < 4; ++r) t[r] = (f32x2){blo(pv[0][r]), bhi(pv[0][r])};
#pragma unroll
      for (int p = 1; p < 8; ++p)
#pragma unroll
        for (int r = 0; r < 4; ++r) t[r] += (f32x2){blo(pv[p][r]), bhi(pv[p][r])};
      *(LAS u32x4*)(kvs + off) = (u32x4){pk2(t[0][0], t[0][1]), pk2(t[1][0], t[1][1]), pk2(t[2][0], t[2][1]), pk2(t[3][0], t[3][1])};
    };
    const unsigned go = (unsigned)tid * 16, gz = 8192u + (unsigned)i16 * 16;
    STAMP(0, 9);
    SCHED();
    KV_GATHER(pv, go, "");
    KV_GATHER(pz, gz, "");
    SCHED();
    qsm(1);
    STAMP(0, 10);
    SCHED();
    KV_WAIT(pv);
    KV_WAIT(pz);
    SCHED();
    finish(pv, go);
    if (wid == 0) finish(pz, gz);
    STAMP(0, 11);
    WAIT_L(0); BAR();
    bf16x8 a[2][3];
#pragma unroll
    for (int sg = 0; sg < 2; ++sg) {
#pragma unroll
      for (int d = 0; d < 2; ++d) a[sg][d] = lds_b128(kvs + wc * 4096 + (sg * 2 + d) * 1024 + lane * 16);
      a[sg][2] = lds_b128(kvs + 8192 + wc * 128 + sg * 64 + g * 16);
    }
#pragma unroll
    for (int m = 0; m < 4; ++m) {
      const bf16x8 bp = __builtin_bit_cast(bf16x8, qp[m >> 1][m & 1]), bn = __builtin_bit_cast(bf16x8, qn[m >> 1][m & 1]);
      f32x4 o0 = MFMA16(a[0][0], bp, z4), o1 = MFMA16(a[0][1], bp, z4), dn = MFMA16(a[0][2], bp, z4);
      o0 = MFMA16(a[1][0], bn, o0); o1 = MFMA16(a[1][1], bn, o1); dn = MFMA16(a[1][2], bn, dn);
      const float rd = __builtin_amdgcn_rcpf(dn[0] + 1e-6f);
      const int rt = panel * 16 + wr * 4 + m;
      u32x4 w;
      w[0] = pk2(o0[0] * rd, o0[1] * rd); w[1] = pk2(o0[2] * rd, o0[3] * rd);
      w[2] = pk2(o1[0] * rd, o1[1] * rd); w[3] = pk2(o1[2] * rd, o1[3] * rd);
      st_sc1((char*)act + ((size_t)rt * NKG2 + 112 + head) * 1024 + lane * 16, w);
    }
  }
  STAMP(0, 3);
}

constexpr int G2_SLOT = 32768;
constexpr int G2_POLL = 4 * G2_SLOT + 4096;
constexpr int KSPLIT = 36;

DI void gemm2_body(const char* __restrict__ act, const u16* __restrict__ w2, const u16* __restrict__ wo,
                   const u16* __restrict__ x, const u16* __restrict__ b2, char* __restrict__ xch, unsigned* cntp, u16* __restrict__ out,
                   int split, int panel, int pm, int pn, LAS char* lds) {
  const unsigned ldsb = (unsigned)(size_t)lds;
  int lane = __builtin_amdgcn_mbcnt_hi(~0u, __builtin_amdgcn_mbcnt_lo(~0u, 0u));
  asm volatile("" : "+v"(lane));
  const int wid = __builtin_amdgcn_readfirstlane(threadIdx.x >> 6);
  const int kh = wid >> 2, wr = (wid >> 1) & 1, wc = wid & 1;
  const int g = lane >> 4, i16 = lane & 15, q4 = i16 >> 2, p4 = lane & 3;
  const int b = blockIdx.x;
  gu32* cnt = (gu32*)cntp;
  const int kt0 = split ? KSPLIT : 0, nst = split ? 72 - KSPLIT : KSPLIT;
  const char* abase = uniform_ptr(act + ((size_t)(pm * 8) * NKG2 + kt0 * 2) * 1024);
  const unsigned avoff = (unsigned)wid * NKG2 * 1024 + lane * 16;
  const unsigned adst = ldsb + wid * 1024;
  const int r8 = lane >> 3;
  const unsigned bvoff = (unsigned)((wid * 8 + r8) * D_ + pn * 128 + (((lane & 7) ^ (((r8 >> 1) & 3) << 1)) << 3)) * 2;
  const unsigned bdst = ldsb + 16384 + wid * 2048;
  const char* wb = uniform_ptr((const char*)w2 + (size_t)kt0 * 64 * D_ * 2);
  auto issue = [&](int q, int t, int slot) {
    if (q < 2) glds16_sc1(avoff + q * 1024, abase + (size_t)t * 2048, adst + q * 8192 + slot * G2_SLOT);
    else       glds16(bvoff + (q - 2) * 128, wb + (size_t)t * 64 * D_ * 2, bdst + (q - 2) * 1024 + slot * G2_SLOT);
  };
  WAIT_V(0); WAIT_L(0); BAR();
  if (threadIdx.x == 0) __hip_atomic_fetch_add(cnt + (split * 8 + panel) * 32, 1u, __ATOMIC_RELAXED, __HIP_MEMORY_SCOPE_AGENT);
#pragma unroll
  for (int st = 0; st < 4; ++st) { issue(2, st, st); issue(3, st, st); }
  if (wid == 0) {
    unsigned spins = 0;
    while (__builtin_amdgcn_readfirstlane(__hip_atomic_load(cnt + panel * 32, __ATOMIC_RELAXED, __HIP_MEMORY_SCOPE_AGENT)) < 16u && ++spins < (1u << 16))
      __builtin_amdgcn_s_sleep(1);
  }
  BAR();
  STAMP(1, 0);
#pragma unroll
  for (int st = 0; st < 4; ++st) { issue(0, st, st); issue(1, st, st); }

  LAS char* ard[2];
  ard[0] = opaque(lds + kh * 8192 + wr * 4096 + lane * 16); ard[1] = opaque(ard[0] + 65536);
  LAS char* brd[2][4];
#pragma unroll
  for (int j = 0; j < 4; ++j) {
    const int r = 4 * (g & 1) + q4, sw = (r >> 1) & 3;
    brd[0][j] = opaque(lds + 16384 + ((kh * 4 + (g >> 1)) * 2 + wc) * 1024 + r * 128 + ((((2 * j) ^ (sw << 1)) + (p4 >> 1)) << 4) + (p4 & 1) * 8);
    brd[1][j] = opaque(brd[0][j] + 65536);
  }
  f32x4 acc[4][4];
#pragma unroll
  for (int m = 0; m < 4; ++m)
#pragma unroll
    for (int n = 0; n < 4; ++n) acc[m][n] = (f32x4){0.f, 0.f, 0.f, 0.f};
  bf16x8 fa[2][4], fb[2][4];
  auto rdA = [&](auto P, auto SL, int m) { fa[P.v][m] = lds_b128(ard[SL.v >> 1] + (SL.v & 1) * G2_SLOT + m * 1024); };
  auto rdB = [&](auto P, auto SL, int n) { LAS char* p = brd[SL.v >> 1][n] + (SL.v & 1) * G2_SLOT; fb[P.v][n] = cat8(lds_tr(p), lds_tr(p + 4096)); };
  auto mma4 = [&](auto P, int n) {
#pragma unroll
    for (int m = 0; m < 4; ++m) acc[m][n] = MFMA16(fb[P.v][n], fa[P.v][m], acc[m][n]);
  };
  auto step = [&](auto SC, auto VM, auto GL, auto RD, int t) {
    constexpr int P = SC.v & 1, SNv = (SC.v + 1) & 3;
    const IC<P> Pc; const IC<P ^ 1> Pn; const IC<SNv> SN;
    if constexpr (RD.v) { WAIT_V(VM.v); WAIT_L(0); BAR(); }
    SCHED();
    if constexpr (GL.v) { issue(0, t + 4, SC.v); issue(1, t + 4, SC.v); }
    if constexpr (RD.v) { rdA(Pn, SN, 0); rdA(Pn, SN, 1); rdB(Pn, SN, 0); }
    mma4(Pc, 0); SCHED();
    if constexpr (GL.v) { issue(2, t + 4, SC.v); issue(3, t + 4, SC.v); }
    if constexpr (RD.v) { rdA(Pn, SN, 2); rdA(Pn, SN, 3); rdB(Pn, SN, 1); }
    mma4(Pc, 1); SCHED();
    if constexpr (RD.v) { rdB(Pn, SN, 2); rdB(Pn, SN, 3); }
    mma4(Pc, 2); SCHED();
    mma4(Pc, 3); SCHED();
  };
  WAIT_V(6); BAR();
  STAMP(1, 1);
  {
    const IC<0> P0; const IC<0> S0;
#pragma unroll
    for (int m = 0; m < 4; ++m) rdA(P0, S0, m);
#pragma unroll
    for (int n = 0; n < 4; ++n) rdB(P0, S0, n);
  }
  const IC<1> Y; const IC<0> N_; const IC<8> V8;
  step(IC<0>{}, IC<4>{}, Y, Y, 0); step(IC<1>{}, IC<6>{}, Y, Y, 1); step(IC<2>{}, V8, Y, Y, 2); step(IC<3>{}, V8, Y, Y, 3);
  const int tp = split ? 8 : -100;
#pragma unroll 1
  for (int t = 4; t < nst - 4; t += 4) {
    if (wid == 0) {
      if (t == tp) {
        asm volatile("s_mov_b32 m0, %2\n\ts_nop 0\n\tglobal_load_lds_dword %0, %1 sc1" ::"v"(0u), "s"(uniform_ptr((const char*)(cntp + (8 + panel) * 32))), "s"(ldsb + G2_POLL) : "memory");
      } else if (t == tp + 4) {
        if (__builtin_amdgcn_readfirstlane(*(LAS unsigned*)(lds + G2_POLL)) < 16u) {
          unsigned spins = 0;
          while (__builtin_amdgcn_readfirstlane(__hip_atomic_load(cnt + (8 + panel) * 32, __ATOMIC_RELAXED, __HIP_MEMORY_SCOPE_AGENT)) < 16u && ++spins < (1u << 16))
            __builtin_amdgcn_s_sleep(1);
        }
      }
    }
    if (t == 52 - KSPLIT && split == 1) wb = uniform_ptr((const char*)wo - (size_t)(56 - KSPLIT) * 64 * D_ * 2);
    step(IC<0>{}, V8, Y, Y, t); step(IC<1>{}, V8, Y, Y, t + 1); step(IC<2>{}, V8, Y, Y, t + 2); step(IC<3>{}, V8, Y, Y, t + 3);
  }
  step(IC<0>{}, V8, N_, Y, 0);
  step(IC<1>{}, IC<4>{}, N_, Y, 0);
  step(IC<2>{}, IC<0>{}, N_, Y, 0);
  step(IC<3>{}, IC<0>{}, N_, N_, 0);
  STAMP(1, 2);
  const int quad = wid & 3;
  {
    LAS char* pp = lds + (quad * 2 + kh) * 8192 + lane * 16;
#pragma unroll
    for (int mm = 0; mm < 2; ++mm)
#pragma unroll
      for (int n = 0; n < 4; ++n) *(LAS f32x4*)(pp + (mm * 4 + n) * 1024) = kh ? acc[mm][n] : acc[2 + mm][n];
  }
  const bool keep = (wr == split);
  const int row0 = pm * 128 + wr * 64 + kh * 32 + i16, col0 = pn * 128 + wc * 64 + 4 * g;
  u32x2 xv[2][4], bv[4];
  if (keep) {
#pragma unroll
    for (int n = 0; n < 4; ++n) bv[n] = *(const u32x2*)(b2 + col0 + n * 16);
#pragma unroll
    for (int mm = 0; mm < 2; ++mm)
#pragma unroll
      for (int n = 0; n < 4; ++n) xv[mm][n] = *(const u32x2*)(x + (size_t)(row0 + mm * 16) * D_ + col0 + n * 16);
  }
  WAIT_L(0); BAR();
  f32x4 sum[2][4];
  {
    LAS char* pp = lds + (quad * 2 + (kh ^ 1)) * 8192 + lane * 16;
#pragma unroll
    for (int mm = 0; mm < 2; ++mm)
#pragma unroll
      for (int n = 0; n < 4; ++n) sum[mm][n] = (kh ? acc[2 + mm][n] : acc[mm][n]) + *(LAS f32x4*)(pp + (mm * 4 + n) * 1024);
  }
  const int piece = kh * 2 + wc;
  if (!keep) {
    char* dst = xch + (size_t)b * 16384 + piece * 4096 + lane * 16;
#pragma unroll
    for (int mm = 0; mm < 2; ++mm)
#pragma unroll
      for (int h = 0; h < 2; ++h)
        st_sc1(dst + (mm * 2 + h) * 1024, (u32x4){pk2(sum[mm][2 * h][0], sum[mm][2 * h][1]), pk2(sum[mm][2 * h][2], sum[mm][2 * h][3]),
                                                  pk2(sum[mm][2 * h + 1][0], sum[mm][2 * h + 1][1]), pk2(sum[mm][2 * h + 1][2], sum[mm][2 * h + 1][3])});
  } else {
    const char* src = xch + (size_t)(b ^ 1) * 16384 + piece * 4096 + lane * 16;
    u32x4 pv[4];
    for (unsigned spins = 0;; ++spins) {
      asm volatile("global_load_dwordx4 %0, %1, off offset:3072 sc1\n\ts_waitcnt vmcnt(0)" : "=&v"(pv[3]) : "v"(src) : "memory");
      if (__all(fresh4(pv[3])) || spins > (1u << 14)) break;
      __builtin_amdgcn_s_sleep(1);
    }
    for (unsigned spins = 0;; ++spins) {
      asm volatile(
          "global_load_dwordx4 %0, %3, off sc1\n\tglobal_load_dwordx4 %1, %3, off offset:1024 sc1\n\t"
          "global_load_dwordx4 %2, %3, off offset:2048 sc1\n\ts_waitcnt vmcnt(0)"
          : "=&v"(pv[0]), "=&v"(pv[1]), "=&v"(pv[2]) : "v"(src) : "memory");
      if (__all(fresh4(pv[0]) && fresh4(pv[1]) && fresh4(pv[2])) || spins > (1u << 14)) break;
      __builtin_amdgcn_s_sleep(1);
    }
#pragma unroll
    for (int mm = 0; mm < 2; ++mm)
#pragma unroll
      for (int n = 0; n < 4; ++n) {
        const unsigned q0 = pv[mm * 2 + (n >> 1)][2 * (n & 1)], q1 = pv[mm * 2 + (n >> 1)][2 * (n & 1) + 1];
        const f32x4 v = sum[mm][n];
        u32x2 o;
        o[0] = pk2(v[0] + blo(q0) + blo(xv[mm][n][0]) + blo(bv[n][0]), v[1] + bhi(q0) + bhi(xv[mm][n][0]) + bhi(bv[n][0]));
        o[1] = pk2(v[2] + blo(q1) + blo(xv[mm][n][1]) + blo(bv[n][1]), v[3] + bhi(q1) + bhi(xv[mm][n][1]) + bhi(bv[n][1]));
        *(u32x2*)(out + (size_t)(row0 + mm * 16) * D_ + col0 + n * 16) = o;
      }
  }
  STAMP(1, 3);
  STAMP(1, 4);
}

__global__ __launch_bounds__(512, 2) void k_main(const u16* __restrict__ xn, const u16* __restrict__ wqkv, const u16* __restrict__ w1,
                                                 const u16* __restrict__ b1, const u16* __restrict__ wphi,
                                                 u16* __restrict__ act, char* __restrict__ kvx, char* __restrict__ xch, unsigned* cnt,
                                                 const u16* __restrict__ w2, const u16* __restrict__ wo,
                                                 const u16* __restrict__ x, const u16* __restrict__ b2, u16* __restrict__ out) {
  extern __shared__ __attribute__((aligned(16))) char smem[];
  LAS char* lds = (LAS char*)smem;
  const int b = blockIdx.x, grp = b & 7, idx = b >> 3;
  const int panel = idx & 7, kind = grp & 1, tcol = (grp >> 1) * 4 + (idx >> 3);
  if (kind == 0) {
    gemm1_body<true>(xn, wqkv, tcol * 64, 0, panel, tcol, b1, wphi, act, kvx, xch, w2, wo, lds);
  } else {
    const int c0 = tcol * 224, woff = c0 & 63;
    gemm1_body<false>(xn, w1, c0 - woff, woff, panel, tcol, b1, wphi, act, kvx, xch, w2, wo, lds);
  }
  gemm2_body((const char*)act, w2, wo, x, b2, xch, cnt, out,   kind ^ 1, panel, panel * 2 + ((grp >> 1) & 1), (grp >> 2) * 4 + (idx >> 3), lds);
}

extern "C" void kernel_launch(void* const* d_in, const int* in_sizes, int n_in, void* d_out, int out_size, void* d_ws, size_t ws_size,
                              hipStream_t stream) {
  const u16* x = (const u16*)d_in[0];
  const u16* wqkv = (const u16*)d_in[1];
  const u16* wphi = (const u16*)d_in[2];
  const u16* wo = (const u16*)d_in[3];
  const u16* w1 = (const u16*)d_in[5];
  const u16* b1 = (const u16*)d_in[6];
  const u16* w2 = (const u16*)d_in[7];
  const u16* b2 = (const u16*)d_in[8];
  char* ws = (char*)d_ws;
  u16* xn = (u16*)(ws + WS_XN);
  u16* act = (u16*)(ws + WS_ACT);
  char* kvx = ws + WS_KVX;
  char* xch = ws + WS_XCH;
  unsigned* cnt = (unsigned*)(ws + WS_CNT);
  (void)hipFuncSetAttribute((const void*)k_main, hipFuncAttributeMaxDynamicSharedMemorySize, G1_LDS);
  k_ln<<<S_ / 8, 512, 0, stream>>>(x, xn, kvx, cnt);
  k_main<<<256, 512, G1_LDS, stream>>>(xn, wqkv, w1, b1, wphi, act, kvx, xch, cnt, w2, wo, x, b2, (u16*)d_out);
}
```

```cpp
#include <hip/hip_runtime.h>
#include <hip/hip_bf16.h>
#include <stdint.h>

typedef __bf16 bf16x8 __attribute__((ext_vector_type(8)));
typedef __bf16 bf16x4 __attribute__((ext_vector_type(4)));
typedef __bf16 bf16x2 __attribute__((ext_vector_type(2)));
typedef float f32x4 __attribute__((ext_vector_type(4)));
typedef float f32x2 __attribute__((ext_vector_type(2)));
typedef unsigned u32x4 __attribute__((ext_vector_type(4)));
typedef unsigned u32x2 __attribute__((ext_vector_type(2)));
typedef unsigned short u16;
#define LAS __attribute__((address_space(3)))
#define DI __device__ __forceinline__

constexpr int S_ = 2048, D_ = 1024, F_ = 3584, H_ = 32;
constexpr int KCAT = F_ + D_;
constexpr int NKG1 = D_ / 32;
constexpr int NKG2 = KCAT / 32;

constexpr size_t WS_XN   = 0;
constexpr size_t WS_ACT  = WS_XN + (size_t)S_ * D_ * 2;
constexpr size_t WS_KVX  = WS_ACT + (size_t)S_ * KCAT * 2;
constexpr int KVX_TILE = 8192 + 256;
constexpr size_t KVX_BYTES = (size_t)8 * 16 * KVX_TILE;
constexpr size_t WS_XCH  = WS_KVX + KVX_BYTES;
constexpr size_t WS_FLG  = WS_XCH + (size_t)256 * 16384;

DI float rbf(float x) { return (float)(__bf16)x; }
DI unsigned pk2(float lo, float hi) { bf16x2 v; v[0] = (__bf16)lo; v[1] = (__bf16)hi; return __builtin_bit_cast(unsigned, v); }
DI float blo(unsigned u) { return __uint_as_float(u << 16); }
DI float bhi(unsigned u) { return __uint_as_float(u & 0xffff0000u); }

#define MFMA16(a, b, c) __builtin_amdgcn_mfma_f32_16x16x32_bf16((a), (b), (c), 0, 0, 0)
#define WAIT_V(n) asm volatile("s_waitcnt vmcnt(%0)" ::"n"(n) : "memory")
#define WAIT_L(n) do { static_assert((n) == 0, ""); asm volatile("" ::: "memory"); __builtin_amdgcn_s_waitcnt(0xC07F); asm volatile("" ::: "memory"); } while (0)
#define BAR() do { asm volatile("" ::: "memory"); __builtin_amdgcn_s_barrier(); asm volatile("" ::: "memory"); } while (0)
#define SCHED() __builtin_amdgcn_sched_barrier(0)
#define STAMP(K, I) do { } while (0)
template <int N> struct IC { static constexpr int v = N; };

DI void glds16_sc1(unsigned voff, const void* sbase, unsigned ldsaddr) {
  asm volatile("s_mov_b32 m0, %2\n\ts_nop 0\n\tglobal_load_lds_dwordx4 %0, %1 sc1" ::"v"(voff), "s"(sbase), "s"(ldsaddr) : "memory");
}
DI void glds16(unsigned voff, const void* sbase, unsigned ldsaddr) {
  asm volatile("s_mov_b32 m0, %2\n\ts_nop 0\n\tglobal_load_lds_dwordx4 %0, %1" ::"v"(voff), "s"(sbase), "s"(ldsaddr) : "memory");
}
DI const char* uniform_ptr(const char* p) {
  const unsigned long long a = (unsigned long long)p;
  unsigned lo = __builtin_amdgcn_readfirstlane((unsigned)a), hi = __builtin_amdgcn_readfirstlane((unsigned)(a >> 32));
  asm volatile("s_nop 4" : "+s"(lo), "+s"(hi));
  return (const char*)(((unsigned long long)hi << 32) | lo);
}
DI LAS char* opaque(LAS char* p) { asm volatile("" : "+v"(p)); return p; }
DI bf16x8 lds_b128(LAS char* p) { return *(LAS bf16x8*)p; }
DI bf16x4 lds_tr(LAS char* p) { return __builtin_amdgcn_ds_read_tr16_b64_v4bf16((LAS bf16x4*)p); }
DI bf16x8 cat8(bf16x4 a, bf16x4 b) { return __builtin_shufflevector(a, b, 0, 1, 2, 3, 4, 5, 6, 7); }
DI bf16x8 pack8(f32x4 a, f32x4 b) {
  bf16x8 v;
  v[0] = (__bf16)a[0]; v[1] = (__bf16)a[1]; v[2] = (__bf16)a[2]; v[3] = (__bf16)a[3];
  v[4] = (__bf16)b[0]; v[5] = (__bf16)b[1]; v[6] = (__bf16)b[2]; v[7] = (__bf16)b[3];
  return v;
}

typedef __attribute__((address_space(1))) unsigned gu32;
typedef unsigned long long u64;
typedef __attribute__((address_space(1))) u64 gu64;
DI void st_sc1(void* p, u32x4 v) { asm volatile("global_store_dwordx4 %0, %1, off sc1\n\ts_nop 1" ::"v"(p), "v"(v) : "memory"); }
DI unsigned umax4(u32x4 v, unsigned m) { return max(max(max(v[0], v[1]), v[2]), max(v[3], m)); }
DI void st8_sc1(void* p, u32x2 v) { asm volatile("global_store_dwordx2 %0, %1, off sc1\n\ts_nop 1" ::"v"(p), "v"(v) : "memory"); }
DI bool fresh4(u32x4 v) { return umax4(v, 0u) != 0xffffffffu; }
#define XRED4(OP, a1, b1, a2, b2) asm volatile( \
    "s_nop 1\n\tv_permlane16_swap_b32 %0, %1\n\tv_permlane16_swap_b32 %2, %3\n\t" \
    OP " %0, %0, %1\n\t" OP " %2, %2, %3\n\tv_mov_b32 %1, %0\n\tv_mov_b32 %3, %2\n\ts_nop 0\n\t" \
    "v_permlane32_swap_b32 %0, %1\n\tv_permlane32_swap_b32 %2, %3\n\t" \
    OP " %0, %0, %1\n\t" OP " %2, %2, %3\n\tv_mov_b32 %1, %0\n\tv_mov_b32 %3, %2\n\ts_nop 0\n\t" \
    "v_permlane16_swap_b32 %0, %1\n\tv_permlane16_swap_b32 %2, %3" \
    : "+v"(a1), "+v"(b1), "+v"(a2), "+v"(b2))
DI float max8(const float* v) {
  const float t1 = fmaxf(fmaxf(v[0], v[1]), v[2]), t2 = fmaxf(fmaxf(v[3], v[4]), v[5]), t3 = fmaxf(fmaxf(v[6], v[7]), t1);
  return fmaxf(t2, t3);
}
DI float min8(const float* v) {
  const float t1 = fminf(fminf(v[0], v[1]), v[2]), t2 = fminf(fminf(v[3], v[4]), v[5]), t3 = fminf(fminf(v[6], v[7]), t1);
  return fminf(t2, t3);
}
DI void dual_softmax2(const f32x4 (&p)[2][2], u32x4 (&pos)[2], u32x4 (&neg)[2]) {
  float v[2][8];
#pragma unroll
  for (int t = 0; t < 2; ++t)
#pragma unroll
    for (int h = 0; h < 2; ++h)
#pragma unroll
      for (int r = 0; r < 2; ++r) {
        unsigned u = pk2(p[t][h][2 * r], p[t][h][2 * r + 1]);
        asm("" : "+v"(u));
        v[t][4 * h + 2 * r] = blo(u); v[t][4 * h + 2 * r + 1] = bhi(u);
      }
  float mx0 = max8(v[0]), nm0 = -min8(v[0]), mx1 = max8(v[1]), nm1 = -min8(v[1]);
  XRED4("v_max_f32", mx0, nm0, mx1, nm1);
  constexpr float L2E = 1.44269504f;
  const f32x2 kp = {L2E, L2E}, kn = {-L2E, -L2E};
  f32x2 ep[2][4], en[2][4];
  float sp[2], sn[2];
#pragma unroll
  for (int t = 0; t < 2; ++t) {
    const float cpv = -(t ? mx1 : mx0) * L2E, cnv = -(t ? nm1 : nm0) * L2E;
    const f32x2 cp = {cpv, cpv}, cn = {cnv, cnv};
#pragma unroll
    for (int r = 0; r < 4; ++r) {
      const f32x2 x = {v[t][2 * r], v[t][2 * r + 1]};
      const f32x2 ap = __builtin_elementwise_fma(x, kp, cp), an = __builtin_elementwise_fma(x, kn, cn);
      ep[t][r] = (f32x2){__builtin_amdgcn_exp2f(ap[0]), __builtin_amdgcn_exp2f(ap[1])};
      en[t][r] = (f32x2){__builtin_amdgcn_exp2f(an[0]), __builtin_amdgcn_exp2f(an[1])};
    }
    const f32x2 s2p = (ep[t][0] + ep[t][1]) + (ep[t][2] + ep[t][3]), s2n = (en[t][0] + en[t][1]) + (en[t][2] + en[t][3]);
    sp[t] = s2p[0] + s2p[1]; sn[t] = s2n[0] + s2n[1];
  }
  XRED4("v_add_f32", sp[0], sn[0], sp[1], sn[1]);
#pragma unroll
  for (int t = 0; t < 2; ++t) {
    const float rp = __builtin_amdgcn_rcpf(sp[t]), rn = __builtin_amdgcn_rcpf(sn[t]);
    const f32x2 rp2 = {rp, rp}, rn2 = {rn, rn};
#pragma unroll
    for (int r = 0; r < 4; ++r) {
      const f32x2 a = ep[t][r] * rp2, b = en[t][r] * rn2;
      pos[t][r] = pk2(a[0], a[1]); neg[t][r] = pk2(b[0], b[1]);
    }
  }
}

DI float sum8lanes(float v) {
  v += __builtin_bit_cast(float, __builtin_amdgcn_update_dpp(0, __builtin_bit_cast(int, v), 0xB1, 0xf, 0xf, true));
  v += __builtin_bit_cast(float, __builtin_amdgcn_update_dpp(0, __builtin_bit_cast(int, v), 0x4E, 0xf, 0xf, true));
  v += __builtin_bit_cast(float, __builtin_amdgcn_update_dpp(0, __builtin_bit_cast(int, v), 0x141, 0xf, 0xf, true));
  return v;
}
DI void wait_flags(const char* p, u64 tag) {
  for (unsigned spins = 0; spins < (1u << 16); ++spins) {
    if (__all(__hip_atomic_load((const gu64*)p, __ATOMIC_RELAXED, __HIP_MEMORY_SCOPE_AGENT) == tag)) break;
    __builtin_amdgcn_s_sleep(1);
  }
}

constexpr int G1_SLOT = 16384;
constexpr int G1_B = 65536, G1_X = 131072;
constexpr int G1_LDS = G1_X + 4096 + 2048;

DI f32x2 gelu2(f32x2 x) {
  const f32x2 c1 = {-0.10294324f, -0.10294324f}, c0 = {-2.3022082f, -2.3022082f}, one = {1.0f, 1.0f};
  const f32x2 a = x * __builtin_elementwise_fma(x * x, c1, c0);
  const f32x2 d = one + (f32x2){__builtin_amdgcn_exp2f(a[0]), __builtin_amdgcn_exp2f(a[1])};
  return x * (f32x2){__builtin_amdgcn_rcpf(d[0]), __builtin_amdgcn_rcpf(d[1])};
}

template <bool QKV>
DI u64 gemm1_body(const u16* __restrict__ x, const u16* __restrict__ W, int wcol0, int woff, int panel, int tcol,
                  const u16* __restrict__ b1, const u16* __restrict__ wphi, char* ws,
                  const u16* __restrict__ w2, const u16* __restrict__ wo, LAS char* lds) {
  char* xn = ws + WS_XN;
  char* act = ws + WS_ACT;
  char* kvx = ws + WS_KVX;
  constexpr int LDW = QKV ? 3 * D_ : F_;
  constexpr int NG = QKV ? 3 : 4;
  constexpr int NT = QKV ? 6 : 7;
  const int tid = threadIdx.x, lane = tid & 63;
  const int wid = __builtin_amdgcn_readfirstlane(tid >> 6);
  const int wr = wid >> 1, wc = wid & 1;
  const int g = lane >> 4, i16 = lane & 15, q4 = i16 >> 2, p4 = lane & 3;
  const unsigned ldsb = (unsigned)(size_t)lds;

  STAMP(0, 0);
  const char* abase = uniform_ptr(xn + (size_t)panel * 16 * NKG1 * 1024);
  const unsigned avoff = (unsigned)(wid * 2) * NKG1 * 1024 + lane * 16;
  const char* bbase = (const char*)W;
  unsigned bvoff[2]; unsigned bdst[2];
#pragma unroll
  for (int j = 0; j < 2; ++j) {
    int pb = QKV ? (wid + 8 * j) : (wid * 2 + j);
    if (QKV && pb >= 12) pb -= 8;
    const int kg = pb / NG, cg = pb % NG;
    const int r = lane >> 3, sw = ((r >> 1) & 1) | ((kg & 1) << 1);
    const int cc = (lane & 7) ^ (sw << 1);
    const int col = QKV ? (cg * D_ + wcol0) : (wcol0 + cg * 64);
    bvoff[j] = (unsigned)(((kg * 8 + r) * LDW + col + cc * 8) * 2);
    bdst[j] = ldsb + G1_B + pb * 1024;
  }
  const unsigned adst = ldsb + wid * 2048;

  LAS char* ard = opaque(lds + wr * 4096 + lane * 16);
  constexpr int NB = QKV ? 2 : 4;
  LAS char* brd[NB];
#pragma unroll
  for (int j = 0; j < NB; ++j) {
    const int n0 = QKV ? (wc * 32 + j * 16) : (woff + wc * 112 + j * 16);
    const int sw = (q4 >> 1) | ((g & 1) << 1);
    brd[j] = opaque(lds + G1_B + (g * NG + (n0 >> 6)) * 1024 + q4 * 128 + (((((n0 & 63) >> 3) ^ (sw << 1)) + (p4 >> 1)) << 4) + (p4 & 1) * 8);
  }

  f32x4 acc[4][NT];
#pragma unroll
  for (int m = 0; m < 4; ++m)
#pragma unroll
    for (int n = 0; n < NT; ++n) acc[m][n] = (f32x4){0.f, 0.f, 0.f, 0.f};

  bf16x8 fa[2][4], fb[2][NT];

  auto issue = [&](int q, int stage, int slot) {
    if (q < 2) glds16_sc1(avoff + q * NKG1 * 1024, abase + (size_t)stage * 1024, adst + q * 1024 + slot * G1_SLOT);
    else       glds16(bvoff[q - 2], bbase + (size_t)stage * 64 * LDW, bdst[q - 2] + slot * G1_SLOT);
  };
  auto rdA = [&](auto P, auto SL, int m) { fa[P.v][m] = lds_b128(ard + SL.v * G1_SLOT + m * 1024); };
  auto rdB = [&](auto P, auto SL, int n) { LAS char* p = brd[n % NB] + (n / NB) * 1024 + SL.v * G1_SLOT; fb[P.v][n] = cat8(lds_tr(p), lds_tr(p + 512)); };
  auto mma4 = [&](auto P, int n) {
#pragma unroll
    for (int m = 0; m < 4; ++m) {
      if (QKV && n >= 4) acc[m][n] = MFMA16(fa[P.v][m], fb[P.v][n], acc[m][n]);
      else               acc[m][n] = MFMA16(fb[P.v][n], fa[P.v][m], acc[m][n]);
    }
  };
  auto step = [&](auto SC, auto VM, auto GL, auto RD, int s) {
    constexpr int P = SC.v & 1, SNv = (SC.v + 1) & 3;
    const IC<P> Pc; const IC<P ^ 1> Pn; const IC<SNv> SN;
    if constexpr (RD.v) { WAIT_V(VM.v); WAIT_L(0); BAR(); }
    SCHED();
    if constexpr (GL.v) issue(0, s + 4, SC.v);
    if constexpr (RD.v) { rdA(Pn, SN, 0); rdA(Pn, SN, 1); rdB(Pn, SN, 0); }
    mma4(Pc, 0); SCHED();
    if constexpr (GL.v) issue(1, s + 4, SC.v);
    if constexpr (RD.v) { rdA(Pn, SN, 2); rdA(Pn, SN, 3); rdB(Pn, SN, 1); }
    mma4(Pc, 1); SCHED();
    if constexpr (GL.v) issue(2, s + 4, SC.v);
    if constexpr (RD.v) { rdB(Pn, SN, 2); rdB(Pn, SN, 3); }
    mma4(Pc, 2); SCHED();
    if constexpr (GL.v) issue(3, s + 4, SC.v);
    if constexpr (RD.v) { rdB(Pn, SN, 4); rdB(Pn, SN, 5); }
    mma4(Pc, 3); SCHED();
    if constexpr (RD.v && NT == 7) rdB(Pn, SN, 6);
    mma4(Pc, 4); SCHED();
    mma4(Pc, 5);
    if constexpr (NT == 7) mma4(Pc, 6);
    SCHED();
  };

  const int slot = (QKV ? 0 : 16) + tcol;
  u64 tag;
  {
    const int r8 = lane >> 3, c8 = lane & 7;
    const int row = panel * 256 + slot * 8 + r8;
    const u16* src = x + (size_t)row * D_ + wid * 128 + c8 * 8;
    u32x4 v0, v1; u32x2 ep;
    asm volatile("global_load_dwordx4 %0, %3, off\n\tglobal_load_dwordx4 %1, %3, off offset:128\n\tglobal_load_dwordx2 %2, %4, off sc1"
                 : "=&v"(v0), "=&v"(v1), "=&v"(ep) : "v"(src), "v"(ws + WS_FLG + 4096) : "memory");
    SCHED();
#pragma unroll
    for (int st = 0; st < 4; ++st) { issue(2, st, st); issue(3, st, st); }
    if (QKV) {
      const u32x4 fill = {~0u, ~0u, ~0u, ~0u};
      char* kp = kvx + (size_t)(panel * 16 + tcol) * KVX_TILE;
      st_sc1(kp + tid * 16, fill);
      st_sc1(kp + 8192 + i16 * 16, fill);
    }
    SCHED();
    asm volatile("s_waitcnt vmcnt(%3)" : "+v"(v0), "+v"(v1), "+v"(ep) : "n"(QKV ? 10 : 8) : "memory");
    STAMP(0, 13);
    SCHED();
    tag = (((u64)(unsigned)__builtin_amdgcn_readfirstlane(ep[1]) << 32) | (unsigned)__builtin_amdgcn_readfirstlane(ep[0])) + 1;
    f32x2 f[8];
#pragma unroll
    for (int i = 0; i < 4; ++i) { f[i] = (f32x2){blo(v0[i]), bhi(v0[i])}; f[4 + i] = (f32x2){blo(v1[i]), bhi(v1[i])}; }
    const f32x2 s2 = ((f[0] + f[1]) + (f[2] + f[3])) + ((f[4] + f[5]) + (f[6] + f[7]));
    const float mw = sum8lanes(s2[0] + s2[1]) * (1.0f / 128.f);
    f32x2 q2 = {0.f, 0.f};
#pragma unroll
    for (int i = 0; i < 8; ++i) { const f32x2 d = f[i] - (f32x2){mw, mw}; q2 = __builtin_elementwise_fma(d, d, q2); }
    const float qq = sum8lanes(q2[0] + q2[1]);
    LAS f32x2* stt = (LAS f32x2*)(lds + G1_X + 4096);
    if (c8 == 0) stt[r8 * 8 + wid] = (f32x2){mw, qq};
    WAIT_L(0); BAR();
    const f32x2 pw = stt[r8 * 8 + c8];
    const float mean = sum8lanes(pw[0]) * 0.125f;
    const float dm = pw[0] - mean;
    const float m2 = sum8lanes(__builtin_fmaf(128.f * dm, dm, pw[1]));
    const float mb = rbf(mean), vb = rbf(m2 * (1.0f / D_));
    const float rs = 1.0f / rbf(sqrtf(rbf(vb + 1e-5f)));
    u32x4 o0, o1;
#pragma unroll
    for (int i = 0; i < 8; ++i) {
      const f32x2 d = f[i] - (f32x2){mb, mb};
      unsigned u = pk2(d[0], d[1]);
      asm("" : "+v"(u));
      const f32x2 y = (f32x2){blo(u), bhi(u)} * (f32x2){rs, rs};
      if (i < 4) o0[i] = pk2(y[0], y[1]); else o1[i - 4] = pk2(y[0], y[1]);
    }
    char* dst = xn + ((size_t)((row >> 4) * NKG1 + wid * 4 + (c8 >> 2)) * 64 + (c8 & 3) * 16 + (row & 15)) * 16;
    st_sc1(dst, o0); st_sc1(dst + 2048, o1);
    STAMP(0, 14);
    WAIT_V(0); BAR();
    STAMP(0, 15);
    if (tid == 0) __hip_atomic_store((gu64*)(ws + WS_FLG + (panel * 32 + slot) * 8), tag, __ATOMIC_RELAXED, __HIP_MEMORY_SCOPE_AGENT);
    if (QKV && wid >= 4) glds16((unsigned)lane * 16, uniform_ptr((const char*)wphi + (size_t)tcol * 4096 + (wid - 4) * 1024), ldsb + G1_X + (wid - 4) * 1024);
    if (wid == 0) wait_flags(ws + WS_FLG + ((QKV && lane >= 32) ? ((lane & 7) * 32 + slot) : (panel * 32 + (lane & 31))) * 8, tag);
    BAR();
  }
  STAMP(0, 12);
#pragma unroll
  for (int st = 0; st < 4; ++st) { issue(0, st, st); issue(1, st, st); }
  WAIT_V(2); BAR();
  STAMP(0, 1);
  {
    const IC<0> P0; const IC<0> S0;
#pragma unroll
    for (int m = 0; m < 4; ++m) rdA(P0, S0, m);
#pragma unroll
    for (int n = 0; n < NT; ++n) rdB(P0, S0, n);
  }
#pragma unroll 1
  for (int s = 0; s < 28; s += 4) {
    step(IC<0>{}, IC<8>{}, IC<1>{}, IC<1>{}, s);
    step(IC<1>{}, IC<8>{}, IC<1>{}, IC<1>{}, s + 1);
    step(IC<2>{}, IC<8>{}, IC<1>{}, IC<1>{}, s + 2);
    step(IC<3>{}, IC<8>{}, IC<1>{}, IC<1>{}, s + 3);
  }
  step(IC<0>{}, IC<8>{}, IC<0>{}, IC<1>{}, 28);
  constexpr int NX = 3;
  {
    int wv = blockIdx.x * 8 + wid;
    if (wv >= 1152) wv -= 1152;
    const char* src = uniform_ptr(wv < 896 ? (const char*)w2 + (size_t)wv * 8192 : (const char*)wo + (size_t)(wv - 896) * 8192);
    asm volatile("s_mov_b32 m0, %2\n\ts_nop 0\n\tglobal_load_lds_dword %0, %1" ::"v"(lane * 128), "s"(src), "s"(ldsb + G1_X + 4096 + wid * 256) : "memory");
    char* xp = ws + WS_XCH + (size_t)blockIdx.x * 16384 + tid * 16;
    const u32x4 fill = {~0u, ~0u, ~0u, ~0u};
    st_sc1(xp, fill); st_sc1(xp + 8192, fill);
  }
  step(IC<1>{}, IC<4 + NX>{}, IC<0>{}, IC<1>{}, 29);
  step(IC<2>{}, IC<0 + NX>{}, IC<0>{}, IC<1>{}, 30);
  step(IC<3>{}, IC<0>{}, IC<0>{}, IC<0>{}, 31);
  STAMP(0, 2);

  if (!QKV) {
    const int ncol0 = tcol * 224 + wc * 112 + 4 * g;
    u32x2 bb[NT];
#pragma unroll
    for (int n = 0; n < NT; ++n) bb[n] = *(const u32x2*)(b1 + ncol0 + n * 16);
    auto act4 = [&](int m, int n) {
      const f32x2 yA = gelu2((f32x2){acc[m][n][0] + blo(bb[n][0]), acc[m][n][1] + bhi(bb[n][0])});
      const f32x2 yB = gelu2((f32x2){acc[m][n][2] + blo(bb[n][1]), acc[m][n][3] + bhi(bb[n][1])});
      return (u32x2){pk2(yA[0], yA[1]), pk2(yB[0], yB[1])};
    };
    const int rt0 = panel * 16 + wr * 4;
    char* obase = (char*)act + ((size_t)rt0 * NKG2 + tcol * 7) * 1024 + lane * 16;
    if (wc == 0) {
#pragma unroll
      for (int m = 0; m < 4; ++m) {
        char* rb = obase + (size_t)m * NKG2 * 1024;
#pragma unroll
        for (int p = 0; p < 3; ++p) { const u32x2 lo = act4(m, 2 * p), hi = act4(m, 2 * p + 1); st_sc1(rb + p * 1024, (u32x4){lo[0], lo[1], hi[0], hi[1]}); }
        st8_sc1(rb + 3 * 1024, act4(m, 6 % NT));
      }
    } else {
#pragma unroll
      for (int m = 0; m < 4; ++m) {
        char* rb = obase + (size_t)m * NKG2 * 1024;
        st8_sc1(rb + 3 * 1024 + 8, act4(m, 0));
#pragma unroll
        for (int p = 0; p < 3; ++p) { const u32x2 lo = act4(m, 2 * p + 1), hi = act4(m, (2 * p + 2) % NT); st_sc1(rb + (4 + p) * 1024, (u32x4){lo[0], lo[1], hi[0], hi[1]}); }
      }
    }
  } else {
    const int head = tcol * 2 + wc;
    LAS char* wp = lds + G1_X + wc * 2048;
    bf16x8 wf[2];
#pragma unroll
    for (int ft = 0; ft < 2; ++ft) {
      LAS char* p = wp + (4 * g + q4) * 64 + (16 * ft + 4 * p4) * 2;
      wf[ft] = cat8(lds_tr(p), lds_tr(p + 16 * 64));
    }
    const f32x4 z4 = {0.f, 0.f, 0.f, 0.f};
    LAS char* sc = lds + (wid >> 2) * G1_B + (wid & 3) * 8704;
#pragma unroll
    for (int mp = 0; mp < 2; ++mp) {
      f32x4 pp[2][2]; u32x4 pos[2], neg[2];
#pragma unroll
      for (int t = 0; t < 2; ++t) {
        const bf16x8 bk = pack8(acc[2 * mp + t][2], acc[2 * mp + t][3]);
        pp[t][0] = MFMA16(wf[0], bk, z4); pp[t][1] = MFMA16(wf[1], bk, z4);
      }
      dual_softmax2(pp, pos, neg);
#pragma unroll
      for (int t = 0; t < 2; ++t) {
        LAS char* rowp = sc + (16 * (2 * mp + t) + i16) * 136 + 8 * g;
        *(LAS u32x2*)(rowp) = (u32x2){pos[t][0], pos[t][1]};      *(LAS u32x2*)(rowp + 32) = (u32x2){pos[t][2], pos[t][3]};
        *(LAS u32x2*)(rowp + 64) = (u32x2){neg[t][0], neg[t][1]}; *(LAS u32x2*)(rowp + 96) = (u32x2){neg[t][2], neg[t][3]};
      }
    }
    WAIT_L(0);
    STAMP(0, 5);
    f32x4 kv[4][3];
#pragma unroll
    for (int ft = 0; ft < 4; ++ft)
#pragma unroll
      for (int d = 0; d < 3; ++d) kv[ft][d] = z4;
    bf16x8 ones;
#pragma unroll
    for (int j = 0; j < 8; ++j) ones[j] = (__bf16)1.0f;
#pragma unroll
    for (int ks = 0; ks < 2; ++ks) {
      const bf16x8 bv0 = pack8(acc[2 * ks][4], acc[2 * ks + 1][4]);
      const bf16x8 bv1 = pack8(acc[2 * ks][5], acc[2 * ks + 1][5]);
#pragma unroll
      for (int ft = 0; ft < 4; ++ft) {
        LAS char* p = sc + (32 * ks + 4 * g + q4) * 136 + (16 * ft + 4 * p4) * 2;
        const bf16x8 a = cat8(lds_tr(p), lds_tr(p + 16 * 136));
        kv[ft][0] = MFMA16(a, bv0, kv[ft][0]);
        kv[ft][1] = MFMA16(a, bv1, kv[ft][1]);
        kv[ft][2] = MFMA16(a, ones, kv[ft][2]);
      }
    }
    STAMP(0, 6);
    WAIT_L(0); BAR();
    STAMP(0, 7);
    {
      LAS char* pp = lds + wid * 8448;
#pragma unroll
      for (int sg = 0; sg < 2; ++sg) {
#pragma unroll
        for (int d = 0; d < 2; ++d) {
          *(LAS f32x4*)(pp + (sg * 2 + d) * 2048 + lane * 16) = kv[2 * sg][d];
          *(LAS f32x4*)(pp + (sg * 2 + d) * 2048 + 1024 + lane * 16) = kv[2 * sg + 1][d];
        }
        if (i16 == 0) {
          *(LAS f32x4*)(pp + 8192 + sg * 128 + g * 16) = kv[2 * sg][2];
          *(LAS f32x4*)(pp + 8192 + sg * 128 + 64 + g * 16) = kv[2 * sg + 1][2];
        }
      }
    }
    WAIT_L(0); BAR();
    char* kvt = kvx + (size_t)tcol * KVX_TILE;
    char* kvmine = kvt + (size_t)panel * 16 * KVX_TILE;
    {
      LAS char* pp = lds + (tid >> 8) * 8448 + ((tid >> 6) & 3) * 2048 + lane * 16;
      f32x4 lo = *(LAS f32x4*)(pp), hi = *(LAS f32x4*)(pp + 1024);
#pragma unroll
      for (int w = 1; w < 4; ++w) { lo += *(LAS f32x4*)(pp + w * 16896); hi += *(LAS f32x4*)(pp + w * 16896 + 1024); }
      st_sc1(kvmine + tid * 16, (u32x4){pk2(lo[0], lo[1]), pk2(lo[2], lo[3]), pk2(hi[0], hi[1]), pk2(hi[2], hi[3])});
    }
    if (tid < 16) {
      LAS char* pp = lds + (tid >> 3) * 8448 + 8192 + ((tid >> 2) & 1) * 128 + (tid & 3) * 16;
      f32x4 lo = *(LAS f32x4*)(pp), hi = *(LAS f32x4*)(pp + 64);
#pragma unroll
      for (int w = 1; w < 4; ++w) { lo += *(LAS f32x4*)(pp + w * 16896); hi += *(LAS f32x4*)(pp + w * 16896 + 64); }
      st_sc1(kvmine + 8192 + tid * 16, (u32x4){pk2(lo[0], lo[1]), pk2(lo[2], lo[3]), pk2(hi[0], hi[1]), pk2(hi[2], hi[3])});
    }
    STAMP(0, 8);
    u32x4 qp[2][2], qn[2][2];
    auto qsm = [&](int mp) {
      f32x4 pp[2][2];
#pragma unroll
      for (int t = 0; t < 2; ++t) {
        const bf16x8 bq = pack8(acc[2 * mp + t][0], acc[2 * mp + t][1]);
        pp[t][0] = MFMA16(wf[0], bq, z4); pp[t][1] = MFMA16(wf[1], bq, z4);
      }
      dual_softmax2(pp, qp[mp], qn[mp]);
    };
    qsm(0);
    const char* kb0 = uniform_ptr(kvt), * kb1 = uniform_ptr(kvt + (size_t)32 * KVX_TILE);
    const char* kb2 = uniform_ptr(kvt + (size_t)64 * KVX_TILE), * kb3 = uniform_ptr(kvt + (size_t)96 * KVX_TILE);
    u32x4 pv[8], pz[8];
#define KV_GATHER(pv, OFF, TAIL) asm volatile( \
          "global_load_dwordx4 %0, %8, %10 sc1\n\tglobal_load_dwordx4 %1, %9, %10 sc1\n\t" \
          "global_load_dwordx4 %2, %8, %11 sc1\n\tglobal_load_dwordx4 %3, %9, %11 sc1\n\t" \
          "global_load_dwordx4 %4, %8, %12 sc1\n\tglobal_load_dwordx4 %5, %9, %12 sc1\n\t" \
          "global_load_dwordx4 %6, %8, %13 sc1\n\tglobal_load_dwordx4 %7, %9, %13 sc1" TAIL \
          : "=&v"(pv[0]), "=&v"(pv[1]), "=&v"(pv[2]), "=&v"(pv[3]), "=&v"(pv[4]), "=&v"(pv[5]), "=&v"(pv[6]), "=&v"(pv[7]) \
          : "v"(OFF), "v"((OFF) + 16 * KVX_TILE), "s"(kb0), "s"(kb1), "s"(kb2), "s"(kb3) : "memory")
#define KV_WAIT(pv) asm volatile("s_waitcnt vmcnt(0)" \
          : "+v"(pv[0]), "+v"(pv[1]), "+v"(pv[2]), "+v"(pv[3]), "+v"(pv[4]), "+v"(pv[5]), "+v"(pv[6]), "+v"(pv[7]) :: "memory")
    LAS char* kvs = lds + 98304;
    auto finish = [&](u32x4 (&pv)[8], unsigned off) {
      for (unsigned spins = 0;; ++spins) {
        unsigned m = 0;
#pragma unroll
        for (int p = 0; p < 8; ++p) m = umax4(pv[p], m);
        if (__all(m != 0xffffffffu) || spins > (1u << 14)) break;
        __builtin_amdgcn_s_sleep(2);
        KV_GATHER(pv, off, "\n\ts_waitcnt vmcnt(0)");
      }
      f32x2 t[4];
#pragma unroll
      for (int r = 0; r < 4; ++r) t[r] = (f32x2){blo(pv[0][r]), bhi(pv[0][r])};
#pragma unroll
      for (int p = 1; p < 8; ++p)
#pragma unroll
        for (int r = 0; r < 4; ++r) t[r] += (f32x2){blo(pv[p][r]), bhi(pv[p][r])};
      *(LAS u32x4*)(kvs + off) = (u32x4){pk2(t[0][0], t[0][1]), pk2(t[1][0], t[1][1]), pk2(t[2][0], t[2][1]), pk2(t[3][0], t[3][1])};
    };
    const unsigned go = (unsigned)tid * 16, gz = 8192u + (unsigned)i16 * 16;
    STAMP(0, 9);
    SCHED();
    KV_GATHER(pv, go, "");
    KV_GATHER(pz, gz, "");
    SCHED();
    qsm(1);
    STAMP(0, 10);
    SCHED();
    KV_WAIT(pv);
    KV_WAIT(pz);
    SCHED();
    finish(pv, go);
    if (wid == 0) finish(pz, gz);
    STAMP(0, 11);
    WAIT_L(0); BAR();
    bf16x8 a[2][3];
#pragma unroll
    for (int sg = 0; sg < 2; ++sg) {
#pragma unroll
      for (int d = 0; d < 2; ++d) a[sg][d] = lds_b128(kvs + wc * 4096 + (sg * 2 + d) * 1024 + lane * 16);
      a[sg][2] = lds_b128(kvs + 8192 + wc * 128 + sg * 64 + g * 16);
    }
#pragma unroll
    for (int m = 0; m < 4; ++m) {
      const bf16x8 bp = __builtin_bit_cast(bf16x8, qp[m >> 1][m & 1]), bn = __builtin_bit_cast(bf16x8, qn[m >> 1][m & 1]);
      f32x4 o0 = MFMA16(a[0][0], bp, z4), o1 = MFMA16(a[0][1], bp, z4), dn = MFMA16(a[0][2], bp, z4);
      o0 = MFMA16(a[1][0], bn, o0); o1 = MFMA16(a[1][1], bn, o1); dn = MFMA16(a[1][2], bn, dn);
      const float rd = __builtin_amdgcn_rcpf(dn[0] + 1e-6f);
      const int rt = panel * 16 + wr * 4 + m;
      u32x4 w;
      w[0] = pk2(o0[0] * rd, o0[1] * rd); w[1] = pk2(o0[2] * rd, o0[3] * rd);
      w[2] = pk2(o1[0] * rd, o1[1] * rd); w[3] = pk2(o1[2] * rd, o1[3] * rd);
      st_sc1((char*)act + ((size_t)rt * NKG2 + 112 + head) * 1024 + lane * 16, w);
    }
  }
  STAMP(0, 3);
  return tag;
}

constexpr int G2_SLOT = 32768;
constexpr int G2_POLL = 4 * G2_SLOT + 4096;
constexpr int KSPLIT = 36;

DI void gemm2_body(char* ws, const u16* __restrict__ w2, const u16* __restrict__ wo,
                   const u16* __restrict__ x, const u16* __restrict__ b2, u16* __restrict__ out,
                   u64 tag, int slot, int split, int panel, int pm, int pn, LAS char* lds) {
  const char* act = ws + WS_ACT;
  char* xch = ws + WS_XCH;
  char* flg2 = ws + WS_FLG + 2048 + panel * 256;
  const unsigned ldsb = (unsigned)(size_t)lds;
  int lane = __builtin_amdgcn_mbcnt_hi(~0u, __builtin_amdgcn_mbcnt_lo(~0u, 0u));
  asm volatile("" : "+v"(lane));
  const int wid = __builtin_amdgcn_readfirstlane(threadIdx.x >> 6);
  const int kh = wid >> 2, wr = (wid >> 1) & 1, wc = wid & 1;
  const int g = lane >> 4, i16 = lane & 15, q4 = i16 >> 2, p4 = lane & 3;
  const int b = blockIdx.x;
  const int kt0 = split ? KSPLIT : 0, nst = split ? 72 - KSPLIT : KSPLIT;
  const char* abase = uniform_ptr(act + ((size_t)(pm * 8) * NKG2 + kt0 * 2) * 1024);
  const unsigned avoff = (unsigned)wid * NKG2 * 1024 + lane * 16;
  const unsigned adst = ldsb + wid * 1024;
  const int r8 = lane >> 3;
  const unsigned bvoff = (unsigned)((wid * 8 + r8) * D_ + pn * 128 + (((lane & 7) ^ (((r8 >> 1) & 3) << 1)) << 3)) * 2;
  const unsigned bdst = ldsb + 16384 + wid * 2048;
  const char* wb = uniform_ptr((const char*)w2 + (size_t)kt0 * 64 * D_ * 2);
  auto issue = [&](int q, int t, int slot) {
    if (q < 2) glds16_sc1(avoff + q * 1024, abase + (size_t)t * 2048, adst + q * 8192 + slot * G2_SLOT);
    else       glds16(bvoff + (q - 2) * 128, wb + (size_t)t * 64 * D_ * 2, bdst + (q - 2) * 1024 + slot * G2_SLOT);
  };
  WAIT_V(0); WAIT_L(0); BAR();
  if (threadIdx.x == 0) __hip_atomic_store((gu64*)(flg2 + slot * 8), tag, __ATOMIC_RELAXED, __HIP_MEMORY_SCOPE_AGENT);
#pragma unroll
  for (int st = 0; st < 4; ++st) { issue(2, st, st); issue(3, st, st); }
  if (wid == 0) wait_flags(flg2 + 128 + i16 * 8, tag);
  BAR();
  STAMP(1, 0);
#pragma unroll
  for (int st = 0; st < 4; ++st) { issue(0, st, st); issue(1, st, st); }

  LAS char* ard[2];
  ard[0] = opaque(lds + kh * 8192 + wr * 4096 + lane * 16); ard[1] = opaque(ard[0] + 65536);
  LAS char* brd[2][4];
#pragma unroll
  for (int j = 0; j < 4; ++j) {
    const int r = 4 * (g & 1) + q4, sw = (r >> 1) & 3;
    brd[0][j] = opaque(lds + 16384 + ((kh * 4 + (g >> 1)) * 2 + wc) * 1024 + r * 128 + ((((2 * j) ^ (sw << 1)) + (p4 >> 1)) << 4) + (p4 & 1) * 8);
    brd[1][j] = opaque(brd[0][j] + 65536);
  }
  f32x4 acc[4][4];
#pragma unroll
  for (int m = 0; m < 4; ++m)
#pragma unroll
    for (int n = 0; n < 4; ++n) acc[m][n] = (f32x4){0.f, 0.f, 0.f, 0.f};
  bf16x8 fa[2][4], fb[2][4];
  auto rdA = [&](auto P, auto SL, int m) { fa[P.v][m] = lds_b128(ard[SL.v >> 1] + (SL.v & 1) * G2_SLOT + m * 1024); };
  auto rdB = [&](auto P, auto SL, int n) { LAS char* p = brd[SL.v >> 1][n] + (SL.v & 1) * G2_SLOT; fb[P.v][n] = cat8(lds_tr(p), lds_tr(p + 4096)); };
  auto mma4 = [&](auto P, int n) {
#pragma unroll
    for (int m = 0; m < 4; ++m) acc[m][n] = MFMA16(fb[P.v][n], fa[P.v][m], acc[m][n]);
  };
  auto step = [&](auto SC, auto VM, auto GL, auto RD, int t) {
    constexpr int P = SC.v & 1, SNv = (SC.v + 1) & 3;
    const IC<P> Pc; const IC<P ^ 1> Pn; const IC<SNv> SN;
    if constexpr (RD.v) { WAIT_V(VM.v); WAIT_L(0); BAR(); }
    SCHED();
    if constexpr (GL.v) { issue(0, t + 4, SC.v); issue(1, t + 4, SC.v); }
    if constexpr (RD.v) { rdA(Pn, SN, 0); rdA(Pn, SN, 1); rdB(Pn, SN, 0); }
    mma4(Pc, 0); SCHED();
    if constexpr (GL.v) { issue(2, t + 4, SC.v); issue(3, t + 4, SC.v); }
    if constexpr (RD.v) { rdA(Pn, SN, 2); rdA(Pn, SN, 3); rdB(Pn, SN, 1); }
    mma4(Pc, 1); SCHED();
    if constexpr (RD.v) { rdB(Pn, SN, 2); rdB(Pn, SN, 3); }
    mma4(Pc, 2); SCHED();
    mma4(Pc, 3); SCHED();
  };
  WAIT_V(2); BAR();
  STAMP(1, 1);
  {
    const IC<0> P0; const IC<0> S0;
#pragma unroll
    for (int m = 0; m < 4; ++m) rdA(P0, S0, m);
#pragma unroll
    for (int n = 0; n < 4; ++n) rdB(P0, S0, n);
  }
  const IC<1> Y; const IC<0> N_; const IC<8> V8;
  const int tp = split ? 8 : 24;
#pragma unroll 1
  for (int t = 0; t < nst - 4; t += 4) {
    if (wid == 0) {
      if (t == tp) {
        asm volatile("s_mov_b32 m0, %2\n\ts_nop 0\n\tglobal_load_lds_dword %0, %1 sc1" ::"v"((lane & 31) * 4), "s"(uniform_ptr(flg2)), "s"(ldsb + G2_POLL) : "memory");
      } else if (t == tp + 4) {
        if (!__all(*(LAS u64*)(lds + G2_POLL + i16 * 8) == tag)) wait_flags(flg2 + i16 * 8, tag);
      }
    }
    if (t == 52 - KSPLIT && split == 1) wb = uniform_ptr((const char*)wo - (size_t)(56 - KSPLIT) * 64 * D_ * 2);
    step(IC<0>{}, V8, Y, Y, t); step(IC<1>{}, V8, Y, Y, t + 1); step(IC<2>{}, V8, Y, Y, t + 2); step(IC<3>{}, V8, Y, Y, t + 3);
  }
  step(IC<0>{}, V8, N_, Y, 0);
  step(IC<1>{}, IC<4>{}, N_, Y, 0);
  step(IC<2>{}, IC<0>{}, N_, Y, 0);
  step(IC<3>{}, IC<0>{}, N_, N_, 0);
  STAMP(1, 2);
  const int quad = wid & 3;
  {
    LAS char* pp = lds + (quad * 2 + kh) * 8192 + lane * 16;
#pragma unroll
    for (int mm = 0; mm < 2; ++mm)
#pragma unroll
      for (int n = 0; n < 4; ++n) *(LAS f32x4*)(pp + (mm * 4 + n) * 1024) = kh ? acc[mm][n] : acc[2 + mm][n];
  }
  const bool keep = (wr == split);
  const int row0 = pm * 128 + wr * 64 + kh * 32 + i16, col0 = pn * 128 + wc * 64 + 4 * g;
  u32x2 xv[2][4], bv[4];
  if (keep) {
#pragma unroll
    for (int n = 0; n < 4; ++n) bv[n] = *(const u32x2*)(b2 + col0 + n * 16);
#pragma unroll
    for (int mm = 0; mm < 2; ++mm)
#pragma unroll
      for (int n = 0; n < 4; ++n) xv[mm][n] = *(const u32x2*)(x + (size_t)(row0 + mm * 16) * D_ + col0 + n * 16);
  }
  WAIT_L(0); BAR();
  f32x4 sum[2][4];
  {
    LAS char* pp = lds + (quad * 2 + (kh ^ 1)) * 8192 + lane * 16;
#pragma unroll
    for (int mm = 0; mm < 2; ++mm)
#pragma unroll
      for (int n = 0; n < 4; ++n) sum[mm][n] = (kh ? acc[2 + mm][n] : acc[mm][n]) + *(LAS f32x4*)(pp + (mm * 4 + n) * 1024);
  }
  const int piece = kh * 2 + wc;
  if (!keep) {
    char* dst = xch + (size_t)b * 16384 + piece * 4096 + lane * 16;
#pragma unroll
    for (int mm = 0; mm < 2; ++mm)
#pragma unroll
      for (int h = 0; h < 2; ++h)
        st_sc1(dst + (mm * 2 + h) * 1024, (u32x4){pk2(sum[mm][2 * h][0], sum[mm][2 * h][1]), pk2(sum[mm][2 * h][2], sum[mm][2 * h][3]),
                                                  pk2(sum[mm][2 * h + 1][0], sum[mm][2 * h + 1][1]), pk2(sum[mm][2 * h + 1][2], sum[mm][2 * h + 1][3])});
    if (b == 0 && wid == 0) {
#pragma unroll
      for (int j = 0; j < 4; ++j) wait_flags(ws + WS_FLG + j * 512 + lane * 8, tag);
      if (lane == 0) __hip_atomic_store((gu64*)(ws + WS_FLG + 4096), tag, __ATOMIC_RELAXED, __HIP_MEMORY_SCOPE_AGENT);
    }
  } else {
    const char* src = xch + (size_t)(b ^ 1) * 16384 + piece * 4096 + lane * 16;
    u32x4 pv[4];
    for (unsigned spins = 0;; ++spins) {
      asm volatile("global_load_dwordx4 %0, %1, off offset:3072 sc1\n\ts_waitcnt vmcnt(0)" : "=&v"(pv[3]) : "v"(src) : "memory");
      if (__all(fresh4(pv[3])) || spins > (1u << 14)) break;
      __builtin_amdgcn_s_sleep(1);
    }
    for (unsigned spins = 0;; ++spins) {
      asm volatile(
          "global_load_dwordx4 %0, %3, off sc1\n\tglobal_load_dwordx4 %1, %3, off offset:1024 sc1\n\t"
          "global_load_dwordx4 %2, %3, off offset:2048 sc1\n\ts_waitcnt vmcnt(0)"
          : "=&v"(pv[0]), "=&v"(pv[1]), "=&v"(pv[2]) : "v"(src) : "memory");
      if (__all(fresh4(pv[0]) && fresh4(pv[1]) && fresh4(pv[2])) || spins > (1u << 14)) break;
      __builtin_amdgcn_s_sleep(1);
    }
#pragma unroll
    for (int mm = 0; mm < 2; ++mm)
#pragma unroll
      for (int n = 0; n < 4; ++n) {
        const unsigned q0 = pv[mm * 2 + (n >> 1)][2 * (n & 1)], q1 = pv[mm * 2 + (n >> 1)][2 * (n & 1) + 1];
        const f32x4 v = sum[mm][n];
        u32x2 o;
        o[0] = pk2(v[0] + blo(q0) + blo(xv[mm][n][0]) + blo(bv[n][0]), v[1] + bhi(q0) + bhi(xv[mm][n][0]) + bhi(bv[n][0]));
        o[1] = pk2(v[2] + blo(q1) + blo(xv[mm][n][1]) + blo(bv[n][1]), v[3] + bhi(q1) + bhi(xv[mm][n][1]) + bhi(bv[n][1]));
        *(u32x2*)(out + (size_t)(row0 + mm * 16) * D_ + col0 + n * 16) = o;
      }
  }
  STAMP(1, 3);
  STAMP(1, 4);
}

__global__ __launch_bounds__(512, 2) void k_main(const u16* __restrict__ x, const u16* __restrict__ wqkv, const u16* __restrict__ w1,
                                                 const u16* __restrict__ b1, const u16* __restrict__ wphi,
                                                 const u16* __restrict__ w2, const u16* __restrict__ wo, const u16* __restrict__ b2,
                                                 char* ws, u16* __restrict__ out) {
  extern __shared__ __attribute__((aligned(16))) char smem[];
  LAS char* lds = (LAS char*)smem;
  const int b = blockIdx.x, grp = b & 7, idx = b >> 3;
  const int panel = idx & 7, kind = grp & 1, tcol = (grp >> 1) * 4 + (idx >> 3);
  u64 tag;
  if (kind == 0) {
    tag = gemm1_body<true>(x, wqkv, tcol * 64, 0, panel, tcol, b1, wphi, ws, w2, wo, lds);
  } else {
    const int c0 = tcol * 224, woff = c0 & 63;
    tag = gemm1_body<false>(x, w1, c0 - woff, woff, panel, tcol, b1, wphi, ws, w2, wo, lds);
  }
  gemm2_body(ws, w2, wo, x, b2, out, tag, kind * 16 + tcol,   kind ^ 1, panel, panel * 2 + ((grp >> 1) & 1), (grp >> 2) * 4 + (idx >> 3), lds);
}

extern "C" void kernel_launch(void* const* d_in, const int* in_sizes, int n_in, void* d_out, int out_size, void* d_ws, size_t ws_size,
                              hipStream_t stream) {
  (void)hipFuncSetAttribute((const void*)k_main, hipFuncAttributeMaxDynamicSharedMemorySize, G1_LDS);
  k_main<<<256, 512, G1_LDS, stream>>>((const u16*)d_in[0], (const u16*)d_in[1], (const u16*)d_in[5], (const u16*)d_in[6], (const u16*)d_in[2],
                                       (const u16*)d_in[7], (const u16*)d_in[3], (const u16*)d_in[8], (char*)d_ws, (u16*)d_out);
}
```

```cpp
#include <hip/hip_runtime.h>
#include <hip/hip_bf16.h>
#include <stdint.h>

typedef __bf16 bf16x8 __attribute__((ext_vector_type(8)));
typedef __bf16 bf16x4 __attribute__((ext_vector_type(4)));
typedef __bf16 bf16x2 __attribute__((ext_vector_type(2)));
typedef float f32x4 __attribute__((ext_vector_type(4)));
typedef float f32x2 __attribute__((ext_vector_type(2)));
typedef unsigned u32x4 __attribute__((ext_vector_type(4)));
typedef unsigned u32x2 __attribute__((ext_vector_type(2)));
typedef unsigned short u16;
#define LAS __attribute__((address_space(3)))
#define DI __device__ __forceinline__

constexpr int S_ = 2048, D_ = 1024, F_ = 3584, H_ = 32;
constexpr int KCAT = F_ + D_;
constexpr int NKG1 = D_ / 32;
constexpr int NKG2 = KCAT / 32;

constexpr size_t WS_XN   = 0;
constexpr size_t WS_ACT  = WS_XN + (size_t)S_ * D_ * 2;
constexpr size_t WS_KVX  = WS_ACT + (size_t)S_ * KCAT * 2;
constexpr int KVX_TILE = 8192 + 256;
constexpr size_t KVX_BYTES = (size_t)8 * 16 * KVX_TILE;
constexpr size_t WS_XCH  = WS_KVX + KVX_BYTES;
constexpr size_t WS_FLG  = WS_XCH + (size_t)256 * 16384;
#define FSV 8
constexpr int FS = FSV;
constexpr size_t WS_FLG2 = WS_FLG + 256 * FS, WS_EPOCH = WS_FLG + 512 * FS + 128;

DI float rbf(float x) { return (float)(__bf16)x; }
DI unsigned pk2(float lo, float hi) { bf16x2 v; v[0] = (__bf16)lo; v[1] = (__bf16)hi; return __builtin_bit_cast(unsigned, v); }
DI float blo(unsigned u) { return __uint_as_float(u << 16); }
DI float bhi(unsigned u) { return __uint_as_float(u & 0xffff0000u); }

#define MFMA16(a, b, c) __builtin_amdgcn_mfma_f32_16x16x32_bf16((a), (b), (c), 0, 0, 0)
#define WAIT_V(n) asm volatile("s_waitcnt vmcnt(%0)" ::"n"(n) : "memory")
#define WAIT_L(n) do { static_assert((n) == 0, ""); asm volatile("" ::: "memory"); __builtin_amdgcn_s_waitcnt(0xC07F); asm volatile("" ::: "memory"); } while (0)
#define BAR() do { asm volatile("" ::: "memory"); __builtin_amdgcn_s_barrier(); asm volatile("" ::: "memory"); } while (0)
#define SCHED() __builtin_amdgcn_sched_barrier(0)
#define STAMP(K, I) do { } while (0)
template <int N> struct IC { static constexpr int v = N; };

DI void glds16_sc1(unsigned voff, const void* sbase, unsigned ldsaddr) {
  asm volatile("s_mov_b32 m0, %2\n\ts_nop 0\n\tglobal_load_lds_dwordx4 %0, %1 sc1" ::"v"(voff), "s"(sbase), "s"(ldsaddr) : "memory");
}
DI void glds16(unsigned voff, const void* sbase, unsigned ldsaddr) {
  asm volatile("s_mov_b32 m0, %2\n\ts_nop 0\n\tglobal_load_lds_dwordx4 %0, %1" ::"v"(voff), "s"(sbase), "s"(ldsaddr) : "memory");
}
DI const char* uniform_ptr(const char* p) {
  const unsigned long long a = (unsigned long long)p;
  unsigned lo = __builtin_amdgcn_readfirstlane((unsigned)a), hi = __builtin_amdgcn_readfirstlane((unsigned)(a >> 32));
  asm volatile("s_nop 4" : "+s"(lo), "+s"(hi));
  return (const char*)(((unsigned long long)hi << 32) | lo);
}
DI LAS char* opaque(LAS char* p) { asm volatile("" : "+v"(p)); return p; }
DI bf16x8 lds_b128(LAS char* p) { return *(LAS bf16x8*)p; }
DI bf16x4 lds_tr(LAS char* p) { return __builtin_amdgcn_ds_read_tr16_b64_v4bf16((LAS bf16x4*)p); }
DI bf16x8 cat8(bf16x4 a, bf16x4 b) { return __builtin_shufflevector(a, b, 0, 1, 2, 3, 4, 5, 6, 7); }
DI bf16x8 pack8(f32x4 a, f32x4 b) {
  bf16x8 v;
  v[0] = (__bf16)a[0]; v[1] = (__bf16)a[1]; v[2] = (__bf16)a[2]; v[3] = (__bf16)a[3];
  v[4] = (__bf16)b[0]; v[5] = (__bf16)b[1]; v[6] = (__bf16)b[2]; v[7] = (__bf16)b[3];
  return v;
}

typedef __attribute__((address_space(1))) unsigned gu32;
typedef unsigned long long u64;
typedef __attribute__((address_space(1))) u64 gu64;
DI void st_sc1(void* p, u32x4 v) { asm volatile("global_store_dwordx4 %0, %1, off sc1\n\ts_nop 1" ::"v"(p), "v"(v) : "memory"); }
DI unsigned umax4(u32x4 v, unsigned m) { return max(max(max(v[0], v[1]), v[2]), max(v[3], m)); }
DI void st8_sc1(void* p, u32x2 v) { asm volatile("global_store_dwordx2 %0, %1, off sc1\n\ts_nop 1" ::"v"(p), "v"(v) : "memory"); }
DI bool fresh4(u32x4 v) { return umax4(v, 0u) != 0xffffffffu; }
#define XRED4(OP, a1, b1, a2, b2) asm volatile( \
    "s_nop 1\n\tv_permlane16_swap_b32 %0, %1\n\tv_permlane16_swap_b32 %2, %3\n\t" \
    OP " %0, %0, %1\n\t" OP " %2, %2, %3\n\tv_mov_b32 %1, %0\n\tv_mov_b32 %3, %2\n\ts_nop 0\n\t" \
    "v_permlane32_swap_b32 %0, %1\n\tv_permlane32_swap_b32 %2, %3\n\t" \
    OP " %0, %0, %1\n\t" OP " %2, %2, %3\n\tv_mov_b32 %1, %0\n\tv_mov_b32 %3, %2\n\ts_nop 0\n\t" \
    "v_permlane16_swap_b32 %0, %1\n\tv_permlane16_swap_b32 %2, %3" \
    : "+v"(a1), "+v"(b1), "+v"(a2), "+v"(b2))
DI float max8(const float* v) {
  const float t1 = fmaxf(fmaxf(v[0], v[1]), v[2]), t2 = fmaxf(fmaxf(v[3], v[4]), v[5]), t3 = fmaxf(fmaxf(v[6], v[7]), t1);
  return fmaxf(t2, t3);
}
DI float min8(const float* v) {
  const float t1 = fminf(fminf(v[0], v[1]), v[2]), t2 = fminf(fminf(v[3], v[4]), v[5]), t3 = fminf(fminf(v[6], v[7]), t1);
  return fminf(t2, t3);
}
DI void dual_softmax2(const f32x4 (&p)[2][2], u32x4 (&pos)[2], u32x4 (&neg)[2]) {
  float v[2][8];
#pragma unroll
  for (int t = 0; t < 2; ++t)
#pragma unroll
    for (int h = 0; h < 2; ++h)
#pragma unroll
      for (int r = 0; r < 2; ++r) {
        unsigned u = pk2(p[t][h][2 * r], p[t][h][2 * r + 1]);
        asm("" : "+v"(u));
        v[t][4 * h + 2 * r] = blo(u); v[t][4 * h + 2 * r + 1] = bhi(u);
      }
  float mx0 = max8(v[0]), nm0 = -min8(v[0]), mx1 = max8(v[1]), nm1 = -min8(v[1]);
  XRED4("v_max_f32", mx0, nm0, mx1, nm1);
  constexpr float L2E = 1.44269504f;
  const f32x2 kp = {L2E, L2E}, kn = {-L2E, -L2E};
  f32x2 ep[2][4], en[2][4];
  float sp[2], sn[2];
#pragma unroll
  for (int t = 0; t < 2; ++t) {
    const float cpv = -(t ? mx1 : mx0) * L2E, cnv = -(t ? nm1 : nm0) * L2E;
    const f32x2 cp = {cpv, cpv}, cn = {cnv, cnv};
#pragma unroll
    for (int r = 0; r < 4; ++r) {
      const f32x2 x = {v[t][2 * r], v[t][2 * r + 1]};
      const f32x2 ap = __builtin_elementwise_fma(x, kp, cp), an = __builtin_elementwise_fma(x, kn, cn);
      ep[t][r] = (f32x2){__builtin_amdgcn_exp2f(ap[0]), __builtin_amdgcn_exp2f(ap[1])};
      en[t][r] = (f32x2){__builtin_amdgcn_exp2f(an[0]), __builtin_amdgcn_exp2f(an[1])};
    }
    const f32x2 s2p = (ep[t][0] + ep[t][1]) + (ep[t][2] + ep[t][3]), s2n = (en[t][0] + en[t][1]) + (en[t][2] + en[t][3]);
    sp[t] = s2p[0] + s2p[1]; sn[t] = s2n[0] + s2n[1];
  }
  XRED4("v_add_f32", sp[0], sn[0], sp[1], sn[1]);
#pragma unroll
  for (int t = 0; t < 2; ++t) {
    const float rp = __builtin_amdgcn_rcpf(sp[t]), rn = __builtin_amdgcn_rcpf(sn[t]);
    const f32x2 rp2 = {rp, rp}, rn2 = {rn, rn};
#pragma unroll
    for (int r = 0; r < 4; ++r) {
      const f32x2 a = ep[t][r] * rp2, b = en[t][r] * rn2;
      pos[t][r] = pk2(a[0], a[1]); neg[t][r] = pk2(b[0], b[1]);
    }
  }
}

DI float sum8lanes(float v) {
  v += __builtin_bit_cast(float, __builtin_amdgcn_update_dpp(0, __builtin_bit_cast(int, v), 0xB1, 0xf, 0xf, true));
  v += __builtin_bit_cast(float, __builtin_amdgcn_update_dpp(0, __builtin_bit_cast(int, v), 0x4E, 0xf, 0xf, true));
  v += __builtin_bit_cast(float, __builtin_amdgcn_update_dpp(0, __builtin_bit_cast(int, v), 0x141, 0xf, 0xf, true));
  return v;
}
#define PSLEEP 1
DI void wait_flags(const char* p, u64 tag) {
  for (unsigned spins = 0; spins < (1u << 16); ++spins) {
    if (__all(__hip_atomic_load((const gu64*)p, __ATOMIC_RELAXED, __HIP_MEMORY_SCOPE_AGENT) == tag)) break;
    __builtin_amdgcn_s_sleep(PSLEEP);
  }
}

constexpr int G1_SLOT = 16384;
constexpr int G1_B = 65536, G1_X = 131072;
constexpr int G1_LDS = G1_X + 4096 + 2048;

DI f32x2 gelu2(f32x2 x) {
  const f32x2 c1 = {-0.10294324f, -0.10294324f}, c0 = {-2.3022082f, -2.3022082f}, one = {1.0f, 1.0f};
  const f32x2 a = x * __builtin_elementwise_fma(x * x, c1, c0);
  const f32x2 d = one + (f32x2){__builtin_amdgcn_exp2f(a[0]), __builtin_amdgcn_exp2f(a[1])};
  return x * (f32x2){__builtin_amdgcn_rcpf(d[0]), __builtin_amdgcn_rcpf(d[1])};
}

template <bool QKV>
DI u64 gemm1_body(const u16* __restrict__ x, const u16* __restrict__ W, int wcol0, int woff, int panel, int tcol,
                  const u16* __restrict__ b1, const u16* __restrict__ wphi, char* ws,
                  const u16* __restrict__ w2, const u16* __restrict__ wo, LAS char* lds) {
  char* xn = ws + WS_XN;
  char* act = ws + WS_ACT;
  char* kvx = ws + WS_KVX;
  constexpr int LDW = QKV ? 3 * D_ : F_;
  constexpr int NG = QKV ? 3 : 4;
  constexpr int NT = QKV ? 6 : 7;
  const int tid = threadIdx.x, lane = tid & 63;
  const int wid = __builtin_amdgcn_readfirstlane(tid >> 6);
  const int wr = wid >> 1, wc = wid & 1;
  const int g = lane >> 4, i16 = lane & 15, q4 = i16 >> 2, p4 = lane & 3;
  const unsigned ldsb = (unsigned)(size_t)lds;

  STAMP(0, 0);
  const char* abase = uniform_ptr(xn + (size_t)panel * 16 * NKG1 * 1024);
  const unsigned avoff = (unsigned)(wid * 2) * NKG1 * 1024 + lane * 16;
  const char* bbase = (const char*)W;
  unsigned bvoff[2]; unsigned bdst[2];
#pragma unroll
  for (int j = 0; j < 2; ++j) {
    int pb = QKV ? (wid + 8 * j) : (wid * 2 + j);
    if (QKV && pb >= 12) pb -= 8;
    const int kg = pb / NG, cg = pb % NG;
    const int r = lane >> 3, sw = ((r >> 1) & 1) | ((kg & 1) << 1);
    const int cc = (lane & 7) ^ (sw << 1);
    const int col = QKV ? (cg * D_ + wcol0) : (wcol0 + cg * 64);
    bvoff[j] = (unsigned)(((kg * 8 + r) * LDW + col + cc * 8) * 2);
    bdst[j] = ldsb + G1_B + pb * 1024;
  }
  const unsigned adst = ldsb + wid * 2048;

  LAS char* ard = opaque(lds + wr * 4096 + lane * 16);
  constexpr int NB = QKV ? 2 : 4;
  LAS char* brd[NB];
#pragma unroll
  for (int j = 0; j < NB; ++j) {
    const int n0 = QKV ? (wc * 32 + j * 16) : (woff + wc * 112 + j * 16);
    const int sw = (q4 >> 1) | ((g & 1) << 1);
    brd[j] = opaque(lds + G1_B + (g * NG + (n0 >> 6)) * 1024 + q4 * 128 + (((((n0 & 63) >> 3) ^ (sw << 1)) + (p4 >> 1)) << 4) + (p4 & 1) * 8);
  }

  f32x4 acc[4][NT];
#pragma unroll
  for (int m = 0; m < 4; ++m)
#pragma unroll
    for (int n = 0; n < NT; ++n) acc[m][n] = (f32x4){0.f, 0.f, 0.f, 0.f};

  bf16x8 fa[2][4], fb[2][NT];

  auto issue = [&](int q, int stage, int slot) {
    if (q < 2) glds16_sc1(avoff + q * NKG1 * 1024, abase + (size_t)stage * 1024, adst + q * 1024 + slot * G1_SLOT);
    else       glds16(bvoff[q - 2], bbase + (size_t)stage * 64 * LDW, bdst[q - 2] + slot * G1_SLOT);
  };
  auto rdA = [&](auto P, auto SL, int m) { fa[P.v][m] = lds_b128(ard + SL.v * G1_SLOT + m * 1024); };
  auto rdB = [&](auto P, auto SL, int n) { LAS char* p = brd[n % NB] + (n / NB) * 1024 + SL.v * G1_SLOT; fb[P.v][n] = cat8(lds_tr(p), lds_tr(p + 512)); };
  auto mma4 = [&](auto P, int n) {
#pragma unroll
    for (int m = 0; m < 4; ++m) {
      if (QKV && n >= 4) acc[m][n] = MFMA16(fa[P.v][m], fb[P.v][n], acc[m][n]);
      else               acc[m][n] = MFMA16(fb[P.v][n], fa[P.v][m], acc[m][n]);
    }
  };
  auto step = [&](auto SC, auto VM, auto GL, auto RD, int s) {
    constexpr int P = SC.v & 1, SNv = (SC.v + 1) & 3;
    const IC<P> Pc; const IC<P ^ 1> Pn; const IC<SNv> SN;
    if constexpr (RD.v) { WAIT_V(VM.v); WAIT_L(0); BAR(); }
    SCHED();
    if constexpr (GL.v) issue(0, s + 4, SC.v);
    if constexpr (RD.v) { rdA(Pn, SN, 0); rdA(Pn, SN, 1); rdB(Pn, SN, 0); }
    mma4(Pc, 0); SCHED();
    if constexpr (GL.v) issue(1, s + 4, SC.v);
    if constexpr (RD.v) { rdA(Pn, SN, 2); rdA(Pn, SN, 3); rdB(Pn, SN, 1); }
    mma4(Pc, 1); SCHED();
    if constexpr (GL.v) issue(2, s + 4, SC.v);
    if constexpr (RD.v) { rdB(Pn, SN, 2); rdB(Pn, SN, 3); }
    mma4(Pc, 2); SCHED();
    if constexpr (GL.v) issue(3, s + 4, SC.v);
    if constexpr (RD.v) { rdB(Pn, SN, 4); rdB(Pn, SN, 5); }
    mma4(Pc, 3); SCHED();
    if constexpr (RD.v && NT == 7) rdB(Pn, SN, 6);
    mma4(Pc, 4); SCHED();
    mma4(Pc, 5);
    if constexpr (NT == 7) mma4(Pc, 6);
    SCHED();
  };

  const int slot = (QKV ? 0 : 16) + tcol;
  u64 tag;
  {
    const int r8 = lane >> 3, c8 = lane & 7;
    const int row = panel * 256 + slot * 8 + r8;
    const u16* src = x + (size_t)row * D_ + wid * 128 + c8 * 8;
    u32x4 v0, v1; u32x2 ep;
    asm volatile("global_load_dwordx4 %0, %3, off\n\tglobal_load_dwordx4 %1, %3, off offset:128\n\tglobal_load_dwordx2 %2, %4, off sc1"
                 : "=&v"(v0), "=&v"(v1), "=&v"(ep) : "v"(src), "v"(ws + WS_EPOCH) : "memory");
    SCHED();
    if (QKV) {
      const u32x4 fill = {~0u, ~0u, ~0u, ~0u};
      char* kp = kvx + (size_t)(panel * 16 + tcol) * KVX_TILE;
      st_sc1(kp + tid * 16, fill);
      st_sc1(kp + 8192 + i16 * 16, fill);
    }
    SCHED();
    asm volatile("s_waitcnt vmcnt(%3)" : "+v"(v0), "+v"(v1), "+v"(ep) : "n"(QKV ? 2 : 0) : "memory");
    STAMP(0, 13);
    SCHED();
    tag = (((u64)(unsigned)__builtin_amdgcn_readfirstlane(ep[1]) << 32) | (unsigned)__builtin_amdgcn_readfirstlane(ep[0])) + 1;
    f32x2 f[8];
#pragma unroll
    for (int i = 0; i < 4; ++i) { f[i] = (f32x2){blo(v0[i]), bhi(v0[i])}; f[4 + i] = (f32x2){blo(v1[i]), bhi(v1[i])}; }
    const f32x2 s2 = ((f[0] + f[1]) + (f[2] + f[3])) + ((f[4] + f[5]) + (f[6] + f[7]));
    const float mw = sum8lanes(s2[0] + s2[1]) * (1.0f / 128.f);
    f32x2 q2 = {0.f, 0.f};
#pragma unroll
    for (int i = 0; i < 8; ++i) { const f32x2 d = f[i] - (f32x2){mw, mw}; q2 = __builtin_elementwise_fma(d, d, q2); }
    const float qq = sum8lanes(q2[0] + q2[1]);
    LAS f32x2* stt = (LAS f32x2*)(lds + G1_X + 4096);
    if (c8 == 0) stt[r8 * 8 + wid] = (f32x2){mw, qq};
    WAIT_L(0); BAR();
    const f32x2 pw = stt[r8 * 8 + c8];
    const float mean = sum8lanes(pw[0]) * 0.125f;
    const float dm = pw[0] - mean;
    const float m2 = sum8lanes(__builtin_fmaf(128.f * dm, dm, pw[1]));
    const float mb = rbf(mean), vb = rbf(m2 * (1.0f / D_));
    const float rs = 1.0f / rbf(sqrtf(rbf(vb + 1e-5f)));
    u32x4 o0, o1;
#pragma unroll
    for (int i = 0; i < 8; ++i) {
      const f32x2 d = f[i] - (f32x2){mb, mb};
      unsigned u = pk2(d[0], d[1]);
      asm("" : "+v"(u));
      const f32x2 y = (f32x2){blo(u), bhi(u)} * (f32x2){rs, rs};
      if (i < 4) o0[i] = pk2(y[0], y[1]); else o1[i - 4] = pk2(y[0], y[1]);
    }
    char* dst = xn + ((size_t)((row >> 4) * NKG1 + wid * 4 + (c8 >> 2)) * 64 + (c8 & 3) * 16 + (row & 15)) * 16;
    st_sc1(dst, o0); st_sc1(dst + 2048, o1);
    STAMP(0, 14);
    WAIT_V(0); BAR();
    STAMP(0, 15);
    if (tid == 0) __hip_atomic_store((gu64*)(ws + WS_FLG + (panel * 32 + slot) * FS), tag, __ATOMIC_RELAXED, __HIP_MEMORY_SCOPE_AGENT);
    if (QKV && wid >= 4) glds16((unsigned)lane * 16, uniform_ptr((const char*)wphi + (size_t)tcol * 4096 + (wid - 4) * 1024), ldsb + G1_X + (wid - 4) * 1024);
#pragma unroll
    for (int st = 0; st < 4; ++st) { issue(2, st, st); issue(3, st, st); }
    if (wid == 0) wait_flags(ws + WS_FLG + ((QKV && lane >= 32) ? ((lane & 7) * 32 + slot) : (panel * 32 + (lane & 31))) * FS, tag);
    BAR();
  }
  STAMP(0, 12);
#pragma unroll
  for (int st = 0; st < 4; ++st) { issue(0, st, st); issue(1, st, st); }
  WAIT_V(2); BAR();
  STAMP(0, 1);
  {
    const IC<0> P0; const IC<0> S0;
#pragma unroll
    for (int m = 0; m < 4; ++m) rdA(P0, S0, m);
#pragma unroll
    for (int n = 0; n < NT; ++n) rdB(P0, S0, n);
  }
#pragma unroll 1
  for (int s = 0; s < 28; s += 4) {
    step(IC<0>{}, IC<8>{}, IC<1>{}, IC<1>{}, s);
    step(IC<1>{}, IC<8>{}, IC<1>{}, IC<1>{}, s + 1);
    step(IC<2>{}, IC<8>{}, IC<1>{}, IC<1>{}, s + 2);
    step(IC<3>{}, IC<8>{}, IC<1>{}, IC<1>{}, s + 3);
  }
  step(IC<0>{}, IC<8>{}, IC<0>{}, IC<1>{}, 28);
  constexpr int NX = 3;
  {
    int wv = blockIdx.x * 8 + wid;
    if (wv >= 1152) wv -= 1152;
    const char* src = uniform_ptr(wv < 896 ? (const char*)w2 + (size_t)wv * 8192 : (const char*)wo + (size_t)(wv - 896) * 8192);
    asm volatile("s_mov_b32 m0, %2\n\ts_nop 0\n\tglobal_load_lds_dword %0, %1" ::"v"(lane * 128), "s"(src), "s"(ldsb + G1_X + 4096 + wid * 256) : "memory");
    char* xp = ws + WS_XCH + (size_t)blockIdx.x * 16384 + tid * 16;
    const u32x4 fill = {~0u, ~0u, ~0u, ~0u};
    st_sc1(xp, fill); st_sc1(xp + 8192, fill);
  }
  step(IC<1>{}, IC<4 + NX>{}, IC<0>{}, IC<1>{}, 29);
  step(IC<2>{}, IC<0 + NX>{}, IC<0>{}, IC<1>{}, 30);
  step(IC<3>{}, IC<0>{}, IC<0>{}, IC<0>{}, 31);
  STAMP(0, 2);

  if (!QKV) {
    const int ncol0 = tcol * 224 + wc * 112 + 4 * g;
    u32x2 bb[NT];
#pragma unroll
    for (int n = 0; n < NT; ++n) bb[n] = *(const u32x2*)(b1 + ncol0 + n * 16);
    auto act4 = [&](int m, int n) {
      const f32x2 yA = gelu2((f32x2){acc[m][n][0] + blo(bb[n][0]), acc[m][n][1] + bhi(bb[n][0])});
      const f32x2 yB = gelu2((f32x2){acc[m][n][2] + blo(bb[n][1]), acc[m][n][3] + bhi(bb[n][1])});
      return (u32x2){pk2(yA[0], yA[1]), pk2(yB[0], yB[1])};
    };
    const int rt0 = panel * 16 + wr * 4;
    char* obase = (char*)act + ((size_t)rt0 * NKG2 + tcol * 7) * 1024 + lane * 16;
    if (wc == 0) {
#pragma unroll
      for (int m = 0; m < 4; ++m) {
        char* rb = obase + (size_t)m * NKG2 * 1024;
#pragma unroll
        for (int p = 0; p < 3; ++p) { const u32x2 lo = act4(m, 2 * p), hi = act4(m, 2 * p + 1); st_sc1(rb + p * 1024, (u32x4){lo[0], lo[1], hi[0], hi[1]}); }
        st8_sc1(rb + 3 * 1024, act4(m, 6 % NT));
      }
    } else {
#pragma unroll
      for (int m = 0; m < 4; ++m) {
        char* rb = obase + (size_t)m * NKG2 * 1024;
        st8_sc1(rb + 3 * 1024 + 8, act4(m, 0));
#pragma unroll
        for (int p = 0; p < 3; ++p) { const u32x2 lo = act4(m, 2 * p + 1), hi = act4(m, (2 * p + 2) % NT); st_sc1(rb + (4 + p) * 1024, (u32x4){lo[0], lo[1], hi[0], hi[1]}); }
      }
    }
  } else {
    const int head = tcol * 2 + wc;
    LAS char* wp = lds + G1_X + wc * 2048;
    bf16x8 wf[2];
#pragma unroll
    for (int ft = 0; ft < 2; ++ft) {
      LAS char* p = wp + (4 * g + q4) * 64 + (16 * ft + 4 * p4) * 2;
      wf[ft] = cat8(lds_tr(p), lds_tr(p + 16 * 64));
    }
    const f32x4 z4 = {0.f, 0.f, 0.f, 0.f};
    LAS char* sc = lds + (wid >> 2) * G1_B + (wid & 3) * 8704;
#pragma unroll
    for (int mp = 0; mp < 2; ++mp) {
      f32x4 pp[2][2]; u32x4 pos[2], neg[2];
#pragma unroll
      for (int t = 0; t < 2; ++t) {
        const bf16x8 bk = pack8(acc[2 * mp + t][2], acc[2 * mp + t][3]);
        pp[t][0] = MFMA16(wf[0], bk, z4); pp[t][1] = MFMA16(wf[1], bk, z4);
      }
      dual_softmax2(pp, pos, neg);
#pragma unroll
      for (int t = 0; t < 2; ++t) {
        LAS char* rowp = sc + (16 * (2 * mp + t) + i16) * 136 + 8 * g;
        *(LAS u32x2*)(rowp) = (u32x2){pos[t][0], pos[t][1]};      *(LAS u32x2*)(rowp + 32) = (u32x2){pos[t][2], pos[t][3]};
        *(LAS u32x2*)(rowp + 64) = (u32x2){neg[t][0], neg[t][1]}; *(LAS u32x2*)(rowp + 96) = (u32x2){neg[t][2], neg[t][3]};
      }
    }
    WAIT_L(0);
    STAMP(0, 5);
    f32x4 kv[4][3];
#pragma unroll
    for (int ft = 0; ft < 4; ++ft)
#pragma unroll
      for (int d = 0; d < 3; ++d) kv[ft][d] = z4;
    bf16x8 ones;
#pragma unroll
    for (int j = 0; j < 8; ++j) ones[j] = (__bf16)1.0f;
#pragma unroll
    for (int ks = 0; ks < 2; ++ks) {
      const bf16x8 bv0 = pack8(acc[2 * ks][4], acc[2 * ks + 1][4]);
      const bf16x8 bv1 = pack8(acc[2 * ks][5], acc[2 * ks + 1][5]);
#pragma unroll
      for (int ft = 0; ft < 4; ++ft) {
        LAS char* p = sc + (32 * ks + 4 * g + q4) * 136 + (16 * ft + 4 * p4) * 2;
        const bf16x8 a = cat8(lds_tr(p), lds_tr(p + 16 * 136));
        kv[ft][0] = MFMA16(a, bv0, kv[ft][0]);
        kv[ft][1] = MFMA16(a, bv1, kv[ft][1]);
        kv[ft][2] = MFMA16(a, ones, kv[ft][2]);
      }
    }
    STAMP(0, 6);
    WAIT_L(0); BAR();
    STAMP(0, 7);
    {
      LAS char* pp = lds + wid * 8448;
#pragma unroll
      for (int sg = 0; sg < 2; ++sg) {
#pragma unroll
        for (int d = 0; d < 2; ++d) {
          *(LAS f32x4*)(pp + (sg * 2 + d) * 2048 + lane * 16) = kv[2 * sg][d];
          *(LAS f32x4*)(pp + (sg * 2 + d) * 2048 + 1024 + lane * 16) = kv[2 * sg + 1][d];
        }
        if (i16 == 0) {
          *(LAS f32x4*)(pp + 8192 + sg * 128 + g * 16) = kv[2 * sg][2];
          *(LAS f32x4*)(pp + 8192 + sg * 128 + 64 + g * 16) = kv[2 * sg + 1][2];
        }
      }
    }
    WAIT_L(0); BAR();
    char* kvt = kvx + (size_t)tcol * KVX_TILE;
    char* kvmine = kvt + (size_t)panel * 16 * KVX_TILE;
    {
      LAS char* pp = lds + (tid >> 8) * 8448 + ((tid >> 6) & 3) * 2048 + lane * 16;
      f32x4 lo = *(LAS f32x4*)(pp), hi = *(LAS f32x4*)(pp + 1024);
#pragma unroll
      for (int w = 1; w < 4; ++w) { lo += *(LAS f32x4*)(pp + w * 16896); hi += *(LAS f32x4*)(pp + w * 16896 + 1024); }
      st_sc1(kvmine + tid * 16, (u32x4){pk2(lo[0], lo[1]), pk2(lo[2], lo[3]), pk2(hi[0], hi[1]), pk2(hi[2], hi[3])});
    }
    if (tid < 16) {
      LAS char* pp = lds + (tid >> 3) * 8448 + 8192 + ((tid >> 2) & 1) * 128 + (tid & 3) * 16;
      f32x4 lo = *(LAS f32x4*)(pp), hi = *(LAS f32x4*)(pp + 64);
#pragma unroll
      for (int w = 1; w < 4; ++w) { lo += *(LAS f32x4*)(pp + w * 16896); hi += *(LAS f32x4*)(pp + w * 16896 + 64); }
      st_sc1(kvmine + 8192 + tid * 16, (u32x4){pk2(lo[0], lo[1]), pk2(lo[2], lo[3]), pk2(hi[0], hi[1]), pk2(hi[2], hi[3])});
    }
    STAMP(0, 8);
    u32x4 qp[2][2], qn[2][2];
    auto qsm = [&](int mp) {
      f32x4 pp[2][2];
#pragma unroll
      for (int t = 0; t < 2; ++t) {
        const bf16x8 bq = pack8(acc[2 * mp + t][0], acc[2 * mp + t][1]);
        pp[t][0] = MFMA16(wf[0], bq, z4); pp[t][1] = MFMA16(wf[1], bq, z4);
      }
      dual_softmax2(pp, qp[mp], qn[mp]);
    };
    qsm(0);
    const char* kb0 = uniform_ptr(kvt), * kb1 = uniform_ptr(kvt + (size_t)32 * KVX_TILE);
    const char* kb2 = uniform_ptr(kvt + (size_t)64 * KVX_TILE), * kb3 = uniform_ptr(kvt + (size_t)96 * KVX_TILE);
    u32x4 pv[8], pz[8];
#define KV_GATHER(pv, OFF, TAIL) asm volatile( \
          "global_load_dwordx4 %0, %8, %10 sc1\n\tglobal_load_dwordx4 %1, %9, %10 sc1\n\t" \
          "global_load_dwordx4 %2, %8, %11 sc1\n\tglobal_load_dwordx4 %3, %9, %11 sc1\n\t" \
          "global_load_dwordx4 %4, %8, %12 sc1\n\tglobal_load_dwordx4 %5, %9, %12 sc1\n\t" \
          "global_load_dwordx4 %6, %8, %13 sc1\n\tglobal_load_dwordx4 %7, %9, %13 sc1" TAIL \
          : "=&v"(pv[0]), "=&v"(pv[1]), "=&v"(pv[2]), "=&v"(pv[3]), "=&v"(pv[4]), "=&v"(pv[5]), "=&v"(pv[6]), "=&v"(pv[7]) \
          : "v"(OFF), "v"((OFF) + 16 * KVX_TILE), "s"(kb0), "s"(kb1), "s"(kb2), "s"(kb3) : "memory")
#define KV_WAIT(pv) asm volatile("s_waitcnt vmcnt(0)" \
          : "+v"(pv[0]), "+v"(pv[1]), "+v"(pv[2]), "+v"(pv[3]), "+v"(pv[4]), "+v"(pv[5]), "+v"(pv[6]), "+v"(pv[7]) :: "memory")
    LAS char* kvs = lds + 98304;
    auto finish = [&](u32x4 (&pv)[8], unsigned off) {
      for (unsigned spins = 0;; ++spins) {
        unsigned m = 0;
#pragma unroll
        for (int p = 0; p < 8; ++p) m = umax4(pv[p], m);
        if (__all(m != 0xffffffffu) || spins > (1u << 14)) break;
        __builtin_amdgcn_s_sleep(2);
        KV_GATHER(pv, off, "\n\ts_waitcnt vmcnt(0)");
      }
      f32x2 t[4];
#pragma unroll
      for (int r = 0; r < 4; ++r) t[r] = (f32x2){blo(pv[0][r]), bhi(pv[0][r])};
#pragma unroll
      for (int p = 1; p < 8; ++p)
#pragma unroll
        for (int r = 0; r < 4; ++r) t[r] += (f32x2){blo(pv[p][r]), bhi(pv[p][r])};
      *(LAS u32x4*)(kvs + off) = (u32x4){pk2(t[0][0], t[0][1]), pk2(t[1][0], t[1][1]), pk2(t[2][0], t[2][1]), pk2(t[3][0], t[3][1])};
    };
    const unsigned go = (unsigned)tid * 16, gz = 8192u + (unsigned)i16 * 16;
    STAMP(0, 9);
    SCHED();
    KV_GATHER(pv, go, "");
    KV_GATHER(pz, gz, "");
    SCHED();
    qsm(1);
    STAMP(0, 10);
    SCHED();
    KV_WAIT(pv);
    KV_WAIT(pz);
    SCHED();
    finish(pv, go);
    if (wid == 0) finish(pz, gz);
    STAMP(0, 11);
    WAIT_L(0); BAR();
    bf16x8 a[2][3];
#pragma unroll
    for (int sg = 0; sg < 2; ++sg) {
#pragma unroll
      for (int d = 0; d < 2; ++d) a[sg][d] = lds_b128(kvs + wc * 4096 + (sg * 2 + d) * 1024 + lane * 16);
      a[sg][2] = lds_b128(kvs + 8192 + wc * 128 + sg * 64 + g * 16);
    }
#pragma unroll
    for (int m = 0; m < 4; ++m) {
      const bf16x8 bp = __builtin_bit_cast(bf16x8, qp[m >> 1][m & 1]), bn = __builtin_bit_cast(bf16x8, qn[m >> 1][m & 1]);
      f32x4 o0 = MFMA16(a[0][0], bp, z4), o1 = MFMA16(a[0][1], bp, z4), dn = MFMA16(a[0][2], bp, z4);
      o0 = MFMA16(a[1][0], bn, o0); o1 = MFMA16(a[1][1], bn, o1); dn = MFMA16(a[1][2], bn, dn);
      const float rd = __builtin_amdgcn_rcpf(dn[0] + 1e-6f);
      const int rt = panel * 16 + wr * 4 + m;
      u32x4 w;
      w[0] = pk2(o0[0] * rd, o0[1] * rd); w[1] = pk2(o0[2] * rd, o0[3] * rd);
      w[2] = pk2(o1[0] * rd, o1[1] * rd); w[3] = pk2(o1[2] * rd, o1[3] * rd);
      st_sc1((char*)act + ((size_t)rt * NKG2 + 112 + head) * 1024 + lane * 16, w);
    }
  }
  STAMP(0, 3);
  return tag;
}

constexpr int G2_SLOT = 32768;
constexpr int G2_POLL = 4 * G2_SLOT + 4096;
constexpr int KSPLIT = 36;

DI void gemm2_body(char* ws, const u16* __restrict__ w2, const u16* __restrict__ wo,
                   const u16* __restrict__ x, const u16* __restrict__ b2, u16* __restrict__ out,
                   u64 tag, int slot, int split, int panel, int pm, int pn, LAS char* lds) {
  const char* act = ws + WS_ACT;
  char* xch = ws + WS_XCH;
  char* flg2 = ws + WS_FLG2 + panel * 32 * FS;
  const unsigned ldsb = (unsigned)(size_t)lds;
  int lane = __builtin_amdgcn_mbcnt_hi(~0u, __builtin_amdgcn_mbcnt_lo(~0u, 0u));
  asm volatile("" : "+v"(lane));
  const int wid = __builtin_amdgcn_readfirstlane(threadIdx.x >> 6);
  const int kh = wid >> 2, wr = (wid >> 1) & 1, wc = wid & 1;
  const int g = lane >> 4, i16 = lane & 15, q4 = i16 >> 2, p4 = lane & 3;
  const int b = blockIdx.x;
  const int kt0 = split ? KSPLIT : 0, nst = split ? 72 - KSPLIT : KSPLIT;
  const char* abase = uniform_ptr(act + ((size_t)(pm * 8) * NKG2 + kt0 * 2) * 1024);
  const unsigned avoff = (unsigned)wid * NKG2 * 1024 + lane * 16;
  const unsigned adst = ldsb + wid * 1024;
  const int r8 = lane >> 3;
  const unsigned bvoff = (unsigned)((wid * 8 + r8) * D_ + pn * 128 + (((lane & 7) ^ (((r8 >> 1) & 3) << 1)) << 3)) * 2;
  const unsigned bdst = ldsb + 16384 + wid * 2048;
  const char* wb = uniform_ptr((const char*)w2 + (size_t)kt0 * 64 * D_ * 2);
  auto issue = [&](int q, int t, int slot) {
    if (q < 2) glds16_sc1(avoff + q * 1024, abase + (size_t)t * 2048, adst + q * 8192 + slot * G2_SLOT);
    else       glds16(bvoff + (q - 2) * 128, wb + (size_t)t * 64 * D_ * 2, bdst + (q - 2) * 1024 + slot * G2_SLOT);
  };
  WAIT_V(0); WAIT_L(0); BAR();
  if (threadIdx.x == 0) __hip_atomic_store((gu64*)(flg2 + slot * FS), tag, __ATOMIC_RELAXED, __HIP_MEMORY_SCOPE_AGENT);
  if (wid == panel) wait_flags(flg2 + (16 + i16) * FS, tag);
#pragma unroll
  for (int st = 0; st < 4; ++st) { issue(2, st, st); issue(3, st, st); }
  BAR();
  STAMP(1, 0);
#pragma unroll
  for (int st = 0; st < 4; ++st) { issue(0, st, st); issue(1, st, st); }

  LAS char* ard[2];
  ard[0] = opaque(lds + kh * 8192 + wr * 4096 + lane * 16); ard[1] = opaque(ard[0] + 65536);
  LAS char* brd[2][4];
#pragma unroll
  for (int j = 0; j < 4; ++j) {
    const int r = 4 * (g & 1) + q4, sw = (r >> 1) & 3;
    brd[0][j] = opaque(lds + 16384 + ((kh * 4 + (g >> 1)) * 2 + wc) * 1024 + r * 128 + ((((2 * j) ^ (sw << 1)) + (p4 >> 1)) << 4) + (p4 & 1) * 8);
    brd[1][j] = opaque(brd[0][j] + 65536);
  }
  f32x4 acc[4][4];
#pragma unroll
  for (int m = 0; m < 4; ++m)
#pragma unroll
    for (int n = 0; n < 4; ++n) acc[m][n] = (f32x4){0.f, 0.f, 0.f, 0.f};
  bf16x8 fa[2][4], fb[2][4];
  auto rdA = [&](auto P, auto SL, int m) { fa[P.v][m] = lds_b128(ard[SL.v >> 1] + (SL.v & 1) * G2_SLOT + m * 1024); };
  auto rdB = [&](auto P, auto SL, int n) { LAS char* p = brd[SL.v >> 1][n] + (SL.v & 1) * G2_SLOT; fb[P.v][n] = cat8(lds_tr(p), lds_tr(p + 4096)); };
  auto mma4 = [&](auto P, int n) {
#pragma unroll
    for (int m = 0; m < 4; ++m) acc[m][n] = MFMA16(fb[P.v][n], fa[P.v][m], acc[m][n]);
  };
  auto step = [&](auto SC, auto VM, auto GL, auto RD, int t) {
    constexpr int P = SC.v & 1, SNv = (SC.v + 1) & 3;
    const IC<P> Pc; const IC<P ^ 1> Pn; const IC<SNv> SN;
    if constexpr (RD.v) { WAIT_V(VM.v); WAIT_L(0); BAR(); }
    SCHED();
    if constexpr (GL.v) { issue(0, t + 4, SC.v); issue(1, t + 4, SC.v); }
    if constexpr (RD.v) { rdA(Pn, SN, 0); rdA(Pn, SN, 1); rdB(Pn, SN, 0); }
    mma4(Pc, 0); SCHED();
    if constexpr (GL.v) { issue(2, t + 4, SC.v); issue(3, t + 4, SC.v); }
    if constexpr (RD.v) { rdA(Pn, SN, 2); rdA(Pn, SN, 3); rdB(Pn, SN, 1); }
    mma4(Pc, 1); SCHED();
    if constexpr (RD.v) { rdB(Pn, SN, 2); rdB(Pn, SN, 3); }
    mma4(Pc, 2); SCHED();
    mma4(Pc, 3); SCHED();
  };
  WAIT_V(2); BAR();
  STAMP(1, 1);
  {
    const IC<0> P0; const IC<0> S0;
#pragma unroll
    for (int m = 0; m < 4; ++m) rdA(P0, S0, m);
#pragma unroll
    for (int n = 0; n < 4; ++n) rdB(P0, S0, n);
  }
  const IC<1> Y; const IC<0> N_; const IC<8> V8;
  const int tp = split ? 8 : 24;
#pragma unroll 1
  for (int t = 0; t < nst - 4; t += 4) {
    if (wid == 0) {
      if (t == tp) {
        asm volatile("s_mov_b32 m0, %2\n\ts_nop 0\n\tglobal_load_lds_dword %0, %1 sc1" ::"v"(((lane >> 1) & 15) * FS + (lane & 1) * 4), "s"(uniform_ptr(flg2)), "s"(ldsb + G2_POLL) : "memory");
      } else if (t == tp + 4) {
        if (!__all(*(LAS u64*)(lds + G2_POLL + i16 * 8) == tag)) wait_flags(flg2 + i16 * FS, tag);
      }
    }
    if (t == 52 - KSPLIT && split == 1) wb = uniform_ptr((const char*)wo - (size_t)(56 - KSPLIT) * 64 * D_ * 2);
    step(IC<0>{}, V8, Y, Y, t); step(IC<1>{}, V8, Y, Y, t + 1); step(IC<2>{}, V8, Y, Y, t + 2); step(IC<3>{}, V8, Y, Y, t + 3);
  }
  step(IC<0>{}, V8, N_, Y, 0);
  step(IC<1>{}, IC<4>{}, N_, Y, 0);
  step(IC<2>{}, IC<0>{}, N_, Y, 0);
  step(IC<3>{}, IC<0>{}, N_, N_, 0);
  STAMP(1, 2);
  const int quad = wid & 3;
  {
    LAS char* pp = lds + (quad * 2 + kh) * 8192 + lane * 16;
#pragma unroll
    for (int mm = 0; mm < 2; ++mm)
#pragma unroll
      for (int n = 0; n < 4; ++n) *(LAS f32x4*)(pp + (mm * 4 + n) * 1024) = kh ? acc[mm][n] : acc[2 + mm][n];
  }
  const bool keep = (wr == split);
  const int row0 = pm * 128 + wr * 64 + kh * 32 + i16, col0 = pn * 128 + wc * 64 + 4 * g;
  u32x2 xv[2][4], bv[4];
  if (keep) {
#pragma unroll
    for (int n = 0; n < 4; ++n) bv[n] = *(const u32x2*)(b2 + col0 + n * 16);
#pragma unroll
    for (int mm = 0; mm < 2; ++mm)
#pragma unroll
      for (int n = 0; n < 4; ++n) xv[mm][n] = *(const u32x2*)(x + (size_t)(row0 + mm * 16) * D_ + col0 + n * 16);
  }
  WAIT_L(0); BAR();
  f32x4 sum[2][4];
  {
    LAS char* pp = lds + (quad * 2 + (kh ^ 1)) * 8192 + lane * 16;
#pragma unroll
    for (int mm = 0; mm < 2; ++mm)
#pragma unroll
      for (int n = 0; n < 4; ++n) sum[mm][n] = (kh ? acc[2 + mm][n] : acc[mm][n]) + *(LAS f32x4*)(pp + (mm * 4 + n) * 1024);
  }
  const int piece = kh * 2 + wc;
  if (!keep) {
    char* dst = xch + (size_t)b * 16384 + piece * 4096 + lane * 16;
#pragma unroll
    for (int mm = 0; mm < 2; ++mm)
#pragma unroll
      for (int h = 0; h < 2; ++h)
        st_sc1(dst + (mm * 2 + h) * 1024, (u32x4){pk2(sum[mm][2 * h][0], sum[mm][2 * h][1]), pk2(sum[mm][2 * h][2], sum[mm][2 * h][3]),
                                                  pk2(sum[mm][2 * h + 1][0], sum[mm][2 * h + 1][1]), pk2(sum[mm][2 * h + 1][2], sum[mm][2 * h + 1][3])});
    if (b == 0 && wid == 0) {
#pragma unroll
      for (int j = 0; j < 4; ++j) wait_flags(ws + WS_FLG + (j * 64 + lane) * FS, tag);
      if (lane == 0) __hip_atomic_store((gu64*)(ws + WS_EPOCH), tag, __ATOMIC_RELAXED, __HIP_MEMORY_SCOPE_AGENT);
    }
  } else {
    const char* src = xch + (size_t)(b ^ 1) * 16384 + piece * 4096 + lane * 16;
    u32x4 pv[4];
    for (unsigned spins = 0;; ++spins) {
      asm volatile("global_load_dwordx4 %0, %1, off offset:3072 sc1\n\ts_waitcnt vmcnt(0)" : "=&v"(pv[3]) : "v"(src) : "memory");
      if (__all(fresh4(pv[3])) || spins > (1u << 14)) break;
      __builtin_amdgcn_s_sleep(1);
    }
    for (unsigned spins = 0;; ++spins) {
      asm volatile(
          "global_load_dwordx4 %0, %3, off sc1\n\tglobal_load_dwordx4 %1, %3, off offset:1024 sc1\n\t"
          "global_load_dwordx4 %2, %3, off offset:2048 sc1\n\ts_waitcnt vmcnt(0)"
          : "=&v"(pv[0]), "=&v"(pv[1]), "=&v"(pv[2]) : "v"(src) : "memory");
      if (__all(fresh4(pv[0]) && fresh4(pv[1]) && fresh4(pv[2])) || spins > (1u << 14)) break;
      __builtin_amdgcn_s_sleep(1);
    }
#pragma unroll
    for (int mm = 0; mm < 2; ++mm)
#pragma unroll
      for (int n = 0; n < 4; ++n) {
        const unsigned q0 = pv[mm * 2 + (n >> 1)][2 * (n & 1)], q1 = pv[mm * 2 + (n >> 1)][2 * (n & 1) + 1];
        const f32x4 v = sum[mm][n];
        u32x2 o;
        o[0] = pk2(v[0] + blo(q0) + blo(xv[mm][n][0]) + blo(bv[n][0]), v[1] + bhi(q0) + bhi(xv[mm][n][0]) + bhi(bv[n][0]));
        o[1] = pk2(v[2] + blo(q1) + blo(xv[mm][n][1]) + blo(bv[n][1]), v[3] + bhi(q1) + bhi(xv[mm][n][1]) + bhi(bv[n][1]));
        *(u32x2*)(out + (size_t)(row0 + mm * 16) * D_ + col0 + n * 16) = o;
      }
  }
  STAMP(1, 3);
  STAMP(1, 4);
}

__global__ __launch_bounds__(512, 2) void k_main(const u16* __restrict__ x, const u16* __restrict__ wqkv, const u16* __restrict__ w1,
                                                 const u16* __restrict__ b1, const u16* __restrict__ wphi,
                                                 const u16* __restrict__ w2, const u16* __restrict__ wo, const u16* __restrict__ b2,
                                                 char* ws, u16* __restrict__ out) {
  extern __shared__ __attribute__((aligned(16))) char smem[];
  LAS char* lds = (LAS char*)smem;
  const int b = blockIdx.x, grp = b & 7, idx = b >> 3;
  const int panel = idx & 7, kind = grp & 1, tcol = (grp >> 1) * 4 + (idx >> 3);
  u64 tag;
  if (kind == 0) {
    tag = gemm1_body<true>(x, wqkv, tcol * 64, 0, panel, tcol, b1, wphi, ws, w2, wo, lds);
  } else {
    const int c0 = tcol * 224, woff = c0 & 63;
    tag = gemm1_body<false>(x, w1, c0 - woff, woff, panel, tcol, b1, wphi, ws, w2, wo, lds);
  }
  gemm2_body(ws, w2, wo, x, b2, out, tag, kind * 16 + tcol,   kind ^ 1, panel, panel * 2 + ((grp >> 1) & 1), (grp >> 2) * 4 + (idx >> 3), lds);
}

extern "C" void kernel_launch(void* const* d_in, const int* in_sizes, int n_in, void* d_out, int out_size, void* d_ws, size_t ws_size,
                              hipStream_t stream) {
  (void)hipFuncSetAttribute((const void*)k_main, hipFuncAttributeMaxDynamicSharedMemorySize, G1_LDS);
  k_main<<<256, 512, G1_LDS, stream>>>((const u16*)d_in[0], (const u16*)d_in[1], (const u16*)d_in[5], (const u16*)d_in[6], (const u16*)d_in[2],
                                       (const u16*)d_in[7], (const u16*)d_in[3], (const u16*)d_in[8], (char*)d_ws, (u16*)d_out);
}
```

```cpp
#include <hip/hip_runtime.h>
#include <hip/hip_bf16.h>
#include <stdint.h>

typedef __bf16 bf16x8 __attribute__((ext_vector_type(8)));
typedef __bf16 bf16x4 __attribute__((ext_vector_type(4)));
typedef __bf16 bf16x2 __attribute__((ext_vector_type(2)));
typedef float f32x4 __attribute__((ext_vector_type(4)));
typedef float f32x2 __attribute__((ext_vector_type(2)));
typedef unsigned u32x4 __attribute__((ext_vector_type(4)));
typedef unsigned u32x2 __attribute__((ext_vector_type(2)));
typedef unsigned short u16;
#define LAS __attribute__((address_space(3)))
#define DI __device__ __forceinline__

constexpr int S_ = 2048, D_ = 1024, F_ = 3584, H_ = 32;
constexpr int KCAT = F_ + D_;
constexpr int NKG1 = D_ / 32;
constexpr int NKG2 = KCAT / 32;

constexpr size_t WS_XN   = 0;
constexpr size_t WS_ACT  = WS_XN + (size_t)S_ * D_ * 2;
constexpr size_t WS_KVX  = WS_ACT + (size_t)S_ * KCAT * 2;
constexpr int KVX_TILE = 8192 + 256;
constexpr size_t KVX_BYTES = (size_t)8 * 16 * KVX_TILE;
constexpr size_t WS_XCH  = WS_KVX + KVX_BYTES;
constexpr size_t WS_FLG  = WS_XCH + (size_t)256 * 16384;
constexpr int FS = 8;
constexpr size_t WS_FLG2 = WS_FLG + 256 * FS, WS_EPOCH = WS_FLG + 512 * FS + 128;

DI float rbf(float x) { return (float)(__bf16)x; }
DI unsigned pk2(float lo, float hi) { bf16x2 v; v[0] = (__bf16)lo; v[1] = (__bf16)hi; return __builtin_bit_cast(unsigned, v); }
DI float blo(unsigned u) { return __uint_as_float(u << 16); }
DI float bhi(unsigned u) { return __uint_as_float(u & 0xffff0000u); }

#define MFMA16(a, b, c) __builtin_amdgcn_mfma_f32_16x16x32_bf16((a), (b), (c), 0, 0, 0)
#define WAIT_V(n) asm volatile("s_waitcnt vmcnt(%0)" ::"n"(n) : "memory")
#define WAIT_L(n) do { static_assert((n) == 0, ""); asm volatile("" ::: "memory"); __builtin_amdgcn_s_waitcnt(0xC07F); asm volatile("" ::: "memory"); } while (0)
#define BAR() do { asm volatile("" ::: "memory"); __builtin_amdgcn_s_barrier(); asm volatile("" ::: "memory"); } while (0)
#define SCHED() __builtin_amdgcn_sched_barrier(0)
template <int N> struct IC { static constexpr int v = N; };

DI void glds16_sc1(unsigned voff, const void* sbase, unsigned ldsaddr) {
  asm volatile("s_mov_b32 m0, %2\n\ts_nop 0\n\tglobal_load_lds_dwordx4 %0, %1 sc1" ::"v"(voff), "s"(sbase), "s"(ldsaddr) : "memory");
}
DI void glds16(unsigned voff, const void* sbase, unsigned ldsaddr) {
  asm volatile("s_mov_b32 m0, %2\n\ts_nop 0\n\tglobal_load_lds_dwordx4 %0, %1" ::"v"(voff), "s"(sbase), "s"(ldsaddr) : "memory");
}
DI const char* uniform_ptr(const char* p) {
  const unsigned long long a = (unsigned long long)p;
  unsigned lo = __builtin_amdgcn_readfirstlane((unsigned)a), hi = __builtin_amdgcn_readfirstlane((unsigned)(a >> 32));
  asm volatile("s_nop 4" : "+s"(lo), "+s"(hi));
  return (const char*)(((unsigned long long)hi << 32) | lo);
}
DI LAS char* opaque(LAS char* p) { asm volatile("" : "+v"(p)); return p; }
DI bf16x8 lds_b128(LAS char* p) { return *(LAS bf16x8*)p; }
DI bf16x4 lds_tr(LAS char* p) { return __builtin_amdgcn_ds_read_tr16_b64_v4bf16((LAS bf16x4*)p); }
DI bf16x8 cat8(bf16x4 a, bf16x4 b) { return __builtin_shufflevector(a, b, 0, 1, 2, 3, 4, 5, 6, 7); }
DI bf16x8 pack8(f32x4 a, f32x4 b) {
  bf16x8 v;
  v[0] = (__bf16)a[0]; v[1] = (__bf16)a[1]; v[2] = (__bf16)a[2]; v[3] = (__bf16)a[3];
  v[4] = (__bf16)b[0]; v[5] = (__bf16)b[1]; v[6] = (__bf16)b[2]; v[7] = (__bf16)b[3];
  return v;
}

typedef __attribute__((address_space(1))) unsigned gu32;
typedef unsigned long long u64;
typedef __attribute__((address_space(1))) u64 gu64;
DI void st_sc1(void* p, u32x4 v) { asm volatile("global_store_dwordx4 %0, %1, off sc1\n\ts_nop 1" ::"v"(p), "v"(v) : "memory"); }
DI unsigned umax4(u32x4 v, unsigned m) { return max(max(max(v[0], v[1]), v[2]), max(v[3], m)); }
DI void st8_sc1(void* p, u32x2 v) { asm volatile("global_store_dwordx2 %0, %1, off sc1\n\ts_nop 1" ::"v"(p), "v"(v) : "memory"); }
DI bool fresh4(u32x4 v) { return umax4(v, 0u) != 0xffffffffu; }
#define XRED4(OP, a1, b1, a2, b2) asm volatile( \
    "s_nop 1\n\tv_permlane16_swap_b32 %0, %1\n\tv_permlane16_swap_b32 %2, %3\n\t" \
    OP " %0, %0, %1\n\t" OP " %2, %2, %3\n\tv_mov_b32 %1, %0\n\tv_mov_b32 %3, %2\n\ts_nop 0\n\t" \
    "v_permlane32_swap_b32 %0, %1\n\tv_permlane32_swap_b32 %2, %3\n\t" \
    OP " %0, %0, %1\n\t" OP " %2, %2, %3\n\tv_mov_b32 %1, %0\n\tv_mov_b32 %3, %2\n\ts_nop 0\n\t" \
    "v_permlane16_swap_b32 %0, %1\n\tv_permlane16_swap_b32 %2, %3" \
    : "+v"(a1), "+v"(b1), "+v"(a2), "+v"(b2))
DI float max8(const float* v) {
  const float t1 = fmaxf(fmaxf(v[0], v[1]), v[2]), t2 = fmaxf(fmaxf(v[3], v[4]), v[5]), t3 = fmaxf(fmaxf(v[6], v[7]), t1);
  return fmaxf(t2, t3);
}
DI float min8(const float* v) {
  const float t1 = fminf(fminf(v[0], v[1]), v[2]), t2 = fminf(fminf(v[3], v[4]), v[5]), t3 = fminf(fminf(v[6], v[7]), t1);
  return fminf(t2, t3);
}
DI void dual_softmax2(const f32x4 (&p)[2][2], u32x4 (&pos)[2], u32x4 (&neg)[2]) {
  float v[2][8];
#pragma unroll
  for (int t = 0; t < 2; ++t)
#pragma unroll
    for (int h = 0; h < 2; ++h)
#pragma unroll
      for (int r = 0; r < 2; ++r) {
        unsigned u = pk2(p[t][h][2 * r], p[t][h][2 * r + 1]);
        asm("" : "+v"(u));
        v[t][4 * h + 2 * r] = blo(u); v[t][4 * h + 2 * r + 1] = bhi(u);
      }
  float mx0 = max8(v[0]), nm0 = -min8(v[0]), mx1 = max8(v[1]), nm1 = -min8(v[1]);
  XRED4("v_max_f32", mx0, nm0, mx1, nm1);
  constexpr float L2E = 1.44269504f;
  const f32x2 kp = {L2E, L2E}, kn = {-L2E, -L2E};
  f32x2 ep[2][4], en[2][4];
  float sp[2], sn[2];
#pragma unroll
  for (int t = 0; t < 2; ++t) {
    const float cpv = t ? mx1 : mx0, cnv = t ? nm1 : nm0;
    const f32x2 cp = {cpv, cpv}, cn = {cnv, cnv};
#pragma unroll
    for (int r = 0; r < 4; ++r) {
      const f32x2 x = {v[t][2 * r], v[t][2 * r + 1]};
      const f32x2 ap = (x - cp) * kp, an = (x + cn) * kn;
      ep[t][r] = (f32x2){__builtin_amdgcn_exp2f(ap[0]), __builtin_amdgcn_exp2f(ap[1])};
      en[t][r] = (f32x2){__builtin_amdgcn_exp2f(an[0]), __builtin_amdgcn_exp2f(an[1])};
    }
    const f32x2 s2p = (ep[t][0] + ep[t][1]) + (ep[t][2] + ep[t][3]), s2n = (en[t][0] + en[t][1]) + (en[t][2] + en[t][3]);
    sp[t] = s2p[0] + s2p[1]; sn[t] = s2n[0] + s2n[1];
  }
  XRED4("v_add_f32", sp[0], sn[0], sp[1], sn[1]);
#pragma unroll
  for (int t = 0; t < 2; ++t) {
    const float rp = __builtin_amdgcn_rcpf(sp[t]), rn = __builtin_amdgcn_rcpf(sn[t]);
    const f32x2 rp2 = {rp, rp}, rn2 = {rn, rn};
#pragma unroll
    for (int r = 0; r < 4; ++r) {
      const f32x2 a = ep[t][r] * rp2, b = en[t][r] * rn2;
      pos[t][r] = pk2(a[0], a[1]); neg[t][r] = pk2(b[0], b[1]);
    }
  }
}

DI float sum8lanes(float v) {
  v += __builtin_bit_cast(float, __builtin_amdgcn_update_dpp(0, __builtin_bit_cast(int, v), 0xB1, 0xf, 0xf, true));
  v += __builtin_bit_cast(float, __builtin_amdgcn_update_dpp(0, __builtin_bit_cast(int, v), 0x4E, 0xf, 0xf, true));
  v += __builtin_bit_cast(float, __builtin_amdgcn_update_dpp(0, __builtin_bit_cast(int, v), 0x141, 0xf, 0xf, true));
  return v;
}
DI void wait_flags(const char* p, u64 tag) {
  for (unsigned spins = 0; spins < (1u << 16); ++spins) {
    if (__all(__hip_atomic_load((const gu64*)p, __ATOMIC_RELAXED, __HIP_MEMORY_SCOPE_AGENT) == tag)) break;
    __builtin_amdgcn_s_sleep(1);
  }
}

constexpr int G1_SLOT = 16384;
constexpr int G1_B = 65536, G1_X = 131072;
constexpr int G1_LDS = G1_X + 4096 + 2048;

DI f32x2 gelu2(f32x2 x) {
  const f32x2 c1 = {-0.10294324f, -0.10294324f}, c0 = {-2.3022082f, -2.3022082f}, one = {1.0f, 1.0f};
  const f32x2 a = x * __builtin_elementwise_fma(x * x, c1, c0);
  const f32x2 d = one + (f32x2){__builtin_amdgcn_exp2f(a[0]), __builtin_amdgcn_exp2f(a[1])};
  return x * (f32x2){__builtin_amdgcn_rcpf(d[0]), __builtin_amdgcn_rcpf(d[1])};
}

template <bool QKV>
DI u64 gemm1_body(const u16* __restrict__ x, const u16* __restrict__ W, int wcol0, int woff, int panel, int tcol,
                  const u16* __restrict__ b1, const u16* __restrict__ wphi, char* ws,
                  const u16* __restrict__ w2, const u16* __restrict__ wo, LAS char* lds) {
  char* xn = ws + WS_XN;
  char* act = ws + WS_ACT;
  char* kvx = ws + WS_KVX;
  constexpr int LDW = QKV ? 3 * D_ : F_;
  constexpr int NG = QKV ? 3 : 4;
  constexpr int NT = QKV ? 6 : 7;
  const int tid = threadIdx.x, lane = tid & 63;
  const int wid = __builtin_amdgcn_readfirstlane(tid >> 6);
  const int wr = wid >> 1, wc = wid & 1;
  const int g = lane >> 4, i16 = lane & 15, q4 = i16 >> 2, p4 = lane & 3;
  const unsigned ldsb = (unsigned)(size_t)lds;

  const char* abase = uniform_ptr(xn + (size_t)panel * 16 * NKG1 * 1024);
  const unsigned avoff = (unsigned)(wid * 2) * NKG1 * 1024 + lane * 16;
  const char* bbase = (const char*)W;
  unsigned bvoff[2]; unsigned bdst[2];
#pragma unroll
  for (int j = 0; j < 2; ++j) {
    int pb = QKV ? (wid + 8 * j) : (wid * 2 + j);
    if (QKV && pb >= 12) pb -= 8;
    const int kg = pb / NG, cg = pb % NG;
    const int r = lane >> 3, sw = ((r >> 1) & 1) | ((kg & 1) << 1);
    const int cc = (lane & 7) ^ (sw << 1);
    const int col = QKV ? (cg * D_ + wcol0) : (wcol0 + cg * 64);
    bvoff[j] = (unsigned)(((kg * 8 + r) * LDW + col + cc * 8) * 2);
    bdst[j] = ldsb + G1_B + pb * 1024;
  }
  const unsigned adst = ldsb + wid * 2048;

  LAS char* ard = opaque(lds + wr * 4096 + lane * 16);
  constexpr int NB = QKV ? 2 : 4;
  LAS char* brd[NB];
#pragma unroll
  for (int j = 0; j < NB; ++j) {
    const int n0 = QKV ? (wc * 32 + j * 16) : (woff + wc * 112 + j * 16);
    const int sw = (q4 >> 1) | ((g & 1) << 1);
    brd[j] = opaque(lds + G1_B + (g * NG + (n0 >> 6)) * 1024 + q4 * 128 + (((((n0 & 63) >> 3) ^ (sw << 1)) + (p4 >> 1)) << 4) + (p4 & 1) * 8);
  }

  f32x4 acc[4][NT];
#pragma unroll
  for (int m = 0; m < 4; ++m)
#pragma unroll
    for (int n = 0; n < NT; ++n) acc[m][n] = (f32x4){0.f, 0.f, 0.f, 0.f};

  bf16x8 fa[2][4], fb[2][NT];

  auto issue = [&](int q, int stage, int slot) {
    if (q < 2) glds16_sc1(avoff + q * NKG1 * 1024, abase + (size_t)stage * 1024, adst + q * 1024 + slot * G1_SLOT);
    else       glds16(bvoff[q - 2], bbase + (size_t)stage * 64 * LDW, bdst[q - 2] + slot * G1_SLOT);
  };
  auto rdA = [&](auto P, auto SL, int m) { fa[P.v][m] = lds_b128(ard + SL.v * G1_SLOT + m * 1024); };
  auto rdB = [&](auto P, auto SL, int n) { LAS char* p = brd[n % NB] + (n / NB) * 1024 + SL.v * G1_SLOT; fb[P.v][n] = cat8(lds_tr(p), lds_tr(p + 512)); };
  auto mma4 = [&](auto P, int n) {
#pragma unroll
    for (int m = 0; m < 4; ++m) {
      if (QKV && n >= 4) acc[m][n] = MFMA16(fa[P.v][m], fb[P.v][n], acc[m][n]);
      else               acc[m][n] = MFMA16(fb[P.v][n], fa[P.v][m], acc[m][n]);
    }
  };
  auto step = [&](auto SC, auto VM, auto GL, auto RD, int s) {
    constexpr int P = SC.v & 1, SNv = (SC.v + 1) & 3;
    const IC<P> Pc; const IC<P ^ 1> Pn; const IC<SNv> SN;
    if constexpr (RD.v) { WAIT_V(VM.v); WAIT_L(0); BAR(); }
    SCHED();
    mma4(Pc, 0); SCHED();
    if constexpr (RD.v) { rdA(Pn, SN, 0); rdA(Pn, SN, 1); rdB(Pn, SN, 0); }
    if constexpr (GL.v) issue(0, s + 4, SC.v);
    SCHED();
    mma4(Pc, 1); SCHED();
    if constexpr (RD.v) { rdA(Pn, SN, 2); rdA(Pn, SN, 3); rdB(Pn, SN, 1); }
    if constexpr (GL.v) issue(1, s + 4, SC.v);
    SCHED();
    mma4(Pc, 2); SCHED();
    if constexpr (RD.v) { rdB(Pn, SN, 2); rdB(Pn, SN, 3); }
    if constexpr (GL.v) issue(2, s + 4, SC.v);
    SCHED();
    mma4(Pc, 3); SCHED();
    if constexpr (RD.v) { rdB(Pn, SN, 4); rdB(Pn, SN, 5); if constexpr (NT == 7) rdB(Pn, SN, 6); }
    if constexpr (GL.v) issue(3, s + 4, SC.v);
    SCHED();
    mma4(Pc, 4); SCHED();
    mma4(Pc, 5);
    if constexpr (NT == 7) mma4(Pc, 6);
    SCHED();
  };

  const int slot = (QKV ? 0 : 16) + tcol;
  u64 tag;
  {
    const int r8 = lane >> 3, c8 = lane & 7;
    const int row = panel * 256 + slot * 8 + r8;
    const u16* src = x + (size_t)row * D_ + wid * 128 + c8 * 8;
    u32x4 v0, v1; u32x2 ep;
    asm volatile("global_load_dwordx4 %0, %3, off\n\tglobal_load_dwordx4 %1, %3, off offset:128\n\tglobal_load_dwordx2 %2, %4, off sc1"
                 : "=&v"(v0), "=&v"(v1), "=&v"(ep) : "v"(src), "v"(ws + WS_EPOCH) : "memory");
    SCHED();
    if (QKV) {
      const u32x4 fill = {~0u, ~0u, ~0u, ~0u};
      char* kp = kvx + (size_t)(panel * 16 + tcol) * KVX_TILE;
      st_sc1(kp + tid * 16, fill);
      st_sc1(kp + 8192 + i16 * 16, fill);
    }
    SCHED();
    asm volatile("s_waitcnt vmcnt(%3)" : "+v"(v0), "+v"(v1), "+v"(ep) : "n"(QKV ? 2 : 0) : "memory");
    SCHED();
    tag = (((u64)(unsigned)__builtin_amdgcn_readfirstlane(ep[1]) << 32) | (unsigned)__builtin_amdgcn_readfirstlane(ep[0])) + 1;
    f32x2 f[8];
#pragma unroll
    for (int i = 0; i < 4; ++i) { f[i] = (f32x2){blo(v0[i]), bhi(v0[i])}; f[4 + i] = (f32x2){blo(v1[i]), bhi(v1[i])}; }
    const f32x2 s2 = ((f[0] + f[1]) + (f[2] + f[3])) + ((f[4] + f[5]) + (f[6] + f[7]));
    const float mw = sum8lanes(s2[0] + s2[1]) * (1.0f / 128.f);
    f32x2 q2 = {0.f, 0.f};
#pragma unroll
    for (int i = 0; i < 8; ++i) { const f32x2 d = f[i] - (f32x2){mw, mw}; q2 = __builtin_elementwise_fma(d, d, q2); }
    const float qq = sum8lanes(q2[0] + q2[1]);
    LAS f32x2* stt = (LAS f32x2*)(lds + G1_X + 4096);
    if (c8 == 0) stt[r8 * 8 + wid] = (f32x2){mw, qq};
    WAIT_L(0); BAR();
    const f32x2 pw = stt[r8 * 8 + c8];
    const float mean = sum8lanes(pw[0]) * 0.125f;
    const float dm = pw[0] - mean;
    const float m2 = sum8lanes(__builtin_fmaf(128.f * dm, dm, pw[1]));
    const float mb = rbf(mean), vb = rbf(m2 * (1.0f / D_));
    const float rs = 1.0f / rbf(sqrtf(rbf(vb + 1e-5f)));
    u32x4 o0, o1;
#pragma unroll
    for (int i = 0; i < 8; ++i) {
      const f32x2 d = f[i] - (f32x2){mb, mb};
      unsigned u = pk2(d[0], d[1]);
      asm("" : "+v"(u));
      const f32x2 y = (f32x2){blo(u), bhi(u)} * (f32x2){rs, rs};
      if (i < 4) o0[i] = pk2(y[0], y[1]); else o1[i - 4] = pk2(y[0], y[1]);
    }
    char* dst = xn + ((size_t)((row >> 4) * NKG1 + wid * 4 + (c8 >> 2)) * 64 + (c8 & 3) * 16 + (row & 15)) * 16;
    st_sc1(dst, o0); st_sc1(dst + 2048, o1);
    WAIT_V(0); BAR();
    if (tid == 0) __hip_atomic_store((gu64*)(ws + WS_FLG + (panel * 32 + slot) * FS), tag, __ATOMIC_RELAXED, __HIP_MEMORY_SCOPE_AGENT);
    if (QKV && wid >= 4) glds16((unsigned)lane * 16, uniform_ptr((const char*)wphi + (size_t)tcol * 4096 + (wid - 4) * 1024), ldsb + G1_X + (wid - 4) * 1024);
#pragma unroll
    for (int st = 0; st < 4; ++st) { issue(2, st, st); issue(3, st, st); }
    if (wid == 0) wait_flags(ws + WS_FLG + ((QKV && lane >= 32) ? ((lane & 7) * 32 + slot) : (panel * 32 + (lane & 31))) * FS, tag);
    BAR();
  }
#pragma unroll
  for (int st = 0; st < 4; ++st) { issue(0, st, st); issue(1, st, st); }
  WAIT_V(2); BAR();
  {
    const IC<0> P0; const IC<0> S0;
#pragma unroll
    for (int m = 0; m < 4; ++m) rdA(P0, S0, m);
#pragma unroll
    for (int n = 0; n < NT; ++n) rdB(P0, S0, n);
  }
#pragma unroll 1
  for (int s = 0; s < 28; s += 4) {
    step(IC<0>{}, IC<8>{}, IC<1>{}, IC<1>{}, s);
    step(IC<1>{}, IC<8>{}, IC<1>{}, IC<1>{}, s + 1);
    step(IC<2>{}, IC<8>{}, IC<1>{}, IC<1>{}, s + 2);
    step(IC<3>{}, IC<8>{}, IC<1>{}, IC<1>{}, s + 3);
  }
  step(IC<0>{}, IC<8>{}, IC<0>{}, IC<1>{}, 28);
  constexpr int NX = 3;
  {
    int wv = blockIdx.x * 8 + wid;
    if (wv >= 1152) wv -= 1152;
    const char* src = uniform_ptr(wv < 896 ? (const char*)w2 + (size_t)wv * 8192 : (const char*)wo + (size_t)(wv - 896) * 8192);
    asm volatile("s_mov_b32 m0, %2\n\ts_nop 0\n\tglobal_load_lds_dword %0, %1" ::"v"(lane * 128), "s"(src), "s"(ldsb + G1_X + 4096 + wid * 256) : "memory");
    char* xp = ws + WS_XCH + (size_t)blockIdx.x * 16384 + tid * 16;
    const u32x4 fill = {~0u, ~0u, ~0u, ~0u};
    st_sc1(xp, fill); st_sc1(xp + 8192, fill);
  }
  step(IC<1>{}, IC<4 + NX>{}, IC<0>{}, IC<1>{}, 29);
  step(IC<2>{}, IC<0 + NX>{}, IC<0>{}, IC<1>{}, 30);
  step(IC<3>{}, IC<0>{}, IC<0>{}, IC<0>{}, 31);

  if (!QKV) {
    const int ncol0 = tcol * 224 + wc * 112 + 4 * g;
    u32x2 bb[NT];
#pragma unroll
    for (int n = 0; n < NT; ++n) bb[n] = *(const u32x2*)(b1 + ncol0 + n * 16);
    auto act4 = [&](int m, int n) {
      const f32x2 yA = gelu2((f32x2){acc[m][n][0] + blo(bb[n][0]), acc[m][n][1] + bhi(bb[n][0])});
      const f32x2 yB = gelu2((f32x2){acc[m][n][2] + blo(bb[n][1]), acc[m][n][3] + bhi(bb[n][1])});
      return (u32x2){pk2(yA[0], yA[1]), pk2(yB[0], yB[1])};
    };
    const int rt0 = panel * 16 + wr * 4;
    char* obase = (char*)act + ((size_t)rt0 * NKG2 + tcol * 7) * 1024 + lane * 16;
    if (wc == 0) {
#pragma unroll
      for (int m = 0; m < 4; ++m) {
        char* rb = obase + (size_t)m * NKG2 * 1024;
#pragma unroll
        for (int p = 0; p < 3; ++p) { const u32x2 lo = act4(m, 2 * p), hi = act4(m, 2 * p + 1); st_sc1(rb + p * 1024, (u32x4){lo[0], lo[1], hi[0], hi[1]}); }
        st8_sc1(rb + 3 * 1024, act4(m, 6 % NT));
      }
    } else {
#pragma unroll
      for (int m = 0; m < 4; ++m) {
        char* rb = obase + (size_t)m * NKG2 * 1024;
        st8_sc1(rb + 3 * 1024 + 8, act4(m, 0));
#pragma unroll
        for (int p = 0; p < 3; ++p) { const u32x2 lo = act4(m, 2 * p + 1), hi = act4(m, (2 * p + 2) % NT); st_sc1(rb + (4 + p) * 1024, (u32x4){lo[0], lo[1], hi[0], hi[1]}); }
      }
    }
  } else {
    const int head = tcol * 2 + wc;
    LAS char* wp = lds + G1_X + wc * 2048;
    bf16x8 wf[2];
#pragma unroll
    for (int ft = 0; ft < 2; ++ft) {
      LAS char* p = wp + (4 * g + q4) * 64 + (16 * ft + 4 * p4) * 2;
      wf[ft] = cat8(lds_tr(p), lds_tr(p + 16 * 64));
    }
    const f32x4 z4 = {0.f, 0.f, 0.f, 0.f};
    LAS char* sc = lds + (wid >> 2) * G1_B + (wid & 3) * 8704;
#pragma unroll
    for (int mp = 0; mp < 2; ++mp) {
      f32x4 pp[2][2]; u32x4 pos[2], neg[2];
#pragma unroll
      for (int t = 0; t < 2; ++t) {
        const bf16x8 bk = pack8(acc[2 * mp + t][2], acc[2 * mp + t][3]);
        pp[t][0] = MFMA16(wf[0], bk, z4); pp[t][1] = MFMA16(wf[1], bk, z4);
      }
      dual_softmax2(pp, pos, neg);
#pragma unroll
      for (int t = 0; t < 2; ++t) {
        LAS char* rowp = sc + (16 * (2 * mp + t) + i16) * 136 + 8 * g;
        *(LAS u32x2*)(rowp) = (u32x2){pos[t][0], pos[t][1]};      *(LAS u32x2*)(rowp + 32) = (u32x2){pos[t][2], pos[t][3]};
        *(LAS u32x2*)(rowp + 64) = (u32x2){neg[t][0], neg[t][1]}; *(LAS u32x2*)(rowp + 96) = (u32x2){neg[t][2], neg[t][3]};
      }
    }
    WAIT_L(0);
    f32x4 kv[4][3];
#pragma unroll
    for (int ft = 0; ft < 4; ++ft)
#pragma unroll
      for (int d = 0; d < 3; ++d) kv[ft][d] = z4;
    bf16x8 ones;
#pragma unroll
    for (int j = 0; j < 8; ++j) ones[j] = (__bf16)1.0f;
#pragma unroll
    for (int ks = 0; ks < 2; ++ks) {
      const bf16x8 bv0 = pack8(acc[2 * ks][4], acc[2 * ks + 1][4]);
      const bf16x8 bv1 = pack8(acc[2 * ks][5], acc[2 * ks + 1][5]);
#pragma unroll
      for (int ft = 0; ft < 4; ++ft) {
        LAS char* p = sc + (32 * ks + 4 * g + q4) * 136 + (16 * ft + 4 * p4) * 2;
        const bf16x8 a = cat8(lds_tr(p), lds_tr(p + 16 * 136));
        kv[ft][0] = MFMA16(a, bv0, kv[ft][0]);
        kv[ft][1] = MFMA16(a, bv1, kv[ft][1]);
        kv[ft][2] = MFMA16(a, ones, kv[ft][2]);
      }
    }
    WAIT_L(0); BAR();
    {
      LAS char* pp = lds + wid * 8448;
#pragma unroll
      for (int sg = 0; sg < 2; ++sg) {
#pragma unroll
        for (int d = 0; d < 2; ++d) {
          *(LAS f32x4*)(pp + (sg * 2 + d) * 2048 + lane * 16) = kv[2 * sg][d];
          *(LAS f32x4*)(pp + (sg * 2 + d) * 2048 + 1024 + lane * 16) = kv[2 * sg + 1][d];
        }
        if (i16 == 0) {
          *(LAS f32x4*)(pp + 8192 + sg * 128 + g * 16) = kv[2 * sg][2];
          *(LAS f32x4*)(pp + 8192 + sg * 128 + 64 + g * 16) = kv[2 * sg + 1][2];
        }
      }
    }
    WAIT_L(0); BAR();
    char* kvt = kvx + (size_t)tcol * KVX_TILE;
    char* kvmine = kvt + (size_t)panel * 16 * KVX_TILE;
    {
      LAS char* pp = lds + (tid >> 8) * 8448 + ((tid >> 6) & 3) * 2048 + lane * 16;
      f32x4 lo = *(LAS f32x4*)(pp), hi = *(LAS f32x4*)(pp + 1024);
#pragma unroll
      for (int w = 1; w < 4; ++w) { lo += *(LAS f32x4*)(pp + w * 16896); hi += *(LAS f32x4*)(pp + w * 16896 + 1024); }
      st_sc1(kvmine + tid * 16, (u32x4){pk2(lo[0], lo[1]), pk2(lo[2], lo[3]), pk2(hi[0], hi[1]), pk2(hi[2], hi[3])});
    }
    if (tid < 16) {
      LAS char* pp = lds + (tid >> 3) * 8448 + 8192 + ((tid >> 2) & 1) * 128 + (tid & 3) * 16;
      f32x4 lo = *(LAS f32x4*)(pp), hi = *(LAS f32x4*)(pp + 64);
#pragma unroll
      for (int w = 1; w < 4; ++w) { lo += *(LAS f32x4*)(pp + w * 16896); hi += *(LAS f32x4*)(pp + w * 16896 + 64); }
      st_sc1(kvmine + 8192 + tid * 16, (u32x4){pk2(lo[0], lo[1]), pk2(lo[2], lo[3]), pk2(hi[0], hi[1]), pk2(hi[2], hi[3])});
    }
    u32x4 qp[2][2], qn[2][2];
    auto qsm = [&](int mp) {
      f32x4 pp[2][2];
#pragma unroll
      for (int t = 0; t < 2; ++t) {
        const bf16x8 bq = pack8(acc[2 * mp + t][0], acc[2 * mp + t][1]);
        pp[t][0] = MFMA16(wf[0], bq, z4); pp[t][1] = MFMA16(wf[1], bq, z4);
      }
      dual_softmax2(pp, qp[mp], qn[mp]);
    };
    qsm(0);
    const char* kb0 = uniform_ptr(kvt), * kb1 = uniform_ptr(kvt + (size_t)32 * KVX_TILE);
    const char* kb2 = uniform_ptr(kvt + (size_t)64 * KVX_TILE), * kb3 = uniform_ptr(kvt + (size_t)96 * KVX_TILE);
    u32x4 pv[8], pz[8];
#define KV_GATHER(pv, OFF, TAIL) asm volatile( \
          "global_load_dwordx4 %0, %8, %10 sc1\n\tglobal_load_dwordx4 %1, %9, %10 sc1\n\t" \
          "global_load_dwordx4 %2, %8, %11 sc1\n\tglobal_load_dwordx4 %3, %9, %11 sc1\n\t" \
          "global_load_dwordx4 %4, %8, %12 sc1\n\tglobal_load_dwordx4 %5, %9, %12 sc1\n\t" \
          "global_load_dwordx4 %6, %8, %13 sc1\n\tglobal_load_dwordx4 %7, %9, %13 sc1" TAIL \
          : "=&v"(pv[0]), "=&v"(pv[1]), "=&v"(pv[2]), "=&v"(pv[3]), "=&v"(pv[4]), "=&v"(pv[5]), "=&v"(pv[6]), "=&v"(pv[7]) \
          : "v"(OFF), "v"((OFF) + 16 * KVX_TILE), "s"(kb0), "s"(kb1), "s"(kb2), "s"(kb3) : "memory")
#define KV_WAIT(pv) asm volatile("s_waitcnt vmcnt(0)" \
          : "+v"(pv[0]), "+v"(pv[1]), "+v"(pv[2]), "+v"(pv[3]), "+v"(pv[4]), "+v"(pv[5]), "+v"(pv[6]), "+v"(pv[7]) :: "memory")
    LAS char* kvs = lds + 98304;
    auto finish = [&](u32x4 (&pv)[8], unsigned off) {
      for (unsigned spins = 0;; ++spins) {
        unsigned m = 0;
#pragma unroll
        for (int p = 0; p < 8; ++p) m = umax4(pv[p], m);
        if (__all(m != 0xffffffffu) || spins > (1u << 14)) break;
        __builtin_amdgcn_s_sleep(2);
        KV_GATHER(pv, off, "\n\ts_waitcnt vmcnt(0)");
      }
      f32x2 t[4];
#pragma unroll
      for (int r = 0; r < 4; ++r) t[r] = (f32x2){blo(pv[0][r]), bhi(pv[0][r])};
#pragma unroll
      for (int p = 1; p < 8; ++p)
#pragma unroll
        for (int r = 0; r < 4; ++r) t[r] += (f32x2){blo(pv[p][r]), bhi(pv[p][r])};
      *(LAS u32x4*)(kvs + off) = (u32x4){pk2(t[0][0], t[0][1]), pk2(t[1][0], t[1][1]), pk2(t[2][0], t[2][1]), pk2(t[3][0], t[3][1])};
    };
    const unsigned go = (unsigned)tid * 16, gz = 8192u + (unsigned)i16 * 16;
    SCHED();
    KV_GATHER(pv, go, "");
    KV_GATHER(pz, gz, "");
    SCHED();
    qsm(1);
    SCHED();
    KV_WAIT(pv);
    KV_WAIT(pz);
    SCHED();
    finish(pv, go);
    if (wid == 0) finish(pz, gz);
    WAIT_L(0); BAR();
    bf16x8 a[2][3];
#pragma unroll
    for (int sg = 0; sg < 2; ++sg) {
#pragma unroll
      for (int d = 0; d < 2; ++d) a[sg][d] = lds_b128(kvs + wc * 4096 + (sg * 2 + d) * 1024 + lane * 16);
      a[sg][2] = lds_b128(kvs + 8192 + wc * 128 + sg * 64 + g * 16);
    }
#pragma unroll
    for (int m = 0; m < 4; ++m) {
      const bf16x8 bp = __builtin_bit_cast(bf16x8, qp[m >> 1][m & 1]), bn = __builtin_bit_cast(bf16x8, qn[m >> 1][m & 1]);
      f32x4 o0 = MFMA16(a[0][0], bp, z4), o1 = MFMA16(a[0][1], bp, z4), dn = MFMA16(a[0][2], bp, z4);
      o0 = MFMA16(a[1][0], bn, o0); o1 = MFMA16(a[1][1], bn, o1); dn = MFMA16(a[1][2], bn, dn);
      const float rd = __builtin_amdgcn_rcpf(dn[0] + 1e-6f);
      const int rt = panel * 16 + wr * 4 + m;
      u32x4 w;
      w[0] = pk2(o0[0] * rd, o0[1] * rd); w[1] = pk2(o0[2] * rd, o0[3] * rd);
      w[2] = pk2(o1[0] * rd, o1[1] * rd); w[3] = pk2(o1[2] * rd, o1[3] * rd);
      st_sc1((char*)act + ((size_t)rt * NKG2 + 112 + head) * 1024 + lane * 16, w);
    }
  }
  return tag;
}

constexpr int G2_SLOT = 32768;
constexpr int G2_POLL = 4 * G2_SLOT + 4096;
constexpr int KSPLIT = 36;

DI void gemm2_body(char* ws, const u16* __restrict__ w2, const u16* __restrict__ wo,
                   const u16* __restrict__ x, const u16* __restrict__ b2, u16* __restrict__ out,
                   u64 tag, int slot, int split, int panel, int pm, int pn, LAS char* lds) {
  const char* act = ws + WS_ACT;
  char* xch = ws + WS_XCH;
  char* flg2 = ws + WS_FLG2 + panel * 32 * FS;
  const unsigned ldsb = (unsigned)(size_t)lds;
  int lane = __builtin_amdgcn_mbcnt_hi(~0u, __builtin_amdgcn_mbcnt_lo(~0u, 0u));
  asm volatile("" : "+v"(lane));
  const int wid = __builtin_amdgcn_readfirstlane(threadIdx.x >> 6);
  const int kh = wid >> 2, wr = (wid >> 1) & 1, wc = wid & 1;
  const int g = lane >> 4, i16 = lane & 15, q4 = i16 >> 2, p4 = lane & 3;
  const int b = blockIdx.x;
  const int kt0 = split ? KSPLIT : 0, nst = split ? 72 - KSPLIT : KSPLIT;
  const char* abase = uniform_ptr(act + ((size_t)(pm * 8) * NKG2 + kt0 * 2) * 1024);
  const unsigned avoff = (unsigned)wid * NKG2 * 1024 + lane * 16;
  const unsigned adst = ldsb + wid * 1024;
  const int r8 = lane >> 3;
  const unsigned bvoff = (unsigned)((wid * 8 + r8) * D_ + pn * 128 + (((lane & 7) ^ (((r8 >> 1) & 3) << 1)) << 3)) * 2;
  const unsigned bdst = ldsb + 16384 + wid * 2048;
  const char* wb = uniform_ptr((const char*)w2 + (size_t)kt0 * 64 * D_ * 2);
  auto issue = [&](int q, int t, int slot) {
    if (q < 2) glds16_sc1(avoff + q * 1024, abase + (size_t)t * 2048, adst + q * 8192 + slot * G2_SLOT);
    else       glds16(bvoff + (q - 2) * 128, wb + (size_t)t * 64 * D_ * 2, bdst + (q - 2) * 1024 + slot * G2_SLOT);
  };
  WAIT_V(0); WAIT_L(0); BAR();
  if (threadIdx.x == 0) __hip_atomic_store((gu64*)(flg2 + slot * FS), tag, __ATOMIC_RELAXED, __HIP_MEMORY_SCOPE_AGENT);
  if (wid == panel) wait_flags(flg2 + (16 + (split ? 10 + (i16 < 6 ? i16 : 0) : (i16 < 11 ? i16 : 0))) * FS, tag);
#pragma unroll
  for (int st = 0; st < 4; ++st) { issue(2, st, st); issue(3, st, st); }
  BAR();
#pragma unroll
  for (int st = 0; st < 4; ++st) { issue(0, st, st); issue(1, st, st); }

  LAS char* ard[2];
  ard[0] = opaque(lds + kh * 8192 + wr * 4096 + lane * 16); ard[1] = opaque(ard[0] + 65536);
  LAS char* brd[2][4];
#pragma unroll
  for (int j = 0; j < 4; ++j) {
    const int r = 4 * (g & 1) + q4, sw = (r >> 1) & 3;
    brd[0][j] = opaque(lds + 16384 + ((kh * 4 + (g >> 1)) * 2 + wc) * 1024 + r * 128 + ((((2 * j) ^ (sw << 1)) + (p4 >> 1)) << 4) + (p4 & 1) * 8);
    brd[1][j] = opaque(brd[0][j] + 65536);
  }
  f32x4 acc[4][4];
#pragma unroll
  for (int m = 0; m < 4; ++m)
#pragma unroll
    for (int n = 0; n < 4; ++n) acc[m][n] = (f32x4){0.f, 0.f, 0.f, 0.f};
  bf16x8 fa[2][4], fb[2][4];
  auto rdA = [&](auto P, auto SL, int m) { fa[P.v][m] = lds_b128(ard[SL.v >> 1] + (SL.v & 1) * G2_SLOT + m * 1024); };
  auto rdB = [&](auto P, auto SL, int n) { LAS char* p = brd[SL.v >> 1][n] + (SL.v & 1) * G2_SLOT; fb[P.v][n] = cat8(lds_tr(p), lds_tr(p + 4096)); };
  auto mma4 = [&](auto P, int n) {
#pragma unroll
    for (int m = 0; m < 4; ++m) acc[m][n] = MFMA16(fb[P.v][n], fa[P.v][m], acc[m][n]);
  };
  auto step = [&](auto SC, auto VM, auto GL, auto RD, int t) {
    constexpr int P = SC.v & 1, SNv = (SC.v + 1) & 3;
    const IC<P> Pc; const IC<P ^ 1> Pn; const IC<SNv> SN;
    if constexpr (RD.v) { WAIT_V(VM.v); WAIT_L(0); BAR(); }
    SCHED();
    mma4(Pc, 0); SCHED();
    if constexpr (RD.v) { rdA(Pn, SN, 0); rdA(Pn, SN, 1); rdA(Pn, SN, 2); rdA(Pn, SN, 3); rdB(Pn, SN, 0); }
    if constexpr (GL.v) issue(0, t + 4, SC.v);
    SCHED();
    mma4(Pc, 1); SCHED();
    if constexpr (RD.v) { rdB(Pn, SN, 1); rdB(Pn, SN, 2); rdB(Pn, SN, 3); }
    if constexpr (GL.v) issue(1, t + 4, SC.v);
    SCHED();
    mma4(Pc, 2); SCHED();
    if constexpr (GL.v) { issue(2, t + 4, SC.v); issue(3, t + 4, SC.v); }
    SCHED();
    mma4(Pc, 3); SCHED();
  };
  WAIT_V(2); BAR();
  {
    const IC<0> P0; const IC<0> S0;
#pragma unroll
    for (int m = 0; m < 4; ++m) rdA(P0, S0, m);
#pragma unroll
    for (int n = 0; n < 4; ++n) rdB(P0, S0, n);
  }
  const IC<1> Y; const IC<0> N_; const IC<8> V8;
  const int tp = split ? 8 : 24;
#pragma unroll 1
  for (int t = 0; t < nst - 4; t += 4) {
    if (wid == 0) {
      if (t == tp) {
        asm volatile("s_mov_b32 m0, %2\n\ts_nop 0\n\tglobal_load_lds_dword %0, %1 sc1" ::"v"((lane >> 1) * FS + (lane & 1) * 4), "s"(uniform_ptr(flg2)), "s"(ldsb + G2_POLL) : "memory");
      } else if (t == tp + 4) {
        if (!__all(*(LAS u64*)(lds + G2_POLL + (lane & 31) * 8) == tag)) wait_flags(flg2 + (lane & 31) * FS, tag);
      }
    }
    if (t == 52 - KSPLIT && split == 1) wb = uniform_ptr((const char*)wo - (size_t)(56 - KSPLIT) * 64 * D_ * 2);
    step(IC<0>{}, V8, Y, Y, t); step(IC<1>{}, V8, Y, Y, t + 1); step(IC<2>{}, V8, Y, Y, t + 2); step(IC<3>{}, V8, Y, Y, t + 3);
  }
  step(IC<0>{}, V8, N_, Y, 0);
  step(IC<1>{}, IC<4>{}, N_, Y, 0);
  step(IC<2>{}, IC<0>{}, N_, Y, 0);
  step(IC<3>{}, IC<0>{}, N_, N_, 0);
  const int quad = wid & 3;
  {
    LAS char* pp = lds + (quad * 2 + kh) * 8192 + lane * 16;
#pragma unroll
    for (int mm = 0; mm < 2; ++mm)
#pragma unroll
      for (int n = 0; n < 4; ++n) *(LAS f32x4*)(pp + (mm * 4 + n) * 1024) = kh ? acc[mm][n] : acc[2 + mm][n];
  }
  const bool keep = (wr == split);
  const int row0 = pm * 128 + wr * 64 + kh * 32 + i16, col0 = pn * 128 + wc * 64 + 4 * g;
  u32x2 xv[2][4], bv[4];
  if (keep) {
#pragma unroll
    for (int n = 0; n < 4; ++n) bv[n] = *(const u32x2*)(b2 + col0 + n * 16);
#pragma unroll
    for (int mm = 0; mm < 2; ++mm)
#pragma unroll
      for (int n = 0; n < 4; ++n) xv[mm][n] = *(const u32x2*)(x + (size_t)(row0 + mm * 16) * D_ + col0 + n * 16);
  }
  WAIT_L(0); BAR();
  f32x4 sum[2][4];
  {
    LAS char* pp = lds + (quad * 2 + (kh ^ 1)) * 8192 + lane * 16;
#pragma unroll
    for (int mm = 0; mm < 2; ++mm)
#pragma unroll
      for (int n = 0; n < 4; ++n) sum[mm][n] = (kh ? acc[2 + mm][n] : acc[mm][n]) + *(LAS f32x4*)(pp + (mm * 4 + n) * 1024);
  }
  const int piece = kh * 2 + wc;
  if (!keep) {
    char* dst = xch + (size_t)b * 16384 + piece * 4096 + lane * 16;
#pragma unroll
    for (int mm = 0; mm < 2; ++mm)
#pragma unroll
      for (int h = 0; h < 2; ++h)
        st_sc1(dst + (mm * 2 + h) * 1024, (u32x4){pk2(sum[mm][2 * h][0], sum[mm][2 * h][1]), pk2(sum[mm][2 * h][2], sum[mm][2 * h][3]),
                                                  pk2(sum[mm][2 * h + 1][0], sum[mm][2 * h + 1][1]), pk2(sum[mm][2 * h + 1][2], sum[mm][2 * h + 1][3])});
    if (b == 0 && wid == 0) {
#pragma unroll
      for (int j = 0; j < 4; ++j) wait_flags(ws + WS_FLG + (j * 64 + lane) * FS, tag);
      if (lane == 0) __hip_atomic_store((gu64*)(ws + WS_EPOCH), tag, __ATOMIC_RELAXED, __HIP_MEMORY_SCOPE_AGENT);
    }
  } else {
    const char* src = xch + (size_t)(b ^ 1) * 16384 + piece * 4096 + lane * 16;
    u32x4 pv[4];
    for (unsigned spins = 0;; ++spins) {
      asm volatile(
          "global_load_dwordx4 %0, %4, off sc1\n\tglobal_load_dwordx4 %1, %4, off offset:1024 sc1\n\t"
          "global_load_dwordx4 %2, %4, off offset:2048 sc1\n\tglobal_load_dwordx4 %3, %4, off offset:3072 sc1\n\ts_waitcnt vmcnt(0)"
          : "=&v"(pv[0]), "=&v"(pv[1]), "=&v"(pv[2]), "=&v"(pv[3]) : "v"(src) : "memory");
      if (__all(fresh4(pv[0]) && fresh4(pv[1]) && fresh4(pv[2]) && fresh4(pv[3])) || spins > (1u << 14)) break;
      __builtin_amdgcn_s_sleep(1);
    }
#pragma unroll
    for (int mm = 0; mm < 2; ++mm)
#pragma unroll
      for (int n = 0; n < 4; ++n) {
        const unsigned q0 = pv[mm * 2 + (n >> 1)][2 * (n & 1)], q1 = pv[mm * 2 + (n >> 1)][2 * (n & 1) + 1];
        const f32x4 v = sum[mm][n];
        u32x2 o;
        o[0] = pk2(v[0] + blo(q0) + blo(xv[mm][n][0]) + blo(bv[n][0]), v[1] + bhi(q0) + bhi(xv[mm][n][0]) + bhi(bv[n][0]));
        o[1] = pk2(v[2] + blo(q1) + blo(xv[mm][n][1]) + blo(bv[n][1]), v[3] + bhi(q1) + bhi(xv[mm][n][1]) + bhi(bv[n][1]));
        *(u32x2*)(out + (size_t)(row0 + mm * 16) * D_ + col0 + n * 16) = o;
      }
  }
}

__global__ __launch_bounds__(512, 2) void k_main(const u16* __restrict__ x, const u16* __restrict__ wqkv, const u16* __restrict__ w1,
                                                 const u16* __restrict__ b1, const u16* __restrict__ wphi,
                                                 const u16* __restrict__ w2, const u16* __restrict__ wo, const u16* __restrict__ b2,
                                                 char* ws, u16* __restrict__ out) {
  extern __shared__ __attribute__((aligned(16))) char smem[];
  LAS char* lds = (LAS char*)smem;
  const int b = blockIdx.x, grp = b & 7, idx = b >> 3;
  const int panel = idx & 7, kind = grp & 1, tcol = (grp >> 1) * 4 + (idx >> 3);
  u64 tag;
  if (kind == 0) {
    tag = gemm1_body<true>(x, wqkv, tcol * 64, 0, panel, tcol, b1, wphi, ws, w2, wo, lds);
  } else {
    const int c0 = tcol * 224, woff = c0 & 63;
    tag = gemm1_body<false>(x, w1, c0 - woff, woff, panel, tcol, b1, wphi, ws, w2, wo, lds);
  }
  gemm2_body(ws, w2, wo, x, b2, out, tag, kind * 16 + tcol,   kind ^ 1, panel, panel * 2 + ((grp >> 1) & 1), (grp >> 2) * 4 + (idx >> 3), lds);
}

extern "C" void kernel_launch(void* const* d_in, const int* in_sizes, int n_in, void* d_out, int out_size, void* d_ws, size_t ws_size,
                              hipStream_t stream) {
  (void)hipFuncSetAttribute((const void*)k_main, hipFuncAttributeMaxDynamicSharedMemorySize, G1_LDS);
  k_main<<<256, 512, G1_LDS, stream>>>((const u16*)d_in[0], (const u16*)d_in[1], (const u16*)d_in[5], (const u16*)d_in[6], (const u16*)d_in[2],
                                       (const u16*)d_in[7], (const u16*)d_in[3], (const u16*)d_in[8], (char*)d_ws, (u16*)d_out);
}
```

```cpp
#include <hip/hip_runtime.h>
#include <hip/hip_bf16.h>
#include <stdint.h>

typedef __bf16 bf16x8 __attribute__((ext_vector_type(8)));
typedef __bf16 bf16x4 __attribute__((ext_vector_type(4)));
typedef __bf16 bf16x2 __attribute__((ext_vector_type(2)));
typedef float f32x4 __attribute__((ext_vector_type(4)));
typedef float f32x2 __attribute__((ext_vector_type(2)));
typedef unsigned u32x4 __attribute__((ext_vector_type(4)));
typedef unsigned u32x2 __attribute__((ext_vector_type(2)));
typedef unsigned short u16;
#define LAS __attribute__((address_space(3)))
#define DI __device__ __forceinline__

constexpr int S_ = 2048, D_ = 1024, F_ = 3584, H_ = 32;
constexpr int KCAT = F_ + D_;
constexpr int NKG1 = D_ / 32;
constexpr int NKG2 = KCAT / 32;

constexpr size_t WS_XN   = 0;
constexpr size_t WS_ACT  = WS_XN + (size_t)S_ * D_ * 2;
constexpr size_t WS_KVX  = WS_ACT + (size_t)S_ * KCAT * 2;
constexpr int KVX_TILE = 8192 + 256;
constexpr size_t KVX_BYTES = (size_t)8 * 16 * KVX_TILE;
constexpr size_t WS_XCH  = WS_KVX + KVX_BYTES;
constexpr size_t WS_FLG  = WS_XCH + (size_t)256 * 16384;
constexpr int FS = 8;
constexpr size_t WS_FLG2 = WS_FLG + 256 * FS, WS_EPOCH = WS_FLG + 512 * FS + 128;

DI float rbf(float x) { return (float)(__bf16)x; }
DI unsigned pk2(float lo, float hi) { bf16x2 v; v[0] = (__bf16)lo; v[1] = (__bf16)hi; return __builtin_bit_cast(unsigned, v); }
DI float blo(unsigned u) { return __uint_as_float(u << 16); }
DI float bhi(unsigned u) { return __uint_as_float(u & 0xffff0000u); }

#define MFMA16(a, b, c) __builtin_amdgcn_mfma_f32_16x16x32_bf16((a), (b), (c), 0, 0, 0)
#define WAIT_V(n) asm volatile("s_waitcnt vmcnt(%0)" ::"n"(n) : "memory")
#define WAIT_L(n) do { static_assert((n) == 0, ""); asm volatile("" ::: "memory"); __builtin_amdgcn_s_waitcnt(0xC07F); asm volatile("" ::: "memory"); } while (0)
#define BAR() do { asm volatile("" ::: "memory"); __builtin_amdgcn_s_barrier(); asm volatile("" ::: "memory"); } while (0)
#define SCHED() __builtin_amdgcn_sched_barrier(0)
template <int N> struct IC { static constexpr int v = N; };

DI void glds16_sc1(unsigned voff, const void* sbase, unsigned ldsaddr) {
  asm volatile("s_mov_b32 m0, %2\n\ts_nop 0\n\tglobal_load_lds_dwordx4 %0, %1 sc1" ::"v"(voff), "s"(sbase), "s"(ldsaddr) : "memory");
}
DI void glds16(unsigned voff, const void* sbase, unsigned ldsaddr) {
  asm volatile("s_mov_b32 m0, %2\n\ts_nop 0\n\tglobal_load_lds_dwordx4 %0, %1" ::"v"(voff), "s"(sbase), "s"(ldsaddr) : "memory");
}
DI const char* uniform_ptr(const char* p) {
  const unsigned long long a = (unsigned long long)p;
  unsigned lo = __builtin_amdgcn_readfirstlane((unsigned)a), hi = __builtin_amdgcn_readfirstlane((unsigned)(a >> 32));
  asm volatile("s_nop 4" : "+s"(lo), "+s"(hi));
  return (const char*)(((unsigned long long)hi << 32) | lo);
}
DI LAS char* opaque(LAS char* p) { asm volatile("" : "+v"(p)); return p; }
DI bf16x8 lds_b128(LAS char* p) { return *(LAS bf16x8*)p; }
DI bf16x4 lds_tr(LAS char* p) { return __builtin_amdgcn_ds_read_tr16_b64_v4bf16((LAS bf16x4*)p); }
DI bf16x8 cat8(bf16x4 a, bf16x4 b) { return __builtin_shufflevector(a, b, 0, 1, 2, 3, 4, 5, 6, 7); }
DI bf16x8 pack8(f32x4 a, f32x4 b) {
  bf16x8 v;
  v[0] = (__bf16)a[0]; v[1] = (__bf16)a[1]; v[2] = (__bf16)a[2]; v[3] = (__bf16)a[3];
  v[4] = (__bf16)b[0]; v[5] = (__bf16)b[1]; v[6] = (__bf16)b[2]; v[7] = (__bf16)b[3];
  return v;
}

typedef __attribute__((address_space(1))) unsigned gu32;
typedef unsigned long long u64;
typedef __attribute__((address_space(1))) u64 gu64;
DI void st_sc1(void* p, u32x4 v) { asm volatile("global_store_dwordx4 %0, %1, off sc1\n\ts_nop 1" ::"v"(p), "v"(v) : "memory"); }
DI unsigned umax4(u32x4 v, unsigned m) { return max(max(max(v[0], v[1]), v[2]), max(v[3], m)); }
DI void st8_sc1(void* p, u32x2 v) { asm volatile("global_store_dwordx2 %0, %1, off sc1\n\ts_nop 1" ::"v"(p), "v"(v) : "memory"); }
DI bool fresh4(u32x4 v) { return umax4(v, 0u) != 0xffffffffu; }
#define XRED4(OP, a1, b1, a2, b2) asm volatile( \
    "s_nop 1\n\tv_permlane16_swap_b32 %0, %1\n\tv_permlane16_swap_b32 %2, %3\n\t" \
    OP " %0, %0, %1\n\t" OP " %2, %2, %3\n\tv_mov_b32 %1, %0\n\tv_mov_b32 %3, %2\n\ts_nop 0\n\t" \
    "v_permlane32_swap_b32 %0, %1\n\tv_permlane32_swap_b32 %2, %3\n\t" \
    OP " %0, %0, %1\n\t" OP " %2, %2, %3\n\tv_mov_b32 %1, %0\n\tv_mov_b32 %3, %2\n\ts_nop 0\n\t" \
    "v_permlane16_swap_b32 %0, %1\n\tv_permlane16_swap_b32 %2, %3" \
    : "+v"(a1), "+v"(b1), "+v"(a2), "+v"(b2))
DI float max8(const float* v) {
  const float t1 = fmaxf(fmaxf(v[0], v[1]), v[2]), t2 = fmaxf(fmaxf(v[3], v[4]), v[5]), t3 = fmaxf(fmaxf(v[6], v[7]), t1);
  return fmaxf(t2, t3);
}
DI float min8(const float* v) {
  const float t1 = fminf(fminf(v[0], v[1]), v[2]), t2 = fminf(fminf(v[3], v[4]), v[5]), t3 = fminf(fminf(v[6], v[7]), t1);
  return fminf(t2, t3);
}
DI void dual_softmax2(const f32x4 (&p)[2][2], u32x4 (&pos)[2], u32x4 (&neg)[2]) {
  float v[2][8];
#pragma unroll
  for (int t = 0; t < 2; ++t)
#pragma unroll
    for (int h = 0; h < 2; ++h)
#pragma unroll
      for (int r = 0; r < 2; ++r) {
        unsigned u = pk2(p[t][h][2 * r], p[t][h][2 * r + 1]);
        asm("" : "+v"(u));
        v[t][4 * h + 2 * r] = blo(u); v[t][4 * h + 2 * r + 1] = bhi(u);
      }
  float mx0 = max8(v[0]), nm0 = -min8(v[0]), mx1 = max8(v[1]), nm1 = -min8(v[1]);
  XRED4("v_max_f32", mx0, nm0, mx1, nm1);
  constexpr float L2E = 1.44269504f;
  const f32x2 kp = {L2E, L2E}, kn = {-L2E, -L2E};
  f32x2 ep[2][4], en[2][4];
  float sp[2], sn[2];
#pragma unroll
  for (int t = 0; t < 2; ++t) {
    const float cpv = t ? mx1 : mx0, cnv = t ? nm1 : nm0;
    const f32x2 cp = {cpv, cpv}, cn = {cnv, cnv};
#pragma unroll
    for (int r = 0; r < 4; ++r) {
      const f32x2 x = {v[t][2 * r], v[t][2 * r + 1]};
      const f32x2 ap = (x - cp) * kp, an = (x + cn) * kn;
      ep[t][r] = (f32x2){__builtin_amdgcn_exp2f(ap[0]), __builtin_amdgcn_exp2f(ap[1])};
      en[t][r] = (f32x2){__builtin_amdgcn_exp2f(an[0]), __builtin_amdgcn_exp2f(an[1])};
    }
    const f32x2 s2p = (ep[t][0] + ep[t][1]) + (ep[t][2] + ep[t][3]), s2n = (en[t][0] + en[t][1]) + (en[t][2] + en[t][3]);
    sp[t] = s2p[0] + s2p[1]; sn[t] = s2n[0] + s2n[1];
  }
  XRED4("v_add_f32", sp[0], sn[0], sp[1], sn[1]);
#pragma unroll
  for (int t = 0; t < 2; ++t) {
    const float rp = __builtin_amdgcn_rcpf(sp[t]), rn = __builtin_amdgcn_rcpf(sn[t]);
    const f32x2 rp2 = {rp, rp}, rn2 = {rn, rn};
#pragma unroll
    for (int r = 0; r < 4; ++r) {
      const f32x2 a = ep[t][r] * rp2, b = en[t][r] * rn2;
      pos[t][r] = pk2(a[0], a[1]); neg[t][r] = pk2(b[0], b[1]);
    }
  }
}

DI float sum8lanes(float v) {
  v += __builtin_bit_cast(float, __builtin_amdgcn_update_dpp(0, __builtin_bit_cast(int, v), 0xB1, 0xf, 0xf, true));
  v += __builtin_bit_cast(float, __builtin_amdgcn_update_dpp(0, __builtin_bit_cast(int, v), 0x4E, 0xf, 0xf, true));
  v += __builtin_bit_cast(float, __builtin_amdgcn_update_dpp(0, __builtin_bit_cast(int, v), 0x141, 0xf, 0xf, true));
  return v;
}
DI void wait_flags(const char* p, u64 tag) {
  for (unsigned spins = 0; spins < (1u << 16); ++spins) {
    if (__all(__hip_atomic_load((const gu64*)p, __ATOMIC_RELAXED, __HIP_MEMORY_SCOPE_AGENT) == tag)) break;
    __builtin_amdgcn_s_sleep(1);
  }
}

constexpr int G1_SLOT = 16384;
constexpr int G1_B = 65536, G1_X = 131072;
constexpr int G1_LDS = G1_X + 4096 + 2048;

DI f32x2 gelu2(f32x2 x) {
  const f32x2 c1 = {-0.10294324f, -0.10294324f}, c0 = {-2.3022082f, -2.3022082f}, one = {1.0f, 1.0f};
  const f32x2 a = x * __builtin_elementwise_fma(x * x, c1, c0);
  const f32x2 d = one + (f32x2){__builtin_amdgcn_exp2f(a[0]), __builtin_amdgcn_exp2f(a[1])};
  return x * (f32x2){__builtin_amdgcn_rcpf(d[0]), __builtin_amdgcn_rcpf(d[1])};
}

#define TDIST 12
template <bool QKV>
DI u64 gemm1_body(const u16* __restrict__ x, const u16* __restrict__ W, int wcol0, int woff, int panel, int tcol,
                  const u16* __restrict__ b1, const u16* __restrict__ wphi, char* ws,
                  const u16* __restrict__ w2, const u16* __restrict__ wo, LAS char* lds) {
  char* xn = ws + WS_XN;
  char* act = ws + WS_ACT;
  char* kvx = ws + WS_KVX;
  constexpr int LDW = QKV ? 3 * D_ : F_;
  constexpr int NG = QKV ? 3 : 4;
  constexpr int NT = QKV ? 6 : 7;
  const int tid = threadIdx.x, lane = tid & 63;
  const int wid = __builtin_amdgcn_readfirstlane(tid >> 6);
  const int wr = wid >> 1, wc = wid & 1;
  const int g = lane >> 4, i16 = lane & 15, q4 = i16 >> 2, p4 = lane & 3;
  const unsigned ldsb = (unsigned)(size_t)lds;

  const char* abase = uniform_ptr(xn + (size_t)panel * 16 * NKG1 * 1024);
  const unsigned avoff = (unsigned)(wid * 2) * NKG1 * 1024 + lane * 16;
  const char* bbase = (const char*)W;
  unsigned bvoff[2]; unsigned bdst[2];
#pragma unroll
  for (int j = 0; j < 2; ++j) {
    int pb = QKV ? (wid + 8 * j) : (wid * 2 + j);
    if (QKV && pb >= 12) pb -= 8;
    const int kg = pb / NG, cg = pb % NG;
    const int r = lane >> 3, sw = ((r >> 1) & 1) | ((kg & 1) << 1);
    const int cc = (lane & 7) ^ (sw << 1);
    const int col = QKV ? (cg * D_ + wcol0) : (wcol0 + cg * 64);
    bvoff[j] = (unsigned)(((kg * 8 + r) * LDW + col + cc * 8) * 2);
    bdst[j] = ldsb + G1_B + pb * 1024;
  }
  const unsigned adst = ldsb + wid * 2048;

  LAS char* ard = opaque(lds + wr * 4096 + lane * 16);
  constexpr int NB = QKV ? 2 : 4;
  LAS char* brd[NB];
#pragma unroll
  for (int j = 0; j < NB; ++j) {
    const int n0 = QKV ? (wc * 32 + j * 16) : (woff + wc * 112 + j * 16);
    const int sw = (q4 >> 1) | ((g & 1) << 1);
    brd[j] = opaque(lds + G1_B + (g * NG + (n0 >> 6)) * 1024 + q4 * 128 + (((((n0 & 63) >> 3) ^ (sw << 1)) + (p4 >> 1)) << 4) + (p4 & 1) * 8);
  }

  f32x4 acc[4][NT];
#pragma unroll
  for (int m = 0; m < 4; ++m)
#pragma unroll
    for (int n = 0; n < NT; ++n) acc[m][n] = (f32x4){0.f, 0.f, 0.f, 0.f};

  bf16x8 fa[2][4], fb[2][NT];

  auto issue = [&](int q, int stage, int slot) {
    if (q < 2) glds16_sc1(avoff + q * NKG1 * 1024, abase + (size_t)stage * 1024, adst + q * 1024 + slot * G1_SLOT);
    else       glds16(bvoff[q - 2], bbase + (size_t)stage * 64 * LDW, bdst[q - 2] + slot * G1_SLOT);
  };
  auto rdA = [&](auto P, auto SL, int m) { fa[P.v][m] = lds_b128(ard + SL.v * G1_SLOT + m * 1024); };
  auto rdB = [&](auto P, auto SL, int n) { LAS char* p = brd[n % NB] + (n / NB) * 1024 + SL.v * G1_SLOT; fb[P.v][n] = cat8(lds_tr(p), lds_tr(p + 512)); };
  auto mma4 = [&](auto P, int n) {
#pragma unroll
    for (int m = 0; m < 4; ++m) {
      if (QKV && n >= 4) acc[m][n] = MFMA16(fa[P.v][m], fb[P.v][n], acc[m][n]);
      else               acc[m][n] = MFMA16(fb[P.v][n], fa[P.v][m], acc[m][n]);
    }
  };
  auto step = [&](auto SC, auto VM, auto GL, auto RD, int s) {
    constexpr int P = SC.v & 1, SNv = (SC.v + 1) & 3;
    const IC<P> Pc; const IC<P ^ 1> Pn; const IC<SNv> SN;
    if constexpr (RD.v) { WAIT_V(VM.v); WAIT_L(0); BAR(); }
    SCHED();
    mma4(Pc, 0); SCHED();
    if constexpr (RD.v) { rdA(Pn, SN, 0); rdA(Pn, SN, 1); rdB(Pn, SN, 0); }
    if constexpr (GL.v) issue(0, s + 4, SC.v);
    SCHED();
    mma4(Pc, 1); SCHED();
    if constexpr (RD.v) { rdA(Pn, SN, 2); rdA(Pn, SN, 3); rdB(Pn, SN, 1); }
    if constexpr (GL.v) issue(1, s + 4, SC.v);
    SCHED();
    mma4(Pc, 2); SCHED();
    if constexpr (RD.v) { rdB(Pn, SN, 2); rdB(Pn, SN, 3); }
    if constexpr (GL.v) issue(2, s + 4, SC.v);
    SCHED();
    mma4(Pc, 3); SCHED();
    if constexpr (RD.v) { rdB(Pn, SN, 4); rdB(Pn, SN, 5); if constexpr (NT == 7) rdB(Pn, SN, 6); }
    if constexpr (GL.v) issue(3, s + 4, SC.v);
    SCHED();
    mma4(Pc, 4); SCHED();
    mma4(Pc, 5);
    if constexpr (NT == 7) mma4(Pc, 6);
    SCHED();
  };

  const int slot = (QKV ? 0 : 16) + tcol;
  u64 tag;
  {
    const int r8 = lane >> 3, c8 = lane & 7;
    const int row = panel * 256 + slot * 8 + r8;
    const u16* src = x + (size_t)row * D_ + wid * 128 + c8 * 8;
    u32x4 v0, v1; u32x2 ep;
    asm volatile("global_load_dwordx4 %0, %3, off\n\tglobal_load_dwordx4 %1, %3, off offset:128\n\tglobal_load_dwordx2 %2, %4, off sc1"
                 : "=&v"(v0), "=&v"(v1), "=&v"(ep) : "v"(src), "v"(ws + WS_EPOCH) : "memory");
    SCHED();
    if (QKV) {
      const u32x4 fill = {~0u, ~0u, ~0u, ~0u};
      char* kp = kvx + (size_t)(panel * 16 + tcol) * KVX_TILE;
      st_sc1(kp + tid * 16, fill);
      st_sc1(kp + 8192 + i16 * 16, fill);
    }
    SCHED();
    asm volatile("s_waitcnt vmcnt(%3)" : "+v"(v0), "+v"(v1), "+v"(ep) : "n"(QKV ? 2 : 0) : "memory");
    SCHED();
    tag = (((u64)(unsigned)__builtin_amdgcn_readfirstlane(ep[1]) << 32) | (unsigned)__builtin_amdgcn_readfirstlane(ep[0])) + 1;
    f32x2 f[8];
#pragma unroll
    for (int i = 0; i < 4; ++i) { f[i] = (f32x2){blo(v0[i]), bhi(v0[i])}; f[4 + i] = (f32x2){blo(v1[i]), bhi(v1[i])}; }
    const f32x2 s2 = ((f[0] + f[1]) + (f[2] + f[3])) + ((f[4] + f[5]) + (f[6] + f[7]));
    const float mw = sum8lanes(s2[0] + s2[1]) * (1.0f / 128.f);
    f32x2 q2 = {0.f, 0.f};
#pragma unroll
    for (int i = 0; i < 8; ++i) { const f32x2 d = f[i] - (f32x2){mw, mw}; q2 = __builtin_elementwise_fma(d, d, q2); }
    const float qq = sum8lanes(q2[0] + q2[1]);
    LAS f32x2* stt = (LAS f32x2*)(lds + G1_X + 4096);
    if (c8 == 0) stt[r8 * 8 + wid] = (f32x2){mw, qq};
    WAIT_L(0); BAR();
    const f32x2 pw = stt[r8 * 8 + c8];
    const float mean = sum8lanes(pw[0]) * 0.125f;
    const float dm = pw[0] - mean;
    const float m2 = sum8lanes(__builtin_fmaf(128.f * dm, dm, pw[1]));
    const float mb = rbf(mean), vb = rbf(m2 * (1.0f / D_));
    const float rs = 1.0f / rbf(sqrtf(rbf(vb + 1e-5f)));
    u32x4 o0, o1;
#pragma unroll
    for (int i = 0; i < 8; ++i) {
      const f32x2 d = f[i] - (f32x2){mb, mb};
      unsigned u = pk2(d[0], d[1]);
      asm("" : "+v"(u));
      const f32x2 y = (f32x2){blo(u), bhi(u)} * (f32x2){rs, rs};
      if (i < 4) o0[i] = pk2(y[0], y[1]); else o1[i - 4] = pk2(y[0], y[1]);
    }
    char* dst = xn + ((size_t)((row >> 4) * NKG1 + wid * 4 + (c8 >> 2)) * 64 + (c8 & 3) * 16 + (row & 15)) * 16;
    st_sc1(dst, o0); st_sc1(dst + 2048, o1);
    WAIT_V(0); BAR();
    if (tid == 0) __hip_atomic_store((gu64*)(ws + WS_FLG + (panel * 32 + slot) * FS), tag, __ATOMIC_RELAXED, __HIP_MEMORY_SCOPE_AGENT);
    if (QKV && wid >= 4) glds16((unsigned)lane * 16, uniform_ptr((const char*)wphi + (size_t)tcol * 4096 + (wid - 4) * 1024), ldsb + G1_X + (wid - 4) * 1024);
#pragma unroll
    for (int st = 0; st < 4; ++st) { issue(2, st, st); issue(3, st, st); }
    if (wid == 0) wait_flags(ws + WS_FLG + ((QKV && lane >= 32) ? ((lane & 7) * 32 + slot) : (panel * 32 + (lane & 31))) * FS, tag);
    BAR();
  }
#pragma unroll
  for (int st = 0; st < 4; ++st) { issue(0, st, st); issue(1, st, st); }
  WAIT_V(2); BAR();
  {
    const IC<0> P0; const IC<0> S0;
#pragma unroll
    for (int m = 0; m < 4; ++m) rdA(P0, S0, m);
#pragma unroll
    for (int n = 0; n < NT; ++n) rdB(P0, S0, n);
  }
#pragma unroll 1
  for (int s = 0; s < 28; s += 4) {
    if (s + TDIST < 32 && wid == (s >> 2)) {
      const int ln = __builtin_amdgcn_mbcnt_hi(~0u, __builtin_amdgcn_mbcnt_lo(~0u, 0u));
      const int seg = QKV ? ((ln & 3) == 3 ? 0 : (ln & 3)) : (ln & 3);
      const unsigned toff = (unsigned)(((s + TDIST) * 32 + panel * 16 + (ln >> 2)) * LDW + (QKV ? seg * D_ : seg * 64) + wcol0) * 2;
      asm volatile("s_mov_b32 m0, %2\n\ts_nop 0\n\tglobal_load_lds_dword %0, %1" ::"v"(toff), "s"(bbase), "s"(ldsb + G1_X + 4096 + 1024) : "memory");
    }
    step(IC<0>{}, IC<8>{}, IC<1>{}, IC<1>{}, s);
    step(IC<1>{}, IC<8>{}, IC<1>{}, IC<1>{}, s + 1);
    step(IC<2>{}, IC<8>{}, IC<1>{}, IC<1>{}, s + 2);
    step(IC<3>{}, IC<8>{}, IC<1>{}, IC<1>{}, s + 3);
  }
  step(IC<0>{}, IC<8>{}, IC<0>{}, IC<1>{}, 28);
  constexpr int NX = 3;
  {
    int wv = blockIdx.x * 8 + wid;
    if (wv >= 1152) wv -= 1152;
    const char* src = uniform_ptr(wv < 896 ? (const char*)w2 + (size_t)wv * 8192 : (const char*)wo + (size_t)(wv - 896) * 8192);
    asm volatile("s_mov_b32 m0, %2\n\ts_nop 0\n\tglobal_load_lds_dword %0, %1" ::"v"(lane * 128), "s"(src), "s"(ldsb + G1_X + 4096 + wid * 256) : "memory");
    char* xp = ws + WS_XCH + (size_t)blockIdx.x * 16384 + tid * 16;
    const u32x4 fill = {~0u, ~0u, ~0u, ~0u};
    st_sc1(xp, fill); st_sc1(xp + 8192, fill);
  }
  step(IC<1>{}, IC<4 + NX>{}, IC<0>{}, IC<1>{}, 29);
  step(IC<2>{}, IC<0 + NX>{}, IC<0>{}, IC<1>{}, 30);
  step(IC<3>{}, IC<0>{}, IC<0>{}, IC<0>{}, 31);

  if (!QKV) {
    const int ncol0 = tcol * 224 + wc * 112 + 4 * g;
    u32x2 bb[NT];
#pragma unroll
    for (int n = 0; n < NT; ++n) bb[n] = *(const u32x2*)(b1 + ncol0 + n * 16);
    auto act4 = [&](int m, int n) {
      const f32x2 yA = gelu2((f32x2){acc[m][n][0] + blo(bb[n][0]), acc[m][n][1] + bhi(bb[n][0])});
      const f32x2 yB = gelu2((f32x2){acc[m][n][2] + blo(bb[n][1]), acc[m][n][3] + bhi(bb[n][1])});
      return (u32x2){pk2(yA[0], yA[1]), pk2(yB[0], yB[1])};
    };
    const int rt0 = panel * 16 + wr * 4;
    char* obase = (char*)act + ((size_t)rt0 * NKG2 + tcol * 7) * 1024 + lane * 16;
    if (wc == 0) {
#pragma unroll
      for (int m = 0; m < 4; ++m) {
        char* rb = obase + (size_t)m * NKG2 * 1024;
#pragma unroll
        for (int p = 0; p < 3; ++p) { const u32x2 lo = act4(m, 2 * p), hi = act4(m, 2 * p + 1); st_sc1(rb + p * 1024, (u32x4){lo[0], lo[1], hi[0], hi[1]}); }
        st8_sc1(rb + 3 * 1024, act4(m, 6 % NT));
      }
    } else {
#pragma unroll
      for (int m = 0; m < 4; ++m) {
        char* rb = obase + (size_t)m * NKG2 * 1024;
        st8_sc1(rb + 3 * 1024 + 8, act4(m, 0));
#pragma unroll
        for (int p = 0; p < 3; ++p) { const u32x2 lo = act4(m, 2 * p + 1), hi = act4(m, (2 * p + 2) % NT); st_sc1(rb + (4 + p) * 1024, (u32x4){lo[0], lo[1], hi[0], hi[1]}); }
      }
    }
  } else {
    const int head = tcol * 2 + wc;
    LAS char* wp = lds + G1_X + wc * 2048;
    bf16x8 wf[2];
#pragma unroll
    for (int ft = 0; ft < 2; ++ft) {
      LAS char* p = wp + (4 * g + q4) * 64 + (16 * ft + 4 * p4) * 2;
      wf[ft] = cat8(lds_tr(p), lds_tr(p + 16 * 64));
    }
    const f32x4 z4 = {0.f, 0.f, 0.f, 0.f};
    LAS char* sc = lds + (wid >> 2) * G1_B + (wid & 3) * 8704;
#pragma unroll
    for (int mp = 0; mp < 2; ++mp) {
      f32x4 pp[2][2]; u32x4 pos[2], neg[2];
#pragma unroll
      for (int t = 0; t < 2; ++t) {
        const bf16x8 bk = pack8(acc[2 * mp + t][2], acc[2 * mp + t][3]);
        pp[t][0] = MFMA16(wf[0], bk, z4); pp[t][1] = MFMA16(wf[1], bk, z4);
      }
      dual_softmax2(pp, pos, neg);
#pragma unroll
      for (int t = 0; t < 2; ++t) {
        LAS char* rowp = sc + (16 * (2 * mp + t) + i16) * 136 + 8 * g;
        *(LAS u32x2*)(rowp) = (u32x2){pos[t][0], pos[t][1]};      *(LAS u32x2*)(rowp + 32) = (u32x2){pos[t][2], pos[t][3]};
        *(LAS u32x2*)(rowp + 64) = (u32x2){neg[t][0], neg[t][1]}; *(LAS u32x2*)(rowp + 96) = (u32x2){neg[t][2], neg[t][3]};
      }
    }
    WAIT_L(0);
    f32x4 kv[4][3];
#pragma unroll
    for (int ft = 0; ft < 4; ++ft)
#pragma unroll
      for (int d = 0; d < 3; ++d) kv[ft][d] = z4;
    bf16x8 ones;
#pragma unroll
    for (int j = 0; j < 8; ++j) ones[j] = (__bf16)1.0f;
#pragma unroll
    for (int ks = 0; ks < 2; ++ks) {
      const bf16x8 bv0 = pack8(acc[2 * ks][4], acc[2 * ks + 1][4]);
      const bf16x8 bv1 = pack8(acc[2 * ks][5], acc[2 * ks + 1][5]);
#pragma unroll
      for (int ft = 0; ft < 4; ++ft) {
        LAS char* p = sc + (32 * ks + 4 * g + q4) * 136 + (16 * ft + 4 * p4) * 2;
        const bf16x8 a = cat8(lds_tr(p), lds_tr(p + 16 * 136));
        kv[ft][0] = MFMA16(a, bv0, kv[ft][0]);
        kv[ft][1] = MFMA16(a, bv1, kv[ft][1]);
        kv[ft][2] = MFMA16(a, ones, kv[ft][2]);
      }
    }
    WAIT_L(0); BAR();
    {
      LAS char* pp = lds + wid * 8448;
#pragma unroll
      for (int sg = 0; sg < 2; ++sg) {
#pragma unroll
        for (int d = 0; d < 2; ++d) {
          *(LAS f32x4*)(pp + (sg * 2 + d) * 2048 + lane * 16) = kv[2 * sg][d];
          *(LAS f32x4*)(pp + (sg * 2 + d) * 2048 + 1024 + lane * 16) = kv[2 * sg + 1][d];
        }
        if (i16 == 0) {
          *(LAS f32x4*)(pp + 8192 + sg * 128 + g * 16) = kv[2 * sg][2];
          *(LAS f32x4*)(pp + 8192 + sg * 128 + 64 + g * 16) = kv[2 * sg + 1][2];
        }
      }
    }
    WAIT_L(0); BAR();
    char* kvt = kvx + (size_t)tcol * KVX_TILE;
    char* kvmine = kvt + (size_t)panel * 16 * KVX_TILE;
    {
      LAS char* pp = lds + (tid >> 8) * 8448 + ((tid >> 6) & 3) * 2048 + lane * 16;
      f32x4 lo = *(LAS f32x4*)(pp), hi = *(LAS f32x4*)(pp + 1024);
#pragma unroll
      for (int w = 1; w < 4; ++w) { lo += *(LAS f32x4*)(pp + w * 16896); hi += *(LAS f32x4*)(pp + w * 16896 + 1024); }
      st_sc1(kvmine + tid * 16, (u32x4){pk2(lo[0], lo[1]), pk2(lo[2], lo[3]), pk2(hi[0], hi[1]), pk2(hi[2], hi[3])});
    }
    if (tid < 16) {
      LAS char* pp = lds + (tid >> 3) * 8448 + 8192 + ((tid >> 2) & 1) * 128 + (tid & 3) * 16;
      f32x4 lo = *(LAS f32x4*)(pp), hi = *(LAS f32x4*)(pp + 64);
#pragma unroll
      for (int w = 1; w < 4; ++w) { lo += *(LAS f32x4*)(pp + w * 16896); hi += *(LAS f32x4*)(pp + w * 16896 + 64); }
      st_sc1(kvmine + 8192 + tid * 16, (u32x4){pk2(lo[0], lo[1]), pk2(lo[2], lo[3]), pk2(hi[0], hi[1]), pk2(hi[2], hi[3])});
    }
    u32x4 qp[2][2], qn[2][2];
    auto qsm = [&](int mp) {
      f32x4 pp[2][2];
#pragma unroll
      for (int t = 0; t < 2; ++t) {
        const bf16x8 bq = pack8(acc[2 * mp + t][0], acc[2 * mp + t][1]);
        pp[t][0] = MFMA16(wf[0], bq, z4); pp[t][1] = MFMA16(wf[1], bq, z4);
      }
      dual_softmax2(pp, qp[mp], qn[mp]);
    };
    qsm(0);
    const char* kb0 = uniform_ptr(kvt), * kb1 = uniform_ptr(kvt + (size_t)32 * KVX_TILE);
    const char* kb2 = uniform_ptr(kvt + (size_t)64 * KVX_TILE), * kb3 = uniform_ptr(kvt + (size_t)96 * KVX_TILE);
    u32x4 pv[8], pz[8];
#define KV_GATHER(pv, OFF, TAIL) asm volatile( \
          "global_load_dwordx4 %0, %8, %10 sc1\n\tglobal_load_dwordx4 %1, %9, %10 sc1\n\t" \
          "global_load_dwordx4 %2, %8, %11 sc1\n\tglobal_load_dwordx4 %3, %9, %11 sc1\n\t" \
          "global_load_dwordx4 %4, %8, %12 sc1\n\tglobal_load_dwordx4 %5, %9, %12 sc1\n\t" \
          "global_load_dwordx4 %6, %8, %13 sc1\n\tglobal_load_dwordx4 %7, %9, %13 sc1" TAIL \
          : "=&v"(pv[0]), "=&v"(pv[1]), "=&v"(pv[2]), "=&v"(pv[3]), "=&v"(pv[4]), "=&v"(pv[5]), "=&v"(pv[6]), "=&v"(pv[7]) \
          : "v"(OFF), "v"((OFF) + 16 * KVX_TILE), "s"(kb0), "s"(kb1), "s"(kb2), "s"(kb3) : "memory")
#define KV_WAIT(pv) asm volatile("s_waitcnt vmcnt(0)" \
          : "+v"(pv[0]), "+v"(pv[1]), "+v"(pv[2]), "+v"(pv[3]), "+v"(pv[4]), "+v"(pv[5]), "+v"(pv[6]), "+v"(pv[7]) :: "memory")
    LAS char* kvs = lds + 98304;
    auto finish = [&](u32x4 (&pv)[8], unsigned off) {
      for (unsigned spins = 0;; ++spins) {
        unsigned m = 0;
#pragma unroll
        for (int p = 0; p < 8; ++p) m = umax4(pv[p], m);
        if (__all(m != 0xffffffffu) || spins > (1u << 14)) break;
        __builtin_amdgcn_s_sleep(2);
        KV_GATHER(pv, off, "\n\ts_waitcnt vmcnt(0)");
      }
      f32x2 t[4];
#pragma unroll
      for (int r = 0; r < 4; ++r) t[r] = (f32x2){blo(pv[0][r]), bhi(pv[0][r])};
#pragma unroll
      for (int p = 1; p < 8; ++p)
#pragma unroll
        for (int r = 0; r < 4; ++r) t[r] += (f32x2){blo(pv[p][r]), bhi(pv[p][r])};
      *(LAS u32x4*)(kvs + off) = (u32x4){pk2(t[0][0], t[0][1]), pk2(t[1][0], t[1][1]), pk2(t[2][0], t[2][1]), pk2(t[3][0], t[3][1])};
    };
    const unsigned go = (unsigned)tid * 16, gz = 8192u + (unsigned)i16 * 16;
    SCHED();
    KV_GATHER(pv, go, "");
    KV_GATHER(pz, gz, "");
    SCHED();
    qsm(1);
    SCHED();
    KV_WAIT(pv);
    KV_WAIT(pz);
    SCHED();
    finish(pv, go);
    if (wid == 0) finish(pz, gz);
    WAIT_L(0); BAR();
    bf16x8 a[2][3];
#pragma unroll
    for (int sg = 0; sg < 2; ++sg) {
#pragma unroll
      for (int d = 0; d < 2; ++d) a[sg][d] = lds_b128(kvs + wc * 4096 + (sg * 2 + d) * 1024 + lane * 16);
      a[sg][2] = lds_b128(kvs + 8192 + wc * 128 + sg * 64 + g * 16);
    }
#pragma unroll
    for (int m = 0; m < 4; ++m) {
      const bf16x8 bp = __builtin_bit_cast(bf16x8, qp[m >> 1][m & 1]), bn = __builtin_bit_cast(bf16x8, qn[m >> 1][m & 1]);
      f32x4 o0 = MFMA16(a[0][0], bp, z4), o1 = MFMA16(a[0][1], bp, z4), dn = MFMA16(a[0][2], bp, z4);
      o0 = MFMA16(a[1][0], bn, o0); o1 = MFMA16(a[1][1], bn, o1); dn = MFMA16(a[1][2], bn, dn);
      const float rd = __builtin_amdgcn_rcpf(dn[0] + 1e-6f);
      const int rt = panel * 16 + wr * 4 + m;
      u32x4 w;
      w[0] = pk2(o0[0] * rd, o0[1] * rd); w[1] = pk2(o0[2] * rd, o0[3] * rd);
      w[2] = pk2(o1[0] * rd, o1[1] * rd); w[3] = pk2(o1[2] * rd, o1[3] * rd);
      st_sc1((char*)act + ((size_t)rt * NKG2 + 112 + head) * 1024 + lane * 16, w);
    }
  }
  return tag;
}

constexpr int G2_SLOT = 32768;
constexpr int G2_POLL = 4 * G2_SLOT + 4096;
constexpr int KSPLIT = 36;

DI void gemm2_body(char* ws, const u16* __restrict__ w2, const u16* __restrict__ wo,
                   const u16* __restrict__ x, const u16* __restrict__ b2, u16* __restrict__ out,
                   u64 tag, int slot, int split, int panel, int pm, int pn, LAS char* lds) {
  const char* act = ws + WS_ACT;
  char* xch = ws + WS_XCH;
  char* flg2 = ws + WS_FLG2 + panel * 32 * FS;
  const unsigned ldsb = (unsigned)(size_t)lds;
  int lane = __builtin_amdgcn_mbcnt_hi(~0u, __builtin_amdgcn_mbcnt_lo(~0u, 0u));
  asm volatile("" : "+v"(lane));
  const int wid = __builtin_amdgcn_readfirstlane(threadIdx.x >> 6);
  const int kh = wid >> 2, wr = (wid >> 1) & 1, wc = wid & 1;
  const int g = lane >> 4, i16 = lane & 15, q4 = i16 >> 2, p4 = lane & 3;
  const int b = blockIdx.x;
  const int kt0 = split ? KSPLIT : 0, nst = split ? 72 - KSPLIT : KSPLIT;
  const char* abase = uniform_ptr(act + ((size_t)(pm * 8) * NKG2 + kt0 * 2) * 1024);
  const unsigned avoff = (unsigned)wid * NKG2 * 1024 + lane * 16;
  const unsigned adst = ldsb + wid * 1024;
  const int r8 = lane >> 3;
  const unsigned bvoff = (unsigned)((wid * 8 + r8) * D_ + pn * 128 + (((lane & 7) ^ (((r8 >> 1) & 3) << 1)) << 3)) * 2;
  const unsigned bdst = ldsb + 16384 + wid * 2048;
  const char* wb = uniform_ptr((const char*)w2 + (size_t)kt0 * 64 * D_ * 2);
  auto issue = [&](int q, int t, int slot) {
    if (q < 2) glds16_sc1(avoff + q * 1024, abase + (size_t)t * 2048, adst + q * 8192 + slot * G2_SLOT);
    else       glds16(bvoff + (q - 2) * 128, wb + (size_t)t * 64 * D_ * 2, bdst + (q - 2) * 1024 + slot * G2_SLOT);
  };
  WAIT_V(0); WAIT_L(0); BAR();
  if (threadIdx.x == 0) __hip_atomic_store((gu64*)(flg2 + slot * FS), tag, __ATOMIC_RELAXED, __HIP_MEMORY_SCOPE_AGENT);
  if (wid == panel) wait_flags(flg2 + (16 + (split ? 10 + (i16 < 6 ? i16 : 0) : (i16 < 11 ? i16 : 0))) * FS, tag);
#pragma unroll
  for (int st = 0; st < 4; ++st) { issue(2, st, st); issue(3, st, st); }
  BAR();
#pragma unroll
  for (int st = 0; st < 4; ++st) { issue(0, st, st); issue(1, st, st); }

  LAS char* ard[2];
  ard[0] = opaque(lds + kh * 8192 + wr * 4096 + lane * 16); ard[1] = opaque(ard[0] + 65536);
  LAS char* brd[2][4];
#pragma unroll
  for (int j = 0; j < 4; ++j) {
    const int r = 4 * (g & 1) + q4, sw = (r >> 1) & 3;
    brd[0][j] = opaque(lds + 16384 + ((kh * 4 + (g >> 1)) * 2 + wc) * 1024 + r * 128 + ((((2 * j) ^ (sw << 1)) + (p4 >> 1)) << 4) + (p4 & 1) * 8);
    brd[1][j] = opaque(brd[0][j] + 65536);
  }
  f32x4 acc[4][4];
#pragma unroll
  for (int m = 0; m < 4; ++m)
#pragma unroll
    for (int n = 0; n < 4; ++n) acc[m][n] = (f32x4){0.f, 0.f, 0.f, 0.f};
  bf16x8 fa[2][4], fb[2][4];
  auto rdA = [&](auto P, auto SL, int m) { fa[P.v][m] = lds_b128(ard[SL.v >> 1] + (SL.v & 1) * G2_SLOT + m * 1024); };
  auto rdB = [&](auto P, auto SL, int n) { LAS char* p = brd[SL.v >> 1][n] + (SL.v & 1) * G2_SLOT; fb[P.v][n] = cat8(lds_tr(p), lds_tr(p + 4096)); };
  auto mma4 = [&](auto P, int n) {
#pragma unroll
    for (int m = 0; m < 4; ++m) acc[m][n] = MFMA16(fb[P.v][n], fa[P.v][m], acc[m][n]);
  };
  auto step = [&](auto SC, auto VM, auto GL, auto RD, int t) {
    constexpr int P = SC.v & 1, SNv = (SC.v + 1) & 3;
    const IC<P> Pc; const IC<P ^ 1> Pn; const IC<SNv> SN;
    if constexpr (RD.v) { WAIT_V(VM.v); WAIT_L(0); BAR(); }
    SCHED();
    mma4(Pc, 0); SCHED();
    if constexpr (RD.v) { rdA(Pn, SN, 0); rdA(Pn, SN, 1); rdA(Pn, SN, 2); rdA(Pn, SN, 3); rdB(Pn, SN, 0); }
    if constexpr (GL.v) issue(0, t + 4, SC.v);
    SCHED();
    mma4(Pc, 1); SCHED();
    if constexpr (RD.v) { rdB(Pn, SN, 1); rdB(Pn, SN, 2); rdB(Pn, SN, 3); }
    if constexpr (GL.v) issue(1, t + 4, SC.v);
    SCHED();
    mma4(Pc, 2); SCHED();
    if constexpr (GL.v) { issue(2, t + 4, SC.v); issue(3, t + 4, SC.v); }
    SCHED();
    mma4(Pc, 3); SCHED();
  };
  WAIT_V(2); BAR();
  {
    const IC<0> P0; const IC<0> S0;
#pragma unroll
    for (int m = 0; m < 4; ++m) rdA(P0, S0, m);
#pragma unroll
    for (int n = 0; n < 4; ++n) rdB(P0, S0, n);
  }
  const IC<1> Y; const IC<0> N_; const IC<8> V8;
  const int tp = split ? 8 : 24;
#pragma unroll 1
  for (int t = 0; t < nst - 4; t += 4) {
    if (wid == 0) {
      if (t == tp) {
        asm volatile("s_mov_b32 m0, %2\n\ts_nop 0\n\tglobal_load_lds_dword %0, %1 sc1" ::"v"((lane >> 1) * FS + (lane & 1) * 4), "s"(uniform_ptr(flg2)), "s"(ldsb + G2_POLL) : "memory");
      } else if (t == tp + 4) {
        if (!__all(*(LAS u64*)(lds + G2_POLL + (lane & 31) * 8) == tag)) wait_flags(flg2 + (lane & 31) * FS, tag);
      }
    }
    if (t == 52 - KSPLIT && split == 1) wb = uniform_ptr((const char*)wo - (size_t)(56 - KSPLIT) * 64 * D_ * 2);
    step(IC<0>{}, V8, Y, Y, t); step(IC<1>{}, V8, Y, Y, t + 1); step(IC<2>{}, V8, Y, Y, t + 2); step(IC<3>{}, V8, Y, Y, t + 3);
  }
  step(IC<0>{}, V8, N_, Y, 0);
  step(IC<1>{}, IC<4>{}, N_, Y, 0);
  step(IC<2>{}, IC<0>{}, N_, Y, 0);
  step(IC<3>{}, IC<0>{}, N_, N_, 0);
  const int quad = wid & 3;
  {
    LAS char* pp = lds + (quad * 2 + kh) * 8192 + lane * 16;
#pragma unroll
    for (int mm = 0; mm < 2; ++mm)
#pragma unroll
      for (int n = 0; n < 4; ++n) *(LAS f32x4*)(pp + (mm * 4 + n) * 1024) = kh ? acc[mm][n] : acc[2 + mm][n];
  }
  const bool keep = (wr == split);
  const int row0 = pm * 128 + wr * 64 + kh * 32 + i16, col0 = pn * 128 + wc * 64 + 4 * g;
  u32x2 xv[2][4], bv[4];
  if (keep) {
#pragma unroll
    for (int n = 0; n < 4; ++n) bv[n] = *(const u32x2*)(b2 + col0 + n * 16);
#pragma unroll
    for (int mm = 0; mm < 2; ++mm)
#pragma unroll
      for (int n = 0; n < 4; ++n) xv[mm][n] = *(const u32x2*)(x + (size_t)(row0 + mm * 16) * D_ + col0 + n * 16);
  }
  WAIT_L(0); BAR();
  f32x4 sum[2][4];
  {
    LAS char* pp = lds + (quad * 2 + (kh ^ 1)) * 8192 + lane * 16;
#pragma unroll
    for (int mm = 0; mm < 2; ++mm)
#pragma unroll
      for (int n = 0; n < 4; ++n) sum[mm][n] = (kh ? acc[2 + mm][n] : acc[mm][n]) + *(LAS f32x4*)(pp + (mm * 4 + n) * 1024);
  }
  const int piece = kh * 2 + wc;
  if (!keep) {
    char* dst = xch + (size_t)b * 16384 + piece * 4096 + lane * 16;
#pragma unroll
    for (int mm = 0; mm < 2; ++mm)
#pragma unroll
      for (int h = 0; h < 2; ++h)
        st_sc1(dst + (mm * 2 + h) * 1024, (u32x4){pk2(sum[mm][2 * h][0], sum[mm][2 * h][1]), pk2(sum[mm][2 * h][2], sum[mm][2 * h][3]),
                                                  pk2(sum[mm][2 * h + 1][0], sum[mm][2 * h + 1][1]), pk2(sum[mm][2 * h + 1][2], sum[mm][2 * h + 1][3])});
    if (b == 0 && wid == 0) {
#pragma unroll
      for (int j = 0; j < 4; ++j) wait_flags(ws + WS_FLG + (j * 64 + lane) * FS, tag);
      if (lane == 0) __hip_atomic_store((gu64*)(ws + WS_EPOCH), tag, __ATOMIC_RELAXED, __HIP_MEMORY_SCOPE_AGENT);
    }
  } else {
    const char* src = xch + (size_t)(b ^ 1) * 16384 + piece * 4096 + lane * 16;
    u32x4 pv[4];
    for (unsigned spins = 0;; ++spins) {
      asm volatile(
          "global_load_dwordx4 %0, %4, off sc1\n\tglobal_load_dwordx4 %1, %4, off offset:1024 sc1\n\t"
          "global_load_dwordx4 %2, %4, off offset:2048 sc1\n\tglobal_load_dwordx4 %3, %4, off offset:3072 sc1\n\ts_waitcnt vmcnt(0)"
          : "=&v"(pv[0]), "=&v"(pv[1]), "=&v"(pv[2]), "=&v"(pv[3]) : "v"(src) : "memory");
      if (__all(fresh4(pv[0]) && fresh4(pv[1]) && fresh4(pv[2]) && fresh4(pv[3])) || spins > (1u << 14)) break;
      __builtin_amdgcn_s_sleep(1);
    }
#pragma unroll
    for (int mm = 0; mm < 2; ++mm)
#pragma unroll
      for (int n = 0; n < 4; ++n) {
        const unsigned q0 = pv[mm * 2 + (n >> 1)][2 * (n & 1)], q1 = pv[mm * 2 + (n >> 1)][2 * (n & 1) + 1];
        const f32x4 v = sum[mm][n];
        u32x2 o;
        o[0] = pk2(v[0] + blo(q0) + blo(xv[mm][n][0]) + blo(bv[n][0]), v[1] + bhi(q0) + bhi(xv[mm][n][0]) + bhi(bv[n][0]));
        o[1] = pk2(v[2] + blo(q1) + blo(xv[mm][n][1]) + blo(bv[n][1]), v[3] + bhi(q1) + bhi(xv[mm][n][1]) + bhi(bv[n][1]));
        *(u32x2*)(out + (size_t)(row0 + mm * 16) * D_ + col0 + n * 16) = o;
      }
  }
}

__global__ __launch_bounds__(512, 2) void k_main(const u16* __restrict__ x, const u16* __restrict__ wqkv, const u16* __restrict__ w1,
                                                 const u16* __restrict__ b1, const u16* __restrict__ wphi,
                                                 const u16* __restrict__ w2, const u16* __restrict__ wo, const u16* __restrict__ b2,
                                                 char* ws, u16* __restrict__ out) {
  extern __shared__ __attribute__((aligned(16))) char smem[];
  LAS char* lds = (LAS char*)smem;
  const int b = blockIdx.x, grp = b & 7, idx = b >> 3;
  const int panel = idx & 7, kind = grp & 1, tcol = (grp >> 1) * 4 + (idx >> 3);
  u64 tag;
  if (kind == 0) {
    tag = gemm1_body<true>(x, wqkv, tcol * 64, 0, panel, tcol, b1, wphi, ws, w2, wo, lds);
  } else {
    const int c0 = tcol * 224, woff = c0 & 63;
    tag = gemm1_body<false>(x, w1, c0 - woff, woff, panel, tcol, b1, wphi, ws, w2, wo, lds);
  }
  gemm2_body(ws, w2, wo, x, b2, out, tag, kind * 16 + tcol,   kind ^ 1, panel, panel * 2 + ((grp >> 1) & 1), (grp >> 2) * 4 + (idx >> 3), lds);
}

extern "C" void kernel_launch(void* const* d_in, const int* in_sizes, int n_in, void* d_out, int out_size, void* d_ws, size_t ws_size,
                              hipStream_t stream) {
  (void)hipFuncSetAttribute((const void*)k_main, hipFuncAttributeMaxDynamicSharedMemorySize, G1_LDS);
  k_main<<<256, 512, G1_LDS, stream>>>((const u16*)d_in[0], (const u16*)d_in[1], (const u16*)d_in[5], (const u16*)d_in[6], (const u16*)d_in[2],
                                       (const u16*)d_in[7], (const u16*)d_in[3], (const u16*)d_in[8], (char*)d_ws, (u16*)d_out);
}
```

```cpp
#include <hip/hip_runtime.h>
#include <hip/hip_bf16.h>
#include <stdint.h>

typedef __bf16 bf16x8 __attribute__((ext_vector_type(8)));
typedef __bf16 bf16x4 __attribute__((ext_vector_type(4)));
typedef __bf16 bf16x2 __attribute__((ext_vector_type(2)));
typedef float f32x4 __attribute__((ext_vector_type(4)));
typedef float f32x2 __attribute__((ext_vector_type(2)));
typedef unsigned u32x4 __attribute__((ext_vector_type(4)));
typedef unsigned u32x2 __attribute__((ext_vector_type(2)));
typedef unsigned short u16;
#define LAS __attribute__((address_space(3)))
#define DI __device__ __forceinline__

constexpr int S_ = 2048, D_ = 1024, F_ = 3584, H_ = 32;
constexpr int KCAT = F_ + D_;
constexpr int NKG1 = D_ / 32;
constexpr int NKG2 = KCAT / 32;

constexpr size_t WS_XN   = 0;
constexpr size_t WS_ACT  = WS_XN + (size_t)S_ * D_ * 2;
constexpr size_t WS_KVX  = WS_ACT + (size_t)S_ * KCAT * 2;
constexpr int KVX_TILE = 8192 + 256;
constexpr size_t KVX_BYTES = (size_t)8 * 16 * KVX_TILE;
constexpr size_t WS_XCH  = WS_KVX + KVX_BYTES;
constexpr size_t WS_FLG  = WS_XCH + (size_t)256 * 16384;
constexpr int FS = 8;
constexpr size_t WS_FLG2 = WS_FLG + 256 * FS, WS_EPOCH = WS_FLG + 512 * FS + 128;

DI float rbf(float x) { return (float)(__bf16)x; }
DI unsigned pk2(float lo, float hi) { bf16x2 v; v[0] = (__bf16)lo; v[1] = (__bf16)hi; return __builtin_bit_cast(unsigned, v); }
DI float blo(unsigned u) { return __uint_as_float(u << 16); }
DI float bhi(unsigned u) { return __uint_as_float(u & 0xffff0000u); }

#define MFMA16(a, b, c) __builtin_amdgcn_mfma_f32_16x16x32_bf16((a), (b), (c), 0, 0, 0)
#define WAIT_V(n) asm volatile("s_waitcnt vmcnt(%0)" ::"n"(n) : "memory")
#define WAIT_L(n) do { static_assert((n) == 0, ""); asm volatile("" ::: "memory"); __builtin_amdgcn_s_waitcnt(0xC07F); asm volatile("" ::: "memory"); } while (0)
#define BAR() do { asm volatile("" ::: "memory"); __builtin_amdgcn_s_barrier(); asm volatile("" ::: "memory"); } while (0)
#define SCHED() __builtin_amdgcn_sched_barrier(0)
template <int N> struct IC { static constexpr int v = N; };

DI void glds16_sc1(unsigned voff, const void* sbase, unsigned ldsaddr) {
  asm volatile("s_mov_b32 m0, %2\n\ts_nop 0\n\tglobal_load_lds_dwordx4 %0, %1 sc1" ::"v"(voff), "s"(sbase), "s"(ldsaddr) : "memory");
}
DI void glds16(unsigned voff, const void* sbase, unsigned ldsaddr) {
  asm volatile("s_mov_b32 m0, %2\n\ts_nop 0\n\tglobal_load_lds_dwordx4 %0, %1" ::"v"(voff), "s"(sbase), "s"(ldsaddr) : "memory");
}
DI const char* uniform_ptr(const char* p) {
  const unsigned long long a = (unsigned long long)p;
  unsigned lo = __builtin_amdgcn_readfirstlane((unsigned)a), hi = __builtin_amdgcn_readfirstlane((unsigned)(a >> 32));
  asm volatile("s_nop 4" : "+s"(lo), "+s"(hi));
  return (const char*)(((unsigned long long)hi << 32) | lo);
}
DI LAS char* opaque(LAS char* p) { asm volatile("" : "+v"(p)); return p; }
DI bf16x8 lds_b128(LAS char* p) { return *(LAS bf16x8*)p; }
DI bf16x4 lds_tr(LAS char* p) { return __builtin_amdgcn_ds_read_tr16_b64_v4bf16((LAS bf16x4*)p); }
DI bf16x8 cat8(bf16x4 a, bf16x4 b) { return __builtin_shufflevector(a, b, 0, 1, 2, 3, 4, 5, 6, 7); }
DI bf16x8 pack8(f32x4 a, f32x4 b) {
  bf16x8 v;
  v[0] = (__bf16)a[0]; v[1] = (__bf16)a[1]; v[2] = (__bf16)a[2]; v[3] = (__bf16)a[3];
  v[4] = (__bf16)b[0]; v[5] = (__bf16)b[1]; v[6] = (__bf16)b[2]; v[7] = (__bf16)b[3];
  return v;
}

typedef __attribute__((address_space(1))) unsigned gu32;
typedef unsigned long long u64;
typedef __attribute__((address_space(1))) u64 gu64;
DI void st_sc1(void* p, u32x4 v) { asm volatile("global_store_dwordx4 %0, %1, off sc1\n\ts_nop 1" ::"v"(p), "v"(v) : "memory"); }
DI unsigned umax4(u32x4 v, unsigned m) { return max(max(max(v[0], v[1]), v[2]), max(v[3], m)); }
DI void st8_sc1(void* p, u32x2 v) { asm volatile("global_store_dwordx2 %0, %1, off sc1\n\ts_nop 1" ::"v"(p), "v"(v) : "memory"); }
DI bool fresh4(u32x4 v) { return umax4(v, 0u) != 0xffffffffu; }
#define XRED4(OP, a1, b1, a2, b2) asm volatile( \
    "s_nop 1\n\tv_permlane16_swap_b32 %0, %1\n\tv_permlane16_swap_b32 %2, %3\n\t" \
    OP " %0, %0, %1\n\t" OP " %2, %2, %3\n\tv_mov_b32 %1, %0\n\tv_mov_b32 %3, %2\n\ts_nop 0\n\t" \
    "v_permlane32_swap_b32 %0, %1\n\tv_permlane32_swap_b32 %2, %3\n\t" \
    OP " %0, %0, %1\n\t" OP " %2, %2, %3\n\tv_mov_b32 %1, %0\n\tv_mov_b32 %3, %2\n\ts_nop 0\n\t" \
    "v_permlane16_swap_b32 %0, %1\n\tv_permlane16_swap_b32 %2, %3" \
    : "+v"(a1), "+v"(b1), "+v"(a2), "+v"(b2))
DI float max8(const float* v) {
  const float t1 = fmaxf(fmaxf(v[0], v[1]), v[2]), t2 = fmaxf(fmaxf(v[3], v[4]), v[5]), t3 = fmaxf(fmaxf(v[6], v[7]), t1);
  return fmaxf(t2, t3);
}
DI float min8(const float* v) {
  const float t1 = fminf(fminf(v[0], v[1]), v[2]), t2 = fminf(fminf(v[3], v[4]), v[5]), t3 = fminf(fminf(v[6], v[7]), t1);
  return fminf(t2, t3);
}
DI void dual_softmax2(const f32x4 (&p)[2][2], u32x4 (&pos)[2], u32x4 (&neg)[2]) {
  float v[2][8];
#pragma unroll
  for (int t = 0; t < 2; ++t)
#pragma unroll
    for (int h = 0; h < 2; ++h)
#pragma unroll
      for (int r = 0; r < 2; ++r) {
        unsigned u = pk2(p[t][h][2 * r], p[t][h][2 * r + 1]);
        asm("" : "+v"(u));
        v[t][4 * h + 2 * r] = blo(u); v[t][4 * h + 2 * r + 1] = bhi(u);
      }
  float mx0 = max8(v[0]), nm0 = -min8(v[0]), mx1 = max8(v[1]), nm1 = -min8(v[1]);
  XRED4("v_max_f32", mx0, nm0, mx1, nm1);
  constexpr float L2E = 1.44269504f;
  const f32x2 kp = {L2E, L2E}, kn = {-L2E, -L2E};
  f32x2 ep[2][4], en[2][4];
  float sp[2], sn[2];
#pragma unroll
  for (int t = 0; t < 2; ++t) {
    const float cpv = t ? mx1 : mx0, cnv = t ? nm1 : nm0;
    const f32x2 cp = {cpv, cpv}, cn = {cnv, cnv};
#pragma unroll
    for (int r = 0; r < 4; ++r) {
      const f32x2 x = {v[t][2 * r], v[t][2 * r + 1]};
      const f32x2 ap = (x - cp) * kp, an = (x + cn) * kn;
      ep[t][r] = (f32x2){__builtin_amdgcn_exp2f(ap[0]), __builtin_amdgcn_exp2f(ap[1])};
      en[t][r] = (f32x2){__builtin_amdgcn_exp2f(an[0]), __builtin_amdgcn_exp2f(an[1])};
    }
    const f32x2 s2p = (ep[t][0] + ep[t][1]) + (ep[t][2] + ep[t][3]), s2n = (en[t][0] + en[t][1]) + (en[t][2] + en[t][3]);
    sp[t] = s2p[0] + s2p[1]; sn[t] = s2n[0] + s2n[1];
  }
  XRED4("v_add_f32", sp[0], sn[0], sp[1], sn[1]);
#pragma unroll
  for (int t = 0; t < 2; ++t) {
    const float rp = __builtin_amdgcn_rcpf(sp[t]), rn = __builtin_amdgcn_rcpf(sn[t]);
    const f32x2 rp2 = {rp, rp}, rn2 = {rn, rn};
#pragma unroll
    for (int r = 0; r < 4; ++r) {
      const f32x2 a = ep[t][r] * rp2, b = en[t][r] * rn2;
      pos[t][r] = pk2(a[0], a[1]); neg[t][r] = pk2(b[0], b[1]);
    }
  }
}

DI float sum8lanes(float v) {
  v += __builtin_bit_cast(float, __builtin_amdgcn_update_dpp(0, __builtin_bit_cast(int, v), 0xB1, 0xf, 0xf, true));
  v += __builtin_bit_cast(float, __builtin_amdgcn_update_dpp(0, __builtin_bit_cast(int, v), 0x4E, 0xf, 0xf, true));
  v += __builtin_bit_cast(float, __builtin_amdgcn_update_dpp(0, __builtin_bit_cast(int, v), 0x141, 0xf, 0xf, true));
  return v;
}
DI void wait_flags(const char* p, u64 tag) {
  for (unsigned spins = 0; spins < (1u << 16); ++spins) {
    if (__all(__hip_atomic_load((const gu64*)p, __ATOMIC_RELAXED, __HIP_MEMORY_SCOPE_AGENT) == tag)) break;
    __builtin_amdgcn_s_sleep(1);
  }
}

constexpr int G1_SLOT = 16384;
constexpr int G1_B = 65536, G1_X = 131072;
constexpr int G1_LDS = G1_X + 4096 + 2048;

DI f32x2 gelu2(f32x2 x) {
  const f32x2 c1 = {-0.10294324f, -0.10294324f}, c0 = {-2.3022082f, -2.3022082f}, one = {1.0f, 1.0f};
  const f32x2 a = x * __builtin_elementwise_fma(x * x, c1, c0);
  const f32x2 d = one + (f32x2){__builtin_amdgcn_exp2f(a[0]), __builtin_amdgcn_exp2f(a[1])};
  return x * (f32x2){__builtin_amdgcn_rcpf(d[0]), __builtin_amdgcn_rcpf(d[1])};
}

template <bool QKV>
DI u64 gemm1_body(const u16* __restrict__ x, const u16* __restrict__ W, int wcol0, int woff, int panel, int tcol,
                  const u16* __restrict__ b1, const u16* __restrict__ wphi, char* ws,
                  const u16* __restrict__ w2, const u16* __restrict__ wo, LAS char* lds) {
  char* xn = ws + WS_XN;
  char* act = ws + WS_ACT;
  char* kvx = ws + WS_KVX;
  constexpr int LDW = QKV ? 3 * D_ : F_;
  constexpr int NG = QKV ? 3 : 4;
  constexpr int NT = QKV ? 6 : 7;
  const int tid = threadIdx.x, lane = tid & 63;
  const int wid = __builtin_amdgcn_readfirstlane(tid >> 6);
  const int wr = wid >> 1, wc = wid & 1;
  const int g = lane >> 4, i16 = lane & 15, q4 = i16 >> 2, p4 = lane & 3;
  const unsigned ldsb = (unsigned)(size_t)lds;

  const char* abase = uniform_ptr(xn + (size_t)panel * 16 * NKG1 * 1024);
  const unsigned avoff = (unsigned)(wid * 2) * NKG1 * 1024 + lane * 16;
  const char* bbase = (const char*)W;
  unsigned bvoff[2]; unsigned bdst[2];
#pragma unroll
  for (int j = 0; j < 2; ++j) {
    int pb = QKV ? (wid + 8 * j) : (wid * 2 + j);
    if (QKV && pb >= 12) pb -= 8;
    const int kg = pb / NG, cg = pb % NG;
    const int r = lane >> 3, sw = ((r >> 1) & 1) | ((kg & 1) << 1);
    const int cc = (lane & 7) ^ (sw << 1);
    const int col = QKV ? (cg * D_ + wcol0) : (wcol0 + cg * 64);
    bvoff[j] = (unsigned)(((kg * 8 + r) * LDW + col + cc * 8) * 2);
    bdst[j] = ldsb + G1_B + pb * 1024;
  }
  const unsigned adst = ldsb + wid * 2048;

  LAS char* ard = opaque(lds + wr * 4096 + lane * 16);
  constexpr int NB = QKV ? 2 : 4;
  LAS char* brd[NB];
#pragma unroll
  for (int j = 0; j < NB; ++j) {
    const int n0 = QKV ? (wc * 32 + j * 16) : (woff + wc * 112 + j * 16);
    const int sw = (q4 >> 1) | ((g & 1) << 1);
    brd[j] = opaque(lds + G1_B + (g * NG + (n0 >> 6)) * 1024 + q4 * 128 + (((((n0 & 63) >> 3) ^ (sw << 1)) + (p4 >> 1)) << 4) + (p4 & 1) * 8);
  }

  f32x4 acc[4][NT];
#pragma unroll
  for (int m = 0; m < 4; ++m)
#pragma unroll
    for (int n = 0; n < NT; ++n) acc[m][n] = (f32x4){0.f, 0.f, 0.f, 0.f};

  bf16x8 fa[2][4], fb[2][NT];

  auto issue = [&](int q, int stage, int slot) {
    if (q < 2) glds16_sc1(avoff + q * NKG1 * 1024, abase + (size_t)stage * 1024, adst + q * 1024 + slot * G1_SLOT);
    else       glds16(bvoff[q - 2], bbase + (size_t)stage * 64 * LDW, bdst[q - 2] + slot * G1_SLOT);
  };
  auto rdA = [&](auto P, auto SL, int m) { fa[P.v][m] = lds_b128(ard + SL.v * G1_SLOT + m * 1024); };
  auto rdB = [&](auto P, auto SL, int n) { LAS char* p = brd[n % NB] + (n / NB) * 1024 + SL.v * G1_SLOT; fb[P.v][n] = cat8(lds_tr(p), lds_tr(p + 512)); };
  auto mma4 = [&](auto P, int n) {
#pragma unroll
    for (int m = 0; m < 4; ++m) {
      if (QKV && n >= 4) acc[m][n] = MFMA16(fa[P.v][m], fb[P.v][n], acc[m][n]);
      else               acc[m][n] = MFMA16(fb[P.v][n], fa[P.v][m], acc[m][n]);
    }
  };
  auto step = [&](auto SC, auto VM, auto GL, auto RD, int s) {
    constexpr int P = SC.v & 1, SNv = (SC.v + 1) & 3;
    const IC<P> Pc; const IC<P ^ 1> Pn; const IC<SNv> SN;
    if constexpr (RD.v) { WAIT_V(VM.v); WAIT_L(0); BAR(); }
    SCHED();
    mma4(Pc, 0); SCHED();
    if constexpr (RD.v) { rdA(Pn, SN, 0); rdA(Pn, SN, 1); rdB(Pn, SN, 0); }
    if constexpr (GL.v) issue(0, s + 4, SC.v);
    SCHED();
    mma4(Pc, 1); SCHED();
    if constexpr (RD.v) { rdA(Pn, SN, 2); rdA(Pn, SN, 3); rdB(Pn, SN, 1); }
    if constexpr (GL.v) issue(1, s + 4, SC.v);
    SCHED();
    mma4(Pc, 2); SCHED();
    if constexpr (RD.v) { rdB(Pn, SN, 2); rdB(Pn, SN, 3); }
    if constexpr (GL.v) issue(2, s + 4, SC.v);
    SCHED();
    mma4(Pc, 3); SCHED();
    if constexpr (RD.v) { rdB(Pn, SN, 4); rdB(Pn, SN, 5); if constexpr (NT == 7) rdB(Pn, SN, 6); }
    if constexpr (GL.v) issue(3, s + 4, SC.v);
    SCHED();
    mma4(Pc, 4); SCHED();
    mma4(Pc, 5);
    if constexpr (NT == 7) mma4(Pc, 6);
    SCHED();
  };

  const int slot = (QKV ? 0 : 16) + tcol;
  u64 tag;
  {
    const int r8 = lane >> 3, c8 = lane & 7;
    const int row = panel * 256 + slot * 8 + r8;
    const u16* src = x + (size_t)row * D_ + wid * 128 + c8 * 8;
    u32x4 v0, v1; u32x2 ep;
    asm volatile("global_load_dwordx4 %0, %3, off\n\tglobal_load_dwordx4 %1, %3, off offset:128\n\tglobal_load_dwordx2 %2, %4, off sc1"
                 : "=&v"(v0), "=&v"(v1), "=&v"(ep) : "v"(src), "v"(ws + WS_EPOCH) : "memory");
    SCHED();
    asm volatile("s_waitcnt vmcnt(0)" : "+v"(v0), "+v"(v1), "+v"(ep) :: "memory");
    SCHED();
    if (QKV) {
      const u32x4 fill = {~0u, ~0u, ~0u, ~0u};
      char* kp = kvx + (size_t)(panel * 16 + tcol) * KVX_TILE;
      st_sc1(kp + tid * 16, fill);
      st_sc1(kp + 8192 + i16 * 16, fill);
    }
    tag = (((u64)(unsigned)__builtin_amdgcn_readfirstlane(ep[1]) << 32) | (unsigned)__builtin_amdgcn_readfirstlane(ep[0])) + 1;
    f32x2 f[8];
#pragma unroll
    for (int i = 0; i < 4; ++i) { f[i] = (f32x2){blo(v0[i]), bhi(v0[i])}; f[4 + i] = (f32x2){blo(v1[i]), bhi(v1[i])}; }
    const f32x2 s2 = ((f[0] + f[1]) + (f[2] + f[3])) + ((f[4] + f[5]) + (f[6] + f[7]));
    const float mw = sum8lanes(s2[0] + s2[1]) * (1.0f / 128.f);
    f32x2 q2 = {0.f, 0.f};
#pragma unroll
    for (int i = 0; i < 8; ++i) { const f32x2 d = f[i] - (f32x2){mw, mw}; q2 = __builtin_elementwise_fma(d, d, q2); }
    const float qq = sum8lanes(q2[0] + q2[1]);
    LAS f32x2* stt = (LAS f32x2*)(lds + G1_X + 4096);
    if (c8 == 0) stt[r8 * 8 + wid] = (f32x2){mw, qq};
    WAIT_L(0); BAR();
    const f32x2 pw = stt[r8 * 8 + c8];
    const float mean = sum8lanes(pw[0]) * 0.125f;
    const float dm = pw[0] - mean;
    const float m2 = sum8lanes(__builtin_fmaf(128.f * dm, dm, pw[1]));
    const float mb = rbf(mean), vb = rbf(m2 * (1.0f / D_));
    const float rs = 1.0f / rbf(sqrtf(rbf(vb + 1e-5f)));
    u32x4 o0, o1;
#pragma unroll
    for (int i = 0; i < 8; ++i) {
      const f32x2 d = f[i] - (f32x2){mb, mb};
      unsigned u = pk2(d[0], d[1]);
      asm("" : "+v"(u));
      const f32x2 y = (f32x2){blo(u), bhi(u)} * (f32x2){rs, rs};
      if (i < 4) o0[i] = pk2(y[0], y[1]); else o1[i - 4] = pk2(y[0], y[1]);
    }
    char* dst = xn + ((size_t)((row >> 4) * NKG1 + wid * 4 + (c8 >> 2)) * 64 + (c8 & 3) * 16 + (row & 15)) * 16;
    st_sc1(dst, o0); st_sc1(dst + 2048, o1);
    WAIT_V(0); BAR();
    if (tid == 0) __hip_atomic_store((gu64*)(ws + WS_FLG + (panel * 32 + slot) * FS), tag, __ATOMIC_RELAXED, __HIP_MEMORY_SCOPE_AGENT);
    if (QKV && wid >= 4) glds16((unsigned)lane * 16, uniform_ptr((const char*)wphi + (size_t)tcol * 4096 + (wid - 4) * 1024), ldsb + G1_X + (wid - 4) * 1024);
    if (!QKV && wid == 4) glds16((unsigned)(lane < 28 ? lane : 27) * 16, uniform_ptr((const char*)(b1 + tcol * 224)), ldsb + G1_X);
#pragma unroll
    for (int st = 0; st < 4; ++st) { issue(2, st, st); issue(3, st, st); }
    if (wid == 0) wait_flags(ws + WS_FLG + ((QKV && lane >= 32) ? ((lane & 7) * 32 + slot) : (panel * 32 + (lane & 31))) * FS, tag);
    BAR();
  }
#pragma unroll
  for (int st = 0; st < 4; ++st) { issue(0, st, st); issue(1, st, st); }
  WAIT_V(2); BAR();
  {
    const IC<0> P0; const IC<0> S0;
#pragma unroll
    for (int m = 0; m < 4; ++m) rdA(P0, S0, m);
#pragma unroll
    for (int n = 0; n < NT; ++n) rdB(P0, S0, n);
  }
#pragma unroll 1
  for (int s = 0; s < 28; s += 4) {
    if (s == 8) {
      const int ln = __builtin_amdgcn_mbcnt_hi(~0u, __builtin_amdgcn_mbcnt_lo(~0u, 0u));
      char* xp = ws + WS_XCH + (size_t)blockIdx.x * 16384 + (wid * 64 + ln) * 16;
      const u32x4 fill = {~0u, ~0u, ~0u, ~0u};
      st_sc1(xp, fill); st_sc1(xp + 8192, fill);
    }
    step(IC<0>{}, IC<8>{}, IC<1>{}, IC<1>{}, s);
    step(IC<1>{}, IC<8>{}, IC<1>{}, IC<1>{}, s + 1);
    step(IC<2>{}, IC<8>{}, IC<1>{}, IC<1>{}, s + 2);
    step(IC<3>{}, IC<8>{}, IC<1>{}, IC<1>{}, s + 3);
  }
  step(IC<0>{}, IC<8>{}, IC<0>{}, IC<1>{}, 28);
  constexpr int NX = 1;
  {
    int wv = blockIdx.x * 8 + wid;
    if (wv >= 1152) wv -= 1152;
    const char* src = uniform_ptr(wv < 896 ? (const char*)w2 + (size_t)wv * 8192 : (const char*)wo + (size_t)(wv - 896) * 8192);
    asm volatile("s_mov_b32 m0, %2\n\ts_nop 0\n\tglobal_load_lds_dword %0, %1" ::"v"(lane * 128), "s"(src), "s"(ldsb + G1_X + 4096 + wid * 256) : "memory");
  }
  step(IC<1>{}, IC<4 + NX>{}, IC<0>{}, IC<1>{}, 29);
  step(IC<2>{}, IC<0 + NX>{}, IC<0>{}, IC<1>{}, 30);
  step(IC<3>{}, IC<0>{}, IC<0>{}, IC<0>{}, 31);

  if (!QKV) {
    u32x2 bb[NT];
#pragma unroll
    for (int n = 0; n < NT; ++n) bb[n] = *(LAS u32x2*)(lds + G1_X + (wc * 112 + 4 * g + n * 16) * 2);
    auto act4 = [&](int m, int n) {
      const f32x2 yA = gelu2((f32x2){acc[m][n][0] + blo(bb[n][0]), acc[m][n][1] + bhi(bb[n][0])});
      const f32x2 yB = gelu2((f32x2){acc[m][n][2] + blo(bb[n][1]), acc[m][n][3] + bhi(bb[n][1])});
      return (u32x2){pk2(yA[0], yA[1]), pk2(yB[0], yB[1])};
    };
    const int rt0 = panel * 16 + wr * 4;
    char* obase = (char*)act + ((size_t)rt0 * NKG2 + tcol * 7) * 1024 + lane * 16;
    if (wc == 0) {
#pragma unroll
      for (int m = 0; m < 4; ++m) {
        char* rb = obase + (size_t)m * NKG2 * 1024;
#pragma unroll
        for (int p = 0; p < 3; ++p) { const u32x2 lo = act4(m, 2 * p), hi = act4(m, 2 * p + 1); st_sc1(rb + p * 1024, (u32x4){lo[0], lo[1], hi[0], hi[1]}); }
        st8_sc1(rb + 3 * 1024, act4(m, 6 % NT));
      }
    } else {
#pragma unroll
      for (int m = 0; m < 4; ++m) {
        char* rb = obase + (size_t)m * NKG2 * 1024;
        st8_sc1(rb + 3 * 1024 + 8, act4(m, 0));
#pragma unroll
        for (int p = 0; p < 3; ++p) { const u32x2 lo = act4(m, 2 * p + 1), hi = act4(m, (2 * p + 2) % NT); st_sc1(rb + (4 + p) * 1024, (u32x4){lo[0], lo[1], hi[0], hi[1]}); }
      }
    }
  } else {
    const int head = tcol * 2 + wc;
    LAS char* wp = lds + G1_X + wc * 2048;
    bf16x8 wf[2];
#pragma unroll
    for (int ft = 0; ft < 2; ++ft) {
      LAS char* p = wp + (4 * g + q4) * 64 + (16 * ft + 4 * p4) * 2;
      wf[ft] = cat8(lds_tr(p), lds_tr(p + 16 * 64));
    }
    const f32x4 z4 = {0.f, 0.f, 0.f, 0.f};
    LAS char* sc = lds + (wid >> 2) * G1_B + (wid & 3) * 8704;
#pragma unroll
    for (int mp = 0; mp < 2; ++mp) {
      f32x4 pp[2][2]; u32x4 pos[2], neg[2];
#pragma unroll
      for (int t = 0; t < 2; ++t) {
        const bf16x8 bk = pack8(acc[2 * mp + t][2], acc[2 * mp + t][3]);
        pp[t][0] = MFMA16(wf[0], bk, z4); pp[t][1] = MFMA16(wf[1], bk, z4);
      }
      dual_softmax2(pp, pos, neg);
#pragma unroll
      for (int t = 0; t < 2; ++t) {
        LAS char* rowp = sc + (16 * (2 * mp + t) + i16) * 136 + 8 * g;
        *(LAS u32x2*)(rowp) = (u32x2){pos[t][0], pos[t][1]};      *(LAS u32x2*)(rowp + 32) = (u32x2){pos[t][2], pos[t][3]};
        *(LAS u32x2*)(rowp + 64) = (u32x2){neg[t][0], neg[t][1]}; *(LAS u32x2*)(rowp + 96) = (u32x2){neg[t][2], neg[t][3]};
      }
    }
    WAIT_L(0);
    f32x4 kv[4][3];
#pragma unroll
    for (int ft = 0; ft < 4; ++ft)
#pragma unroll
      for (int d = 0; d < 3; ++d) kv[ft][d] = z4;
    bf16x8 ones;
#pragma unroll
    for (int j = 0; j < 8; ++j) ones[j] = (__bf16)1.0f;
#pragma unroll
    for (int ks = 0; ks < 2; ++ks) {
      const bf16x8 bv0 = pack8(acc[2 * ks][4], acc[2 * ks + 1][4]);
      const bf16x8 bv1 = pack8(acc[2 * ks][5], acc[2 * ks + 1][5]);
#pragma unroll
      for (int ft = 0; ft < 4; ++ft) {
        LAS char* p = sc + (32 * ks + 4 * g + q4) * 136 + (16 * ft + 4 * p4) * 2;
        const bf16x8 a = cat8(lds_tr(p), lds_tr(p + 16 * 136));
        kv[ft][0] = MFMA16(a, bv0, kv[ft][0]);
        kv[ft][1] = MFMA16(a, bv1, kv[ft][1]);
        kv[ft][2] = MFMA16(a, ones, kv[ft][2]);
      }
    }
    WAIT_L(0); BAR();
    {
      LAS char* pp = lds + wid * 8448;
#pragma unroll
      for (int sg = 0; sg < 2; ++sg) {
#pragma unroll
        for (int d = 0; d < 2; ++d) {
          *(LAS f32x4*)(pp + (sg * 2 + d) * 2048 + lane * 16) = kv[2 * sg][d];
          *(LAS f32x4*)(pp + (sg * 2 + d) * 2048 + 1024 + lane * 16) = kv[2 * sg + 1][d];
        }
        if (i16 == 0) {
          *(LAS f32x4*)(pp + 8192 + sg * 128 + g * 16) = kv[2 * sg][2];
          *(LAS f32x4*)(pp + 8192 + sg * 128 + 64 + g * 16) = kv[2 * sg + 1][2];
        }
      }
    }
    WAIT_L(0); BAR();
    char* kvt = kvx + (size_t)tcol * KVX_TILE;
    char* kvmine = kvt + (size_t)panel * 16 * KVX_TILE;
    {
      LAS char* pp = lds + (tid >> 8) * 8448 + ((tid >> 6) & 3) * 2048 + lane * 16;
      f32x4 lo = *(LAS f32x4*)(pp), hi = *(LAS f32x4*)(pp + 1024);
#pragma unroll
      for (int w = 1; w < 4; ++w) { lo += *(LAS f32x4*)(pp + w * 16896); hi += *(LAS f32x4*)(pp + w * 16896 + 1024); }
      st_sc1(kvmine + tid * 16, (u32x4){pk2(lo[0], lo[1]), pk2(lo[2], lo[3]), pk2(hi[0], hi[1]), pk2(hi[2], hi[3])});
    }
    if (tid < 16) {
      LAS char* pp = lds + (tid >> 3) * 8448 + 8192 + ((tid >> 2) & 1) * 128 + (tid & 3) * 16;
      f32x4 lo = *(LAS f32x4*)(pp), hi = *(LAS f32x4*)(pp + 64);
#pragma unroll
      for (int w = 1; w < 4; ++w) { lo += *(LAS f32x4*)(pp + w * 16896); hi += *(LAS f32x4*)(pp + w * 16896 + 64); }
      st_sc1(kvmine + 8192 + tid * 16, (u32x4){pk2(lo[0], lo[1]), pk2(lo[2], lo[3]), pk2(hi[0], hi[1]), pk2(hi[2], hi[3])});
    }
    u32x4 qp[2][2], qn[2][2];
    auto qsm = [&](int mp) {
      f32x4 pp[2][2];
#pragma unroll
      for (int t = 0; t < 2; ++t) {
        const bf16x8 bq = pack8(acc[2 * mp + t][0], acc[2 * mp + t][1]);
        pp[t][0] = MFMA16(wf[0], bq, z4); pp[t][1] = MFMA16(wf[1], bq, z4);
      }
      dual_softmax2(pp, qp[mp], qn[mp]);
    };
    qsm(0);
    const char* kb0 = uniform_ptr(kvt), * kb1 = uniform_ptr(kvt + (size_t)32 * KVX_TILE);
    const char* kb2 = uniform_ptr(kvt + (size_t)64 * KVX_TILE), * kb3 = uniform_ptr(kvt + (size_t)96 * KVX_TILE);
    u32x4 pv[8], pz[8];
#define KV_GATHER(pv, OFF, TAIL) asm volatile( \
          "global_load_dwordx4 %0, %8, %10 sc1\n\tglobal_load_dwordx4 %1, %9, %10 sc1\n\t" \
          "global_load_dwordx4 %2, %8, %11 sc1\n\tglobal_load_dwordx4 %3, %9, %11 sc1\n\t" \
          "global_load_dwordx4 %4, %8, %12 sc1\n\tglobal_load_dwordx4 %5, %9, %12 sc1\n\t" \
          "global_load_dwordx4 %6, %8, %13 sc1\n\tglobal_load_dwordx4 %7, %9, %13 sc1" TAIL \
          : "=&v"(pv[0]), "=&v"(pv[1]), "=&v"(pv[2]), "=&v"(pv[3]), "=&v"(pv[4]), "=&v"(pv[5]), "=&v"(pv[6]), "=&v"(pv[7]) \
          : "v"(OFF), "v"((OFF) + 16 * KVX_TILE), "s"(kb0), "s"(kb1), "s"(kb2), "s"(kb3) : "memory")
#define KV_WAIT(pv) asm volatile("s_waitcnt vmcnt(0)" \
          : "+v"(pv[0]), "+v"(pv[1]), "+v"(pv[2]), "+v"(pv[3]), "+v"(pv[4]), "+v"(pv[5]), "+v"(pv[6]), "+v"(pv[7]) :: "memory")
    LAS char* kvs = lds + 98304;
    auto finish = [&](u32x4 (&pv)[8], unsigned off) {
      for (unsigned spins = 0;; ++spins) {
        unsigned m = 0;
#pragma unroll
        for (int p = 0; p < 8; ++p) m = umax4(pv[p], m);
        if (__all(m != 0xffffffffu) || spins > (1u << 14)) break;
        __builtin_amdgcn_s_sleep(2);
        KV_GATHER(pv, off, "\n\ts_waitcnt vmcnt(0)");
      }
      f32x2 t[4];
#pragma unroll
      for (int r = 0; r < 4; ++r) t[r] = (f32x2){blo(pv[0][r]), bhi(pv[0][r])};
#pragma unroll
      for (int p = 1; p < 8; ++p)
#pragma unroll
        for (int r = 0; r < 4; ++r) t[r] += (f32x2){blo(pv[p][r]), bhi(pv[p][r])};
      *(LAS u32x4*)(kvs + off) = (u32x4){pk2(t[0][0], t[0][1]), pk2(t[1][0], t[1][1]), pk2(t[2][0], t[2][1]), pk2(t[3][0], t[3][1])};
    };
    const unsigned go = (unsigned)tid * 16, gz = 8192u + (unsigned)i16 * 16;
    SCHED();
    KV_GATHER(pv, go, "");
    KV_GATHER(pz, gz, "");
    SCHED();
    qsm(1);
    SCHED();
    KV_WAIT(pv);
    KV_WAIT(pz);
    SCHED();
    finish(pv, go);
    if (wid == 0) finish(pz, gz);
    WAIT_L(0); BAR();
    bf16x8 a[2][3];
#pragma unroll
    for (int sg = 0; sg < 2; ++sg) {
#pragma unroll
      for (int d = 0; d < 2; ++d) a[sg][d] = lds_b128(kvs + wc * 4096 + (sg * 2 + d) * 1024 + lane * 16);
      a[sg][2] = lds_b128(kvs + 8192 + wc * 128 + sg * 64 + g * 16);
    }
#pragma unroll
    for (int m = 0; m < 4; ++m) {
      const bf16x8 bp = __builtin_bit_cast(bf16x8, qp[m >> 1][m & 1]), bn = __builtin_bit_cast(bf16x8, qn[m >> 1][m & 1]);
      f32x4 o0 = MFMA16(a[0][0], bp, z4), o1 = MFMA16(a[0][1], bp, z4), dn = MFMA16(a[0][2], bp, z4);
      o0 = MFMA16(a[1][0], bn, o0); o1 = MFMA16(a[1][1], bn, o1); dn = MFMA16(a[1][2], bn, dn);
      const float rd = __builtin_amdgcn_rcpf(dn[0] + 1e-6f);
      const int rt = panel * 16 + wr * 4 + m;
      u32x4 w;
      w[0] = pk2(o0[0] * rd, o0[1] * rd); w[1] = pk2(o0[2] * rd, o0[3] * rd);
      w[2] = pk2(o1[0] * rd, o1[1] * rd); w[3] = pk2(o1[2] * rd, o1[3] * rd);
      st_sc1((char*)act + ((size_t)rt * NKG2 + 112 + head) * 1024 + lane * 16, w);
    }
  }
  return tag;
}

constexpr int G2_SLOT = 32768;
constexpr int G2_POLL = 4 * G2_SLOT + 4096;
constexpr int KSPLIT = 36;

DI void gemm2_body(char* ws, const u16* __restrict__ w2, const u16* __restrict__ wo,
                   const u16* __restrict__ x, const u16* __restrict__ b2, u16* __restrict__ out,
                   u64 tag, int slot, int split, int panel, int pm, int pn, LAS char* lds) {
  const char* act = ws + WS_ACT;
  char* xch = ws + WS_XCH;
  char* flg2 = ws + WS_FLG2 + panel * 32 * FS;
  const unsigned ldsb = (unsigned)(size_t)lds;
  int lane = __builtin_amdgcn_mbcnt_hi(~0u, __builtin_amdgcn_mbcnt_lo(~0u, 0u));
  asm volatile("" : "+v"(lane));
  const int wid = __builtin_amdgcn_readfirstlane(threadIdx.x >> 6);
  const int kh = wid >> 2, wr = (wid >> 1) & 1, wc = wid & 1;
  const int g = lane >> 4, i16 = lane & 15, q4 = i16 >> 2, p4 = lane & 3;
  const int b = blockIdx.x;
  const int kt0 = split ? KSPLIT : 0, nst = split ? 72 - KSPLIT : KSPLIT;
  const char* abase = uniform_ptr(act + ((size_t)(pm * 8) * NKG2 + kt0 * 2) * 1024);
  const unsigned avoff = (unsigned)wid * NKG2 * 1024 + lane * 16;
  const unsigned adst = ldsb + wid * 1024;
  const int r8 = lane >> 3;
  const unsigned bvoff = (unsigned)((wid * 8 + r8) * D_ + pn * 128 + (((lane & 7) ^ (((r8 >> 1) & 3) << 1)) << 3)) * 2;
  const unsigned bdst = ldsb + 16384 + wid * 2048;
  const char* wb = uniform_ptr((const char*)w2 + (size_t)kt0 * 64 * D_ * 2);
  auto issue = [&](int q, int t, int slot) {
    if (q < 2) glds16_sc1(avoff + q * 1024, abase + (size_t)t * 2048, adst + q * 8192 + slot * G2_SLOT);
    else       glds16(bvoff + (q - 2) * 128, wb + (size_t)t * 64 * D_ * 2, bdst + (q - 2) * 1024 + slot * G2_SLOT);
  };
  WAIT_V(0); WAIT_L(0); BAR();
  if (threadIdx.x == 0) __hip_atomic_store((gu64*)(flg2 + slot * FS), tag, __ATOMIC_RELAXED, __HIP_MEMORY_SCOPE_AGENT);
  if (wid == panel) wait_flags(flg2 + (16 + (split ? 10 + (i16 < 6 ? i16 : 0) : (i16 < 11 ? i16 : 0))) * FS, tag);
#pragma unroll
  for (int st = 0; st < 4; ++st) { issue(2, st, st); issue(3, st, st); }
  BAR();
#pragma unroll
  for (int st = 0; st < 4; ++st) { issue(0, st, st); issue(1, st, st); }

  LAS char* ard[2];
  ard[0] = opaque(lds + kh * 8192 + wr * 4096 + lane * 16); ard[1] = opaque(ard[0] + 65536);
  LAS char* brd[2][4];
#pragma unroll
  for (int j = 0; j < 4; ++j) {
    const int r = 4 * (g & 1) + q4, sw = (r >> 1) & 3;
    brd[0][j] = opaque(lds + 16384 + ((kh * 4 + (g >> 1)) * 2 + wc) * 1024 + r * 128 + ((((2 * j) ^ (sw << 1)) + (p4 >> 1)) << 4) + (p4 & 1) * 8);
    brd[1][j] = opaque(brd[0][j] + 65536);
  }
  f32x4 acc[4][4];
#pragma unroll
  for (int m = 0; m < 4; ++m)
#pragma unroll
    for (int n = 0; n < 4; ++n) acc[m][n] = (f32x4){0.f, 0.f, 0.f, 0.f};
  bf16x8 fa[2][4], fb[2][4];
  auto rdA = [&](auto P, auto SL, int m) { fa[P.v][m] = lds_b128(ard[SL.v >> 1] + (SL.v & 1) * G2_SLOT + m * 1024); };
  auto rdB = [&](auto P, auto SL, int n) { LAS char* p = brd[SL.v >> 1][n] + (SL.v & 1) * G2_SLOT; fb[P.v][n] = cat8(lds_tr(p), lds_tr(p + 4096)); };
  auto mma4 = [&](auto P, int n) {
#pragma unroll
    for (int m = 0; m < 4; ++m) acc[m][n] = MFMA16(fb[P.v][n], fa[P.v][m], acc[m][n]);
  };
  auto step = [&](auto SC, auto VM, auto GL, auto RD, int t) {
    constexpr int P = SC.v & 1, SNv = (SC.v + 1) & 3;
    const IC<P> Pc; const IC<P ^ 1> Pn; const IC<SNv> SN;
    if constexpr (RD.v) { WAIT_V(VM.v); WAIT_L(0); BAR(); }
    SCHED();
    mma4(Pc, 0); SCHED();
    if constexpr (RD.v) { rdA(Pn, SN, 0); rdA(Pn, SN, 1); rdA(Pn, SN, 2); rdA(Pn, SN, 3); rdB(Pn, SN, 0); }
    if constexpr (GL.v) issue(0, t + 4, SC.v);
    SCHED();
    mma4(Pc, 1); SCHED();
    if constexpr (RD.v) { rdB(Pn, SN, 1); rdB(Pn, SN, 2); rdB(Pn, SN, 3); }
    if constexpr (GL.v) issue(1, t + 4, SC.v);
    SCHED();
    mma4(Pc, 2); SCHED();
    if constexpr (GL.v) { issue(2, t + 4, SC.v); issue(3, t + 4, SC.v); }
    SCHED();
    mma4(Pc, 3); SCHED();
  };
  WAIT_V(2); BAR();
  {
    const IC<0> P0; const IC<0> S0;
#pragma unroll
    for (int m = 0; m < 4; ++m) rdA(P0, S0, m);
#pragma unroll
    for (int n = 0; n < 4; ++n) rdB(P0, S0, n);
  }
  const IC<1> Y; const IC<0> N_; const IC<8> V8;
  const int tp = split ? 8 : 24;
#pragma unroll 1
  for (int t = 0; t < nst - 4; t += 4) {
    if (wid == 0) {
      if (t == tp) {
        asm volatile("s_mov_b32 m0, %2\n\ts_nop 0\n\tglobal_load_lds_dword %0, %1 sc1" ::"v"((lane >> 1) * FS + (lane & 1) * 4), "s"(uniform_ptr(flg2)), "s"(ldsb + G2_POLL) : "memory");
      } else if (t == tp + 4) {
        if (!__all(*(LAS u64*)(lds + G2_POLL + (lane & 31) * 8) == tag)) wait_flags(flg2 + (lane & 31) * FS, tag);
      }
    }
    if (t == 52 - KSPLIT && split == 1) wb = uniform_ptr((const char*)wo - (size_t)(56 - KSPLIT) * 64 * D_ * 2);
    step(IC<0>{}, V8, Y, Y, t); step(IC<1>{}, V8, Y, Y, t + 1); step(IC<2>{}, V8, Y, Y, t + 2); step(IC<3>{}, V8, Y, Y, t + 3);
  }
  step(IC<0>{}, V8, N_, Y, 0);
  step(IC<1>{}, IC<4>{}, N_, Y, 0);
  step(IC<2>{}, IC<0>{}, N_, Y, 0);
  step(IC<3>{}, IC<0>{}, N_, N_, 0);
  const int quad = wid & 3;
  {
    LAS char* pp = lds + (quad * 2 + kh) * 8192 + lane * 16;
#pragma unroll
    for (int mm = 0; mm < 2; ++mm)
#pragma unroll
      for (int n = 0; n < 4; ++n) *(LAS f32x4*)(pp + (mm * 4 + n) * 1024) = kh ? acc[mm][n] : acc[2 + mm][n];
  }
  const bool keep = (wr == split);
  const int row0 = pm * 128 + wr * 64 + kh * 32 + i16, col0 = pn * 128 + wc * 64 + 4 * g;
  u32x2 xv[2][4], bv[4];
  if (keep) {
#pragma unroll
    for (int n = 0; n < 4; ++n) bv[n] = *(const u32x2*)(b2 + col0 + n * 16);
#pragma unroll
    for (int mm = 0; mm < 2; ++mm)
#pragma unroll
      for (int n = 0; n < 4; ++n) xv[mm][n] = *(const u32x2*)(x + (size_t)(row0 + mm * 16) * D_ + col0 + n * 16);
  }
  WAIT_L(0); BAR();
  f32x4 sum[2][4];
  {
    LAS char* pp = lds + (quad * 2 + (kh ^ 1)) * 8192 + lane * 16;
#pragma unroll
    for (int mm = 0; mm < 2; ++mm)
#pragma unroll
      for (int n = 0; n < 4; ++n) sum[mm][n] = (kh ? acc[2 + mm][n] : acc[mm][n]) + *(LAS f32x4*)(pp + (mm * 4 + n) * 1024);
  }
  const int piece = kh * 2 + wc;
  if (!keep) {
    char* dst = xch + (size_t)b * 16384 + piece * 4096 + lane * 16;
#pragma unroll
    for (int mm = 0; mm < 2; ++mm)
#pragma unroll
      for (int h = 0; h < 2; ++h)
        st_sc1(dst + (mm * 2 + h) * 1024, (u32x4){pk2(sum[mm][2 * h][0], sum[mm][2 * h][1]), pk2(sum[mm][2 * h][2], sum[mm][2 * h][3]),
                                                  pk2(sum[mm][2 * h + 1][0], sum[mm][2 * h + 1][1]), pk2(sum[mm][2 * h + 1][2], sum[mm][2 * h + 1][3])});
    if (b == 0 && wid == 0) {
#pragma unroll
      for (int j = 0; j < 4; ++j) wait_flags(ws + WS_FLG + (j * 64 + lane) * FS, tag);
      if (lane == 0) __hip_atomic_store((gu64*)(ws + WS_EPOCH), tag, __ATOMIC_RELAXED, __HIP_MEMORY_SCOPE_AGENT);
    }
  } else {
    const char* src = xch + (size_t)(b ^ 1) * 16384 + piece * 4096 + lane * 16;
    u32x4 pv[4];
    for (unsigned spins = 0;; ++spins) {
      asm volatile(
          "global_load_dwordx4 %0, %4, off sc1\n\tglobal_load_dwordx4 %1, %4, off offset:1024 sc1\n\t"
          "global_load_dwordx4 %2, %4, off offset:2048 sc1\n\tglobal_load_dwordx4 %3, %4, off offset:3072 sc1\n\ts_waitcnt vmcnt(0)"
          : "=&v"(pv[0]), "=&v"(pv[1]), "=&v"(pv[2]), "=&v"(pv[3]) : "v"(src) : "memory");
      if (__all(fresh4(pv[0]) && fresh4(pv[1]) && fresh4(pv[2]) && fresh4(pv[3])) || spins > (1u << 14)) break;
      __builtin_amdgcn_s_sleep(1);
    }
#pragma unroll
    for (int mm = 0; mm < 2; ++mm)
#pragma unroll
      for (int n = 0; n < 4; ++n) {
        const unsigned q0 = pv[mm * 2 + (n >> 1)][2 * (n & 1)], q1 = pv[mm * 2 + (n >> 1)][2 * (n & 1) + 1];
        const f32x4 v = sum[mm][n];
        u32x2 o;
        o[0] = pk2(v[0] + blo(q0) + blo(xv[mm][n][0]) + blo(bv[n][0]), v[1] + bhi(q0) + bhi(xv[mm][n][0]) + bhi(bv[n][0]));
        o[1] = pk2(v[2] + blo(q1) + blo(xv[mm][n][1]) + blo(bv[n][1]), v[3] + bhi(q1) + bhi(xv[mm][n][1]) + bhi(bv[n][1]));
        *(u32x2*)(out + (size_t)(row0 + mm * 16) * D_ + col0 + n * 16) = o;
      }
  }
}

__global__ __launch_bounds__(512, 2) void k_main(const u16* __restrict__ x, const u16* __restrict__ wqkv, const u16* __restrict__ w1,
                                                 const u16* __restrict__ b1, const u16* __restrict__ wphi,
                                                 const u16* __restrict__ w2, const u16* __restrict__ wo, const u16* __restrict__ b2,
                                                 char* ws, u16* __restrict__ out) {
  extern __shared__ __attribute__((aligned(16))) char smem[];
  LAS char* lds = (LAS char*)smem;
  const int b = blockIdx.x, grp = b & 7, idx = b >> 3;
  const int panel = idx & 7, kind = grp & 1, tcol = (grp >> 1) * 4 + (idx >> 3);
  u64 tag;
  if (kind == 0) {
    tag = gemm1_body<true>(x, wqkv, tcol * 64, 0, panel, tcol, b1, wphi, ws, w2, wo, lds);
  } else {
    const int c0 = tcol * 224, woff = c0 & 63;
    tag = gemm1_body<false>(x, w1, c0 - woff, woff, panel, tcol, b1, wphi, ws, w2, wo, lds);
  }
  gemm2_body(ws, w2, wo, x, b2, out, tag, kind * 16 + tcol,   kind ^ 1, panel, panel * 2 + ((grp >> 1) & 1), (grp >> 2) * 4 + (idx >> 3), lds);
}

extern "C" void kernel_launch(void* const* d_in, const int* in_sizes, int n_in, void* d_out, int out_size, void* d_ws, size_t ws_size,
                              hipStream_t stream) {
  (void)hipFuncSetAttribute((const void*)k_main, hipFuncAttributeMaxDynamicSharedMemorySize, G1_LDS);
  k_main<<<256, 512, G1_LDS, stream>>>((const u16*)d_in[0], (const u16*)d_in[1], (const u16*)d_in[5], (const u16*)d_in[6], (const u16*)d_in[2],
                                       (const u16*)d_in[7], (const u16*)d_in[3], (const u16*)d_in[8], (char*)d_ws, (u16*)d_out);
}
```

```cpp
#include <hip/hip_runtime.h>
#include <hip/hip_bf16.h>
#include <stdint.h>

typedef __bf16 bf16x8 __attribute__((ext_vector_type(8)));
typedef __bf16 bf16x4 __attribute__((ext_vector_type(4)));
typedef __bf16 bf16x2 __attribute__((ext_vector_type(2)));
typedef float f32x4 __attribute__((ext_vector_type(4)));
typedef float f32x2 __attribute__((ext_vector_type(2)));
typedef unsigned u32x4 __attribute__((ext_vector_type(4)));
typedef unsigned u32x2 __attribute__((ext_vector_type(2)));
typedef unsigned short u16;
#define LAS __attribute__((address_space(3)))
#define DI __device__ __forceinline__

constexpr int S_ = 2048, D_ = 1024, F_ = 3584, H_ = 32;
constexpr int KCAT = F_ + D_;
constexpr int NKG1 = D_ / 32;
constexpr int NKG2 = KCAT / 32;

constexpr size_t WS_XN   = 0;
constexpr size_t WS_ACT  = WS_XN + (size_t)S_ * D_ * 2;
constexpr size_t WS_KVX  = WS_ACT + (size_t)S_ * KCAT * 2;
constexpr int KVX_TILE = 8192 + 256;
constexpr size_t KVX_BYTES = (size_t)8 * 16 * KVX_TILE;
constexpr size_t WS_XCH  = WS_KVX + KVX_BYTES;
constexpr size_t WS_FLG  = WS_XCH + (size_t)256 * 16384;
constexpr int FS = 8;
constexpr size_t WS_FLG2 = WS_FLG + 256 * FS, WS_EPOCH = WS_FLG + 512 * FS + 128;

DI float rbf(float x) { return (float)(__bf16)x; }
DI unsigned pk2(float lo, float hi) { bf16x2 v; v[0] = (__bf16)lo; v[1] = (__bf16)hi; return __builtin_bit_cast(unsigned, v); }
DI float blo(unsigned u) { return __uint_as_float(u << 16); }
DI float bhi(unsigned u) { return __uint_as_float(u & 0xffff0000u); }

#define MFMA16(a, b, c) __builtin_amdgcn_mfma_f32_16x16x32_bf16((a), (b), (c), 0, 0, 0)
#define WAIT_V(n) asm volatile("s_waitcnt vmcnt(%0)" ::"n"(n) : "memory")
#define WAIT_L(n) do { static_assert((n) == 0, ""); asm volatile("" ::: "memory"); __builtin_amdgcn_s_waitcnt(0xC07F); asm volatile("" ::: "memory"); } while (0)
#define BAR() do { asm volatile("" ::: "memory"); __builtin_amdgcn_s_barrier(); asm volatile("" ::: "memory"); } while (0)
#define SCHED() __builtin_amdgcn_sched_barrier(0)
template <int N> struct IC { static constexpr int v = N; };

DI void glds16_sc1(unsigned voff, const void* sbase, unsigned ldsaddr) {
  asm volatile("s_mov_b32 m0, %2\n\ts_nop 0\n\tglobal_load_lds_dwordx4 %0, %1 sc1" ::"v"(voff), "s"(sbase), "s"(ldsaddr) : "memory");
}
DI void glds16(unsigned voff, const void* sbase, unsigned ldsaddr) {
  asm volatile("s_mov_b32 m0, %2\n\ts_nop 0\n\tglobal_load_lds_dwordx4 %0, %1" ::"v"(voff), "s"(sbase), "s"(ldsaddr) : "memory");
}
DI const char* uniform_ptr(const char* p) {
  const unsigned long long a = (unsigned long long)p;
  unsigned lo = __builtin_amdgcn_readfirstlane((unsigned)a), hi = __builtin_amdgcn_readfirstlane((unsigned)(a >> 32));
  asm volatile("s_nop 4" : "+s"(lo), "+s"(hi));
  return (const char*)(((unsigned long long)hi << 32) | lo);
}
DI LAS char* opaque(LAS char* p) { asm volatile("" : "+v"(p)); return p; }
DI bf16x8 lds_b128(LAS char* p) { return *(LAS bf16x8*)p; }
DI bf16x4 lds_tr(LAS char* p) { return __builtin_amdgcn_ds_read_tr16_b64_v4bf16((LAS bf16x4*)p); }
DI bf16x8 cat8(bf16x4 a, bf16x4 b) { return __builtin_shufflevector(a, b, 0, 1, 2, 3, 4, 5, 6, 7); }
DI bf16x8 pack8(f32x4 a, f32x4 b) {
  bf16x8 v;
  v[0] = (__bf16)a[0]; v[1] = (__bf16)a[1]; v[2] = (__bf16)a[2]; v[3] = (__bf16)a[3];
  v[4] = (__bf16)b[0]; v[5] = (__bf16)b[1]; v[6] = (__bf16)b[2]; v[7] = (__bf16)b[3];
  return v;
}

typedef __attribute__((address_space(1))) unsigned gu32;
typedef unsigned long long u64;
typedef __attribute__((address_space(1))) u64 gu64;
DI void st_sc1(void* p, u32x4 v) { asm volatile("global_store_dwordx4 %0, %1, off sc1\n\ts_nop 1" ::"v"(p), "v"(v) : "memory"); }
DI unsigned umax4(u32x4 v, unsigned m) { return max(max(max(v[0], v[1]), v[2]), max(v[3], m)); }
DI void st8_sc1(void* p, u32x2 v) { asm volatile("global_store_dwordx2 %0, %1, off sc1\n\ts_nop 1" ::"v"(p), "v"(v) : "memory"); }
DI bool fresh4(u32x4 v) { return umax4(v, 0u) != 0xffffffffu; }
#define XRED4(OP, a1, b1, a2, b2) asm volatile( \
    "s_nop 1\n\tv_permlane16_swap_b32 %0, %1\n\tv_permlane16_swap_b32 %2, %3\n\t" \
    OP " %0, %0, %1\n\t" OP " %2, %2, %3\n\tv_mov_b32 %1, %0\n\tv_mov_b32 %3, %2\n\ts_nop 0\n\t" \
    "v_permlane32_swap_b32 %0, %1\n\tv_permlane32_swap_b32 %2, %3\n\t" \
    OP " %0, %0, %1\n\t" OP " %2, %2, %3\n\tv_mov_b32 %1, %0\n\tv_mov_b32 %3, %2\n\ts_nop 0\n\t" \
    "v_permlane16_swap_b32 %0, %1\n\tv_permlane16_swap_b32 %2, %3" \
    : "+v"(a1), "+v"(b1), "+v"(a2), "+v"(b2))
DI float max8(const float* v) {
  const float t1 = fmaxf(fmaxf(v[0], v[1]), v[2]), t2 = fmaxf(fmaxf(v[3], v[4]), v[5]), t3 = fmaxf(fmaxf(v[6], v[7]), t1);
  return fmaxf(t2, t3);
}
DI float min8(const float* v) {
  const float t1 = fminf(fminf(v[0], v[1]), v[2]), t2 = fminf(fminf(v[3], v[4]), v[5]), t3 = fminf(fminf(v[6], v[7]), t1);
  return fminf(t2, t3);
}
DI void dual_softmax2(const f32x4 (&p)[2][2], u32x4 (&pos)[2], u32x4 (&neg)[2]) {
  float v[2][8];
#pragma unroll
  for (int t = 0; t < 2; ++t)
#pragma unroll
    for (int h = 0; h < 2; ++h)
#pragma unroll
      for (int r = 0; r < 2; ++r) {
        unsigned u = pk2(p[t][h][2 * r], p[t][h][2 * r + 1]);
        asm("" : "+v"(u));
        v[t][4 * h + 2 * r] = blo(u); v[t][4 * h + 2 * r + 1] = bhi(u);
      }
  float mx0 = max8(v[0]), nm0 = -min8(v[0]), mx1 = max8(v[1]), nm1 = -min8(v[1]);
  XRED4("v_max_f32", mx0, nm0, mx1, nm1);
  constexpr float L2E = 1.44269504f;
  const f32x2 kp = {L2E, L2E}, kn = {-L2E, -L2E};
  f32x2 ep[2][4], en[2][4];
  float sp[2], sn[2];
#pragma unroll
  for (int t = 0; t < 2; ++t) {
    const float cpv = t ? mx1 : mx0, cnv = t ? nm1 : nm0;
    const f32x2 cp = {cpv, cpv}, cn = {cnv, cnv};
#pragma unroll
    for (int r = 0; r < 4; ++r) {
      const f32x2 x = {v[t][2 * r], v[t][2 * r + 1]};
      const f32x2 ap = (x - cp) * kp, an = (x + cn) * kn;
      ep[t][r] = (f32x2){__builtin_amdgcn_exp2f(ap[0]), __builtin_amdgcn_exp2f(ap[1])};
      en[t][r] = (f32x2){__builtin_amdgcn_exp2f(an[0]), __builtin_amdgcn_exp2f(an[1])};
    }
    const f32x2 s2p = (ep[t][0] + ep[t][1]) + (ep[t][2] + ep[t][3]), s2n = (en[t][0] + en[t][1]) + (en[t][2] + en[t][3]);
    sp[t] = s2p[0] + s2p[1]; sn[t] = s2n[0] + s2n[1];
  }
  XRED4("v_add_f32", sp[0], sn[0], sp[1], sn[1]);
#pragma unroll
  for (int t = 0; t < 2; ++t) {
    const float rp = __builtin_amdgcn_rcpf(sp[t]), rn = __builtin_amdgcn_rcpf(sn[t]);
    const f32x2 rp2 = {rp, rp}, rn2 = {rn, rn};
#pragma unroll
    for (int r = 0; r < 4; ++r) {
      const f32x2 a = ep[t][r] * rp2, b = en[t][r] * rn2;
      pos[t][r] = pk2(a[0], a[1]); neg[t][r] = pk2(b[0], b[1]);
    }
  }
}

DI float sum8lanes(float v) {
  v += __builtin_bit_cast(float, __builtin_amdgcn_update_dpp(0, __builtin_bit_cast(int, v), 0xB1, 0xf, 0xf, true));
  v += __builtin_bit_cast(float, __builtin_amdgcn_update_dpp(0, __builtin_bit_cast(int, v), 0x4E, 0xf, 0xf, true));
  v += __builtin_bit_cast(float, __builtin_amdgcn_update_dpp(0, __builtin_bit_cast(int, v), 0x141, 0xf, 0xf, true));
  return v;
}
DI void wait_flags(const char* p, u64 tag) {
  for (unsigned spins = 0; spins < (1u << 16); ++spins) {
    if (__all(__hip_atomic_load((const gu64*)p, __ATOMIC_RELAXED, __HIP_MEMORY_SCOPE_AGENT) == tag)) break;
    __builtin_amdgcn_s_sleep(1);
  }
}

constexpr int G1_SLOT = 16384;
constexpr int G1_B = 65536, G1_X = 131072;
constexpr int G1_LDS = G1_X + 4096 + 2048;

DI f32x2 gelu2(f32x2 x) {
  const f32x2 c1 = {-0.10294324f, -0.10294324f}, c0 = {-2.3022082f, -2.3022082f}, one = {1.0f, 1.0f};
  const f32x2 a = x * __builtin_elementwise_fma(x * x, c1, c0);
  const f32x2 d = one + (f32x2){__builtin_amdgcn_exp2f(a[0]), __builtin_amdgcn_exp2f(a[1])};
  return x * (f32x2){__builtin_amdgcn_rcpf(d[0]), __builtin_amdgcn_rcpf(d[1])};
}

template <bool QKV>
DI u64 gemm1_body(const u16* __restrict__ x, const u16* __restrict__ W, int wcol0, int woff, int panel, int tcol,
                  const u16* __restrict__ b1, const u16* __restrict__ wphi, char* ws,
                  const u16* __restrict__ w2, const u16* __restrict__ wo, LAS char* lds) {
  char* xn = ws + WS_XN;
  char* act = ws + WS_ACT;
  char* kvx = ws + WS_KVX;
  constexpr int LDW = QKV ? 3 * D_ : F_;
  constexpr int NG = QKV ? 3 : 4;
  constexpr int NT = QKV ? 6 : 7;
  const int tid = threadIdx.x, lane = tid & 63;
  const int wid = __builtin_amdgcn_readfirstlane(tid >> 6);
  const int wr = wid >> 1, wc = wid & 1;
  const int g = lane >> 4, i16 = lane & 15, q4 = i16 >> 2, p4 = lane & 3;
  const unsigned ldsb = (unsigned)(size_t)lds;

  const char* abase = uniform_ptr(xn + (size_t)panel * 16 * NKG1 * 1024);
  const unsigned avoff = (unsigned)(wid * 2) * NKG1 * 1024 + lane * 16;
  const char* bbase = (const char*)W;
  unsigned bvoff[2]; unsigned bdst[2];
#pragma unroll
  for (int j = 0; j < 2; ++j) {
    int pb = QKV ? (wid + 8 * j) : (wid * 2 + j);
    if (QKV && pb >= 12) pb -= 8;
    const int kg = pb / NG, cg = pb % NG;
    const int r = lane >> 3, sw = ((r >> 1) & 1) | ((kg & 1) << 1);
    const int cc = (lane & 7) ^ (sw << 1);
    const int col = QKV ? (cg * D_ + wcol0) : (wcol0 + cg * 64);
    bvoff[j] = (unsigned)(((kg * 8 + r) * LDW + col + cc * 8) * 2);
    bdst[j] = ldsb + G1_B + pb * 1024;
  }
  const unsigned adst = ldsb + wid * 2048;

  LAS char* ard = opaque(lds + wr * 4096 + lane * 16);
  constexpr int NB = QKV ? 2 : 4;
  LAS char* brd[NB];
#pragma unroll
  for (int j = 0; j < NB; ++j) {
    const int n0 = QKV ? (wc * 32 + j * 16) : (woff + wc * 112 + j * 16);
    const int sw = (q4 >> 1) | ((g & 1) << 1);
    brd[j] = opaque(lds + G1_B + (g * NG + (n0 >> 6)) * 1024 + q4 * 128 + (((((n0 & 63) >> 3) ^ (sw << 1)) + (p4 >> 1)) << 4) + (p4 & 1) * 8);
  }

  f32x4 acc[4][NT];
#pragma unroll
  for (int m = 0; m < 4; ++m)
#pragma unroll
    for (int n = 0; n < NT; ++n) acc[m][n] = (f32x4){0.f, 0.f, 0.f, 0.f};

  bf16x8 fa[2][4], fb[2][NT];

  auto issue = [&](int q, int stage, int slot) {
    if (q < 2) glds16_sc1(avoff + q * NKG1 * 1024, abase + (size_t)stage * 1024, adst + q * 1024 + slot * G1_SLOT);
    else       glds16(bvoff[q - 2], bbase + (size_t)stage * 64 * LDW, bdst[q - 2] + slot * G1_SLOT);
  };
  auto rdA = [&](auto P, auto SL, int m) { fa[P.v][m] = lds_b128(ard + SL.v * G1_SLOT + m * 1024); };
  auto rdB = [&](auto P, auto SL, int n) { LAS char* p = brd[n % NB] + (n / NB) * 1024 + SL.v * G1_SLOT; fb[P.v][n] = cat8(lds_tr(p), lds_tr(p + 512)); };
  auto rdBh = [&](auto P, auto SL, int n, int h) {
    const bf16x4 v = lds_tr(brd[n % NB] + (n / NB) * 1024 + SL.v * G1_SLOT + h * 512);
    fb[P.v][n][4 * h] = v[0]; fb[P.v][n][4 * h + 1] = v[1]; fb[P.v][n][4 * h + 2] = v[2]; fb[P.v][n][4 * h + 3] = v[3];
  };
  auto mma4 = [&](auto P, int n) {
#pragma unroll
    for (int m = 0; m < 4; ++m) {
      if (QKV && n >= 4) acc[m][n] = MFMA16(fa[P.v][m], fb[P.v][n], acc[m][n]);
      else               acc[m][n] = MFMA16(fb[P.v][n], fa[P.v][m], acc[m][n]);
    }
  };
  auto step = [&](auto SC, auto VM, auto GL, auto RD, int s) {
    constexpr int P = SC.v & 1, SNv = (SC.v + 1) & 3;
    const IC<P> Pc; const IC<P ^ 1> Pn; const IC<SNv> SN;
    if constexpr (RD.v) { WAIT_V(VM.v); WAIT_L(0); BAR(); }
    SCHED();
#pragma unroll
    for (int i = 0; i < 4 * NT; ++i) {
      const int m = i & 3, n = i >> 2;
      if (QKV && n >= 4) acc[m][n] = MFMA16(fa[P][m], fb[P][n], acc[m][n]);
      else               acc[m][n] = MFMA16(fb[P][n], fa[P][m], acc[m][n]);
      if (i < 4)                { if constexpr (RD.v) rdA(Pn, SN, i); }
      else if (i < 4 + 2 * NT)  { if constexpr (RD.v) rdBh(Pn, SN, (i - 4) >> 1, (i - 4) & 1); }
      if (i % 4 == 3 && i < 16) { if constexpr (GL.v) issue(i / 4, s + 4, SC.v); }
      SCHED();
    }
  };

  const int slot = (QKV ? 0 : 16) + tcol;
  u64 tag;
  {
    const int r8 = lane >> 3, c8 = lane & 7;
    const int row = panel * 256 + slot * 8 + r8;
    const u16* src = x + (size_t)row * D_ + wid * 128 + c8 * 8;
    u32x4 v0, v1; u32x2 ep;
    asm volatile("global_load_dwordx4 %0, %3, off\n\tglobal_load_dwordx4 %1, %3, off offset:128\n\tglobal_load_dwordx2 %2, %4, off sc1"
                 : "=&v"(v0), "=&v"(v1), "=&v"(ep) : "v"(src), "v"(ws + WS_EPOCH) : "memory");
    SCHED();
    asm volatile("s_waitcnt vmcnt(0)" : "+v"(v0), "+v"(v1), "+v"(ep) :: "memory");
    SCHED();
    if (QKV) {
      const u32x4 fill = {~0u, ~0u, ~0u, ~0u};
      char* kp = kvx + (size_t)(panel * 16 + tcol) * KVX_TILE;
      st_sc1(kp + tid * 16, fill);
      st_sc1(kp + 8192 + i16 * 16, fill);
    }
    tag = (((u64)(unsigned)__builtin_amdgcn_readfirstlane(ep[1]) << 32) | (unsigned)__builtin_amdgcn_readfirstlane(ep[0])) + 1;
    f32x2 f[8];
#pragma unroll
    for (int i = 0; i < 4; ++i) { f[i] = (f32x2){blo(v0[i]), bhi(v0[i])}; f[4 + i] = (f32x2){blo(v1[i]), bhi(v1[i])}; }
    const f32x2 s2 = ((f[0] + f[1]) + (f[2] + f[3])) + ((f[4] + f[5]) + (f[6] + f[7]));
    const float mw = sum8lanes(s2[0] + s2[1]) * (1.0f / 128.f);
    f32x2 q2 = {0.f, 0.f};
#pragma unroll
    for (int i = 0; i < 8; ++i) { const f32x2 d = f[i] - (f32x2){mw, mw}; q2 = __builtin_elementwise_fma(d, d, q2); }
    const float qq = sum8lanes(q2[0] + q2[1]);
    LAS f32x2* stt = (LAS f32x2*)(lds + G1_X + 4096);
    if (c8 == 0) stt[r8 * 8 + wid] = (f32x2){mw, qq};
    WAIT_L(0); BAR();
    const f32x2 pw = stt[r8 * 8 + c8];
    const float mean = sum8lanes(pw[0]) * 0.125f;
    const float dm = pw[0] - mean;
    const float m2 = sum8lanes(__builtin_fmaf(128.f * dm, dm, pw[1]));
    const float mb = rbf(mean), vb = rbf(m2 * (1.0f / D_));
    const float rs = 1.0f / rbf(sqrtf(rbf(vb + 1e-5f)));
    u32x4 o0, o1;
#pragma unroll
    for (int i = 0; i < 8; ++i) {
      const f32x2 d = f[i] - (f32x2){mb, mb};
      unsigned u = pk2(d[0], d[1]);
      asm("" : "+v"(u));
      const f32x2 y = (f32x2){blo(u), bhi(u)} * (f32x2){rs, rs};
      if (i < 4) o0[i] = pk2(y[0], y[1]); else o1[i - 4] = pk2(y[0], y[1]);
    }
    char* dst = xn + ((size_t)((row >> 4) * NKG1 + wid * 4 + (c8 >> 2)) * 64 + (c8 & 3) * 16 + (row & 15)) * 16;
    st_sc1(dst, o0); st_sc1(dst + 2048, o1);
    WAIT_V(0); BAR();
    if (tid == 0) __hip_atomic_store((gu64*)(ws + WS_FLG + (panel * 32 + slot) * FS), tag, __ATOMIC_RELAXED, __HIP_MEMORY_SCOPE_AGENT);
    if (QKV && wid >= 4) glds16((unsigned)lane * 16, uniform_ptr((const char*)wphi + (size_t)tcol * 4096 + (wid - 4) * 1024), ldsb + G1_X + (wid - 4) * 1024);
    if (!QKV && wid == 4) glds16((unsigned)(lane < 28 ? lane : 27) * 16, uniform_ptr((const char*)(b1 + tcol * 224)), ldsb + G1_X);
#pragma unroll
    for (int st = 0; st < 4; ++st) { issue(2, st, st); issue(3, st, st); }
    if (wid == 0) wait_flags(ws + WS_FLG + ((QKV && lane >= 32) ? ((lane & 7) * 32 + slot) : (panel * 32 + (lane & 31))) * FS, tag);
    BAR();
  }
#pragma unroll
  for (int st = 0; st < 4; ++st) { issue(0, st, st); issue(1, st, st); }
  WAIT_V(2); BAR();
  {
    const IC<0> P0; const IC<0> S0;
#pragma unroll
    for (int m = 0; m < 4; ++m) rdA(P0, S0, m);
#pragma unroll
    for (int n = 0; n < NT; ++n) rdB(P0, S0, n);
  }
#pragma unroll 1
  for (int s = 0; s < 28; s += 4) {
    if (s == 8) {
      const int ln = __builtin_amdgcn_mbcnt_hi(~0u, __builtin_amdgcn_mbcnt_lo(~0u, 0u));
      char* xp = ws + WS_XCH + (size_t)blockIdx.x * 16384 + (wid * 64 + ln) * 16;
      const u32x4 fill = {~0u, ~0u, ~0u, ~0u};
      st_sc1(xp, fill); st_sc1(xp + 8192, fill);
    }
    step(IC<0>{}, IC<8>{}, IC<1>{}, IC<1>{}, s);
    step(IC<1>{}, IC<8>{}, IC<1>{}, IC<1>{}, s + 1);
    step(IC<2>{}, IC<8>{}, IC<1>{}, IC<1>{}, s + 2);
    step(IC<3>{}, IC<8>{}, IC<1>{}, IC<1>{}, s + 3);
  }
  step(IC<0>{}, IC<8>{}, IC<0>{}, IC<1>{}, 28);
  constexpr int NX = 1;
  {
    int wv = blockIdx.x * 8 + wid;
    if (wv >= 1152) wv -= 1152;
    const char* src = uniform_ptr(wv < 896 ? (const char*)w2 + (size_t)wv * 8192 : (const char*)wo + (size_t)(wv - 896) * 8192);
    asm volatile("s_mov_b32 m0, %2\n\ts_nop 0\n\tglobal_load_lds_dword %0, %1" ::"v"(lane * 128), "s"(src), "s"(ldsb + G1_X + 4096 + wid * 256) : "memory");
  }
  step(IC<1>{}, IC<4 + NX>{}, IC<0>{}, IC<1>{}, 29);
  step(IC<2>{}, IC<0 + NX>{}, IC<0>{}, IC<1>{}, 30);
  step(IC<3>{}, IC<0>{}, IC<0>{}, IC<0>{}, 31);

  if (!QKV) {
    u32x2 bb[NT];
#pragma unroll
    for (int n = 0; n < NT; ++n) bb[n] = *(LAS u32x2*)(lds + G1_X + (wc * 112 + 4 * g + n * 16) * 2);
    auto act4 = [&](int m, int n) {
      const f32x2 yA = gelu2((f32x2){acc[m][n][0] + blo(bb[n][0]), acc[m][n][1] + bhi(bb[n][0])});
      const f32x2 yB = gelu2((f32x2){acc[m][n][2] + blo(bb[n][1]), acc[m][n][3] + bhi(bb[n][1])});
      return (u32x2){pk2(yA[0], yA[1]), pk2(yB[0], yB[1])};
    };
    const int rt0 = panel * 16 + wr * 4;
    char* obase = (char*)act + ((size_t)rt0 * NKG2 + tcol * 7) * 1024 + lane * 16;
    if (wc == 0) {
#pragma unroll
      for (int m = 0; m < 4; ++m) {
        char* rb = obase + (size_t)m * NKG2 * 1024;
#pragma unroll
        for (int p = 0; p < 3; ++p) { const u32x2 lo = act4(m, 2 * p), hi = act4(m, 2 * p + 1); st_sc1(rb + p * 1024, (u32x4){lo[0], lo[1], hi[0], hi[1]}); }
        st8_sc1(rb + 3 * 1024, act4(m, 6 % NT));
      }
    } else {
#pragma unroll
      for (int m = 0; m < 4; ++m) {
        char* rb = obase + (size_t)m * NKG2 * 1024;
        st8_sc1(rb + 3 * 1024 + 8, act4(m, 0));
#pragma unroll
        for (int p = 0; p < 3; ++p) { const u32x2 lo = act4(m, 2 * p + 1), hi = act4(m, (2 * p + 2) % NT); st_sc1(rb + (4 + p) * 1024, (u32x4){lo[0], lo[1], hi[0], hi[1]}); }
      }
    }
  } else {
    const int head = tcol * 2 + wc;
    LAS char* wp = lds + G1_X + wc * 2048;
    bf16x8 wf[2];
#pragma unroll
    for (int ft = 0; ft < 2; ++ft) {
      LAS char* p = wp + (4 * g + q4) * 64 + (16 * ft + 4 * p4) * 2;
      wf[ft] = cat8(lds_tr(p), lds_tr(p + 16 * 64));
    }
    const f32x4 z4 = {0.f, 0.f, 0.f, 0.f};
    LAS char* sc = lds + (wid >> 2) * G1_B + (wid & 3) * 8704;
#pragma unroll
    for (int mp = 0; mp < 2; ++mp) {
      f32x4 pp[2][2]; u32x4 pos[2], neg[2];
#pragma unroll
      for (int t = 0; t < 2; ++t) {
        const bf16x8 bk = pack8(acc[2 * mp + t][2], acc[2 * mp + t][3]);
        pp[t][0] = MFMA16(wf[0], bk, z4); pp[t][1] = MFMA16(wf[1], bk, z4);
      }
      dual_softmax2(pp, pos, neg);
#pragma unroll
      for (int t = 0; t < 2; ++t) {
        LAS char* rowp = sc + (16 * (2 * mp + t) + i16) * 136 + 8 * g;
        *(LAS u32x2*)(rowp) = (u32x2){pos[t][0], pos[t][1]};      *(LAS u32x2*)(rowp + 32) = (u32x2){pos[t][2], pos[t][3]};
        *(LAS u32x2*)(rowp + 64) = (u32x2){neg[t][0], neg[t][1]}; *(LAS u32x2*)(rowp + 96) = (u32x2){neg[t][2], neg[t][3]};
      }
    }
    WAIT_L(0);
    f32x4 kv[4][3];
#pragma unroll
    for (int ft = 0; ft < 4; ++ft)
#pragma unroll
      for (int d = 0; d < 3; ++d) kv[ft][d] = z4;
    bf16x8 ones;
#pragma unroll
    for (int j = 0; j < 8; ++j) ones[j] = (__bf16)1.0f;
#pragma unroll
    for (int ks = 0; ks < 2; ++ks) {
      const bf16x8 bv0 = pack8(acc[2 * ks][4], acc[2 * ks + 1][4]);
      const bf16x8 bv1 = pack8(acc[2 * ks][5], acc[2 * ks + 1][5]);
#pragma unroll
      for (int ft = 0; ft < 4; ++ft) {
        LAS char* p = sc + (32 * ks + 4 * g + q4) * 136 + (16 * ft + 4 * p4) * 2;
        const bf16x8 a = cat8(lds_tr(p), lds_tr(p + 16 * 136));
        kv[ft][0] = MFMA16(a, bv0, kv[ft][0]);
        kv[ft][1] = MFMA16(a, bv1, kv[ft][1]);
        kv[ft][2] = MFMA16(a, ones, kv[ft][2]);
      }
    }
    WAIT_L(0); BAR();
    {
      LAS char* pp = lds + wid * 8448;
#pragma unroll
      for (int sg = 0; sg < 2; ++sg) {
#pragma unroll
        for (int d = 0; d < 2; ++d) {
          *(LAS f32x4*)(pp + (sg * 2 + d) * 2048 + lane * 16) = kv[2 * sg][d];
          *(LAS f32x4*)(pp + (sg * 2 + d) * 2048 + 1024 + lane * 16) = kv[2 * sg + 1][d];
        }
        if (i16 == 0) {
          *(LAS f32x4*)(pp + 8192 + sg * 128 + g * 16) = kv[2 * sg][2];
          *(LAS f32x4*)(pp + 8192 + sg * 128 + 64 + g * 16) = kv[2 * sg + 1][2];
        }
      }
    }
    WAIT_L(0); BAR();
    char* kvt = kvx + (size_t)tcol * KVX_TILE;
    char* kvmine = kvt + (size_t)panel * 16 * KVX_TILE;
    {
      LAS char* pp = lds + (tid >> 8) * 8448 + ((tid >> 6) & 3) * 2048 + lane * 16;
      f32x4 lo = *(LAS f32x4*)(pp), hi = *(LAS f32x4*)(pp + 1024);
#pragma unroll
      for (int w = 1; w < 4; ++w) { lo += *(LAS f32x4*)(pp + w * 16896); hi += *(LAS f32x4*)(pp + w * 16896 + 1024); }
      st_sc1(kvmine + tid * 16, (u32x4){pk2(lo[0], lo[1]), pk2(lo[2], lo[3]), pk2(hi[0], hi[1]), pk2(hi[2], hi[3])});
    }
    if (tid < 16) {
      LAS char* pp = lds + (tid >> 3) * 8448 + 8192 + ((tid >> 2) & 1) * 128 + (tid & 3) * 16;
      f32x4 lo = *(LAS f32x4*)(pp), hi = *(LAS f32x4*)(pp + 64);
#pragma unroll
      for (int w = 1; w < 4; ++w) { lo += *(LAS f32x4*)(pp + w * 16896); hi += *(LAS f32x4*)(pp + w * 16896 + 64); }
      st_sc1(kvmine + 8192 + tid * 16, (u32x4){pk2(lo[0], lo[1]), pk2(lo[2], lo[3]), pk2(hi[0], hi[1]), pk2(hi[2], hi[3])});
    }
    u32x4 qp[2][2], qn[2][2];
    auto qsm = [&](int mp) {
      f32x4 pp[2][2];
#pragma unroll
      for (int t = 0; t < 2; ++t) {
        const bf16x8 bq = pack8(acc[2 * mp + t][0], acc[2 * mp + t][1]);
        pp[t][0] = MFMA16(wf[0], bq, z4); pp[t][1] = MFMA16(wf[1], bq, z4);
      }
      dual_softmax2(pp, qp[mp], qn[mp]);
    };
    qsm(0);
    const char* kb0 = uniform_ptr(kvt), * kb1 = uniform_ptr(kvt + (size_t)32 * KVX_TILE);
    const char* kb2 = uniform_ptr(kvt + (size_t)64 * KVX_TILE), * kb3 = uniform_ptr(kvt + (size_t)96 * KVX_TILE);
    u32x4 pv[8], pz[8];
#define KV_GATHER(pv, OFF, TAIL) asm volatile( \
          "global_load_dwordx4 %0, %8, %10 sc1\n\tglobal_load_dwordx4 %1, %9, %10 sc1\n\t" \
          "global_load_dwordx4 %2, %8, %11 sc1\n\tglobal_load_dwordx4 %3, %9, %11 sc1\n\t" \
          "global_load_dwordx4 %4, %8, %12 sc1\n\tglobal_load_dwordx4 %5, %9, %12 sc1\n\t" \
          "global_load_dwordx4 %6, %8, %13 sc1\n\tglobal_load_dwordx4 %7, %9, %13 sc1" TAIL \
          : "=&v"(pv[0]), "=&v"(pv[1]), "=&v"(pv[2]), "=&v"(pv[3]), "=&v"(pv[4]), "=&v"(pv[5]), "=&v"(pv[6]), "=&v"(pv[7]) \
          : "v"(OFF), "v"((OFF) + 16 * KVX_TILE), "s"(kb0), "s"(kb1), "s"(kb2), "s"(kb3) : "memory")
#define KV_WAIT(pv) asm volatile("s_waitcnt vmcnt(0)" \
          : "+v"(pv[0]), "+v"(pv[1]), "+v"(pv[2]), "+v"(pv[3]), "+v"(pv[4]), "+v"(pv[5]), "+v"(pv[6]), "+v"(pv[7]) :: "memory")
    LAS char* kvs = lds + 98304;
    auto finish = [&](u32x4 (&pv)[8], unsigned off) {
      for (unsigned spins = 0;; ++spins) {
        unsigned m = 0;
#pragma unroll
        for (int p = 0; p < 8; ++p) m = umax4(pv[p], m);
        if (__all(m != 0xffffffffu) || spins > (1u << 14)) break;
        __builtin_amdgcn_s_sleep(2);
        KV_GATHER(pv, off, "\n\ts_waitcnt vmcnt(0)");
      }
      f32x2 t[4];
#pragma unroll
      for (int r = 0; r < 4; ++r) t[r] = (f32x2){blo(pv[0][r]), bhi(pv[0][r])};
#pragma unroll
      for (int p = 1; p < 8; ++p)
#pragma unroll
        for (int r = 0; r < 4; ++r) t[r] += (f32x2){blo(pv[p][r]), bhi(pv[p][r])};
      *(LAS u32x4*)(kvs + off) = (u32x4){pk2(t[0][0], t[0][1]), pk2(t[1][0], t[1][1]), pk2(t[2][0], t[2][1]), pk2(t[3][0], t[3][1])};
    };
    const unsigned go = (unsigned)tid * 16, gz = 8192u + (unsigned)i16 * 16;
    SCHED();
    KV_GATHER(pv, go, "");
    KV_GATHER(pz, gz, "");
    SCHED();
    qsm(1);
    SCHED();
    KV_WAIT(pv);
    KV_WAIT(pz);
    SCHED();
    finish(pv, go);
    if (wid == 0) finish(pz, gz);
    WAIT_L(0); BAR();
    bf16x8 a[2][3];
#pragma unroll
    for (int sg = 0; sg < 2; ++sg) {
#pragma unroll
      for (int d = 0; d < 2; ++d) a[sg][d] = lds_b128(kvs + wc * 4096 + (sg * 2 + d) * 1024 + lane * 16);
      a[sg][2] = lds_b128(kvs + 8192 + wc * 128 + sg * 64 + g * 16);
    }
#pragma unroll
    for (int m = 0; m < 4; ++m) {
      const bf16x8 bp = __builtin_bit_cast(bf16x8, qp[m >> 1][m & 1]), bn = __builtin_bit_cast(bf16x8, qn[m >> 1][m & 1]);
      f32x4 o0 = MFMA16(a[0][0], bp, z4), o1 = MFMA16(a[0][1], bp, z4), dn = MFMA16(a[0][2], bp, z4);
      o0 = MFMA16(a[1][0], bn, o0); o1 = MFMA16(a[1][1], bn, o1); dn = MFMA16(a[1][2], bn, dn);
      const float rd = __builtin_amdgcn_rcpf(dn[0] + 1e-6f);
      const int rt = panel * 16 + wr * 4 + m;
      u32x4 w;
      w[0] = pk2(o0[0] * rd, o0[1] * rd); w[1] = pk2(o0[2] * rd, o0[3] * rd);
      w[2] = pk2(o1[0] * rd, o1[1] * rd); w[3] = pk2(o1[2] * rd, o1[3] * rd);
      st_sc1((char*)act + ((size_t)rt * NKG2 + 112 + head) * 1024 + lane * 16, w);
    }
  }
  return tag;
}

constexpr int G2_SLOT = 32768;
constexpr int G2_POLL = 4 * G2_SLOT + 4096;
constexpr int KSPLIT = 36;

DI void gemm2_body(char* ws, const u16* __restrict__ w2, const u16* __restrict__ wo,
                   const u16* __restrict__ x, const u16* __restrict__ b2, u16* __restrict__ out,
                   u64 tag, int slot, int split, int panel, int pm, int pn, LAS char* lds) {
  const char* act = ws + WS_ACT;
  char* xch = ws + WS_XCH;
  char* flg2 = ws + WS_FLG2 + panel * 32 * FS;
  const unsigned ldsb = (unsigned)(size_t)lds;
  int lane = __builtin_amdgcn_mbcnt_hi(~0u, __builtin_amdgcn_mbcnt_lo(~0u, 0u));
  asm volatile("" : "+v"(lane));
  const int wid = __builtin_amdgcn_readfirstlane(threadIdx.x >> 6);
  const int kh = wid >> 2, wr = (wid >> 1) & 1, wc = wid & 1;
  const int g = lane >> 4, i16 = lane & 15, q4 = i16 >> 2, p4 = lane & 3;
  const int b = blockIdx.x;
  const int kt0 = split ? KSPLIT : 0, nst = split ? 72 - KSPLIT : KSPLIT;
  const char* abase = uniform_ptr(act + ((size_t)(pm * 8) * NKG2 + kt0 * 2) * 1024);
  const unsigned avoff = (unsigned)wid * NKG2 * 1024 + lane * 16;
  const unsigned adst = ldsb + wid * 1024;
  const int r8 = lane >> 3;
  const unsigned bvoff = (unsigned)((wid * 8 + r8) * D_ + pn * 128 + (((lane & 7) ^ (((r8 >> 1) & 3) << 1)) << 3)) * 2;
  const unsigned bdst = ldsb + 16384 + wid * 2048;
  const char* wb = uniform_ptr((const char*)w2 + (size_t)kt0 * 64 * D_ * 2);
  auto issue = [&](int q, int t, int slot) {
    if (q < 2) glds16_sc1(avoff + q * 1024, abase + (size_t)t * 2048, adst + q * 8192 + slot * G2_SLOT);
    else       glds16(bvoff + (q - 2) * 128, wb + (size_t)t * 64 * D_ * 2, bdst + (q - 2) * 1024 + slot * G2_SLOT);
  };
  WAIT_V(0); WAIT_L(0); BAR();
  if (threadIdx.x == 0) __hip_atomic_store((gu64*)(flg2 + slot * FS), tag, __ATOMIC_RELAXED, __HIP_MEMORY_SCOPE_AGENT);
  if (wid == panel) wait_flags(flg2 + (16 + (split ? 10 + (i16 < 6 ? i16 : 0) : (i16 < 11 ? i16 : 0))) * FS, tag);
#pragma unroll
  for (int st = 0; st < 4; ++st) { issue(2, st, st); issue(3, st, st); }
  BAR();
#pragma unroll
  for (int st = 0; st < 4; ++st) { issue(0, st, st); issue(1, st, st); }

  LAS char* ard[2];
  ard[0] = opaque(lds + kh * 8192 + wr * 4096 + lane * 16); ard[1] = opaque(ard[0] + 65536);
  LAS char* brd[2][4];
#pragma unroll
  for (int j = 0; j < 4; ++j) {
    const int r = 4 * (g & 1) + q4, sw = (r >> 1) & 3;
    brd[0][j] = opaque(lds + 16384 + ((kh * 4 + (g >> 1)) * 2 + wc) * 1024 + r * 128 + ((((2 * j) ^ (sw << 1)) + (p4 >> 1)) << 4) + (p4 & 1) * 8);
    brd[1][j] = opaque(brd[0][j] + 65536);
  }
  f32x4 acc[4][4];
#pragma unroll
  for (int m = 0; m < 4; ++m)
#pragma unroll
    for (int n = 0; n < 4; ++n) acc[m][n] = (f32x4){0.f, 0.f, 0.f, 0.f};
  bf16x8 fa[2][4], fb[2][4];
  auto rdA = [&](auto P, auto SL, int m) { fa[P.v][m] = lds_b128(ard[SL.v >> 1] + (SL.v & 1) * G2_SLOT + m * 1024); };
  auto rdB = [&](auto P, auto SL, int n) { LAS char* p = brd[SL.v >> 1][n] + (SL.v & 1) * G2_SLOT; fb[P.v][n] = cat8(lds_tr(p), lds_tr(p + 4096)); };
  auto rdBh = [&](auto P, auto SL, int n, int h) {
    const bf16x4 v = lds_tr(brd[SL.v >> 1][n] + (SL.v & 1) * G2_SLOT + h * 4096);
    fb[P.v][n][4 * h] = v[0]; fb[P.v][n][4 * h + 1] = v[1]; fb[P.v][n][4 * h + 2] = v[2]; fb[P.v][n][4 * h + 3] = v[3];
  };
  auto mma4 = [&](auto P, int n) {
#pragma unroll
    for (int m = 0; m < 4; ++m) acc[m][n] = MFMA16(fb[P.v][n], fa[P.v][m], acc[m][n]);
  };
  auto step = [&](auto SC, auto VM, auto GL, auto RD, int t) {
    constexpr int P = SC.v & 1, SNv = (SC.v + 1) & 3;
    const IC<P> Pc; const IC<P ^ 1> Pn; const IC<SNv> SN;
    if constexpr (RD.v) { WAIT_V(VM.v); WAIT_L(0); BAR(); }
    SCHED();
#pragma unroll
    for (int i = 0; i < 16; ++i) {
      acc[i & 3][i >> 2] = MFMA16(fb[P][i >> 2], fa[P][i & 3], acc[i & 3][i >> 2]);
      if (i < 4)       { if constexpr (RD.v) rdA(Pn, SN, i); }
      else if (i < 12) { if constexpr (RD.v) rdBh(Pn, SN, (i - 4) >> 1, (i - 4) & 1); }
      if (i % 3 == 2 && i < 12) { if constexpr (GL.v) issue(i / 3, t + 4, SC.v); }
      SCHED();
    }
  };
  WAIT_V(2); BAR();
  {
    const IC<0> P0; const IC<0> S0;
#pragma unroll
    for (int m = 0; m < 4; ++m) rdA(P0, S0, m);
#pragma unroll
    for (int n = 0; n < 4; ++n) rdB(P0, S0, n);
  }
  const IC<1> Y; const IC<0> N_; const IC<8> V8;
  const int tp = split ? 8 : 24;
#pragma unroll 1
  for (int t = 0; t < nst - 4; t += 4) {
    if (wid == 0) {
      if (t == tp) {
        asm volatile("s_mov_b32 m0, %2\n\ts_nop 0\n\tglobal_load_lds_dword %0, %1 sc1" ::"v"((lane >> 1) * FS + (lane & 1) * 4), "s"(uniform_ptr(flg2)), "s"(ldsb + G2_POLL) : "memory");
      } else if (t == tp + 4) {
        if (!__all(*(LAS u64*)(lds + G2_POLL + (lane & 31) * 8) == tag)) wait_flags(flg2 + (lane & 31) * FS, tag);
      }
    }
    if (t == 52 - KSPLIT && split == 1) wb = uniform_ptr((const char*)wo - (size_t)(56 - KSPLIT) * 64 * D_ * 2);
    step(IC<0>{}, V8, Y, Y, t); step(IC<1>{}, V8, Y, Y, t + 1); step(IC<2>{}, V8, Y, Y, t + 2); step(IC<3>{}, V8, Y, Y, t + 3);
  }
  step(IC<0>{}, V8, N_, Y, 0);
  step(IC<1>{}, IC<4>{}, N_, Y, 0);
  step(IC<2>{}, IC<0>{}, N_, Y, 0);
  step(IC<3>{}, IC<0>{}, N_, N_, 0);
  const int quad = wid & 3;
  {
    LAS char* pp = lds + (quad * 2 + kh) * 8192 + lane * 16;
#pragma unroll
    for (int mm = 0; mm < 2; ++mm)
#pragma unroll
      for (int n = 0; n < 4; ++n) *(LAS f32x4*)(pp + (mm * 4 + n) * 1024) = kh ? acc[mm][n] : acc[2 + mm][n];
  }
  const bool keep = (wr == split);
  const int row0 = pm * 128 + wr * 64 + kh * 32 + i16, col0 = pn * 128 + wc * 64 + 4 * g;
  u32x2 xv[2][4], bv[4];
  if (keep) {
#pragma unroll
    for (int n = 0; n < 4; ++n) bv[n] = *(const u32x2*)(b2 + col0 + n * 16);
#pragma unroll
    for (int mm = 0; mm < 2; ++mm)
#pragma unroll
      for (int n = 0; n < 4; ++n) xv[mm][n] = *(const u32x2*)(x + (size_t)(row0 + mm * 16) * D_ + col0 + n * 16);
  }
  WAIT_L(0); BAR();
  f32x4 sum[2][4];
  {
    LAS char* pp = lds + (quad * 2 + (kh ^ 1)) * 8192 + lane * 16;
#pragma unroll
    for (int mm = 0; mm < 2; ++mm)
#pragma unroll
      for (int n = 0; n < 4; ++n) sum[mm][n] = (kh ? acc[2 + mm][n] : acc[mm][n]) + *(LAS f32x4*)(pp + (mm * 4 + n) * 1024);
  }
  const int piece = kh * 2 + wc;
  if (!keep) {
    char* dst = xch + (size_t)b * 16384 + piece * 4096 + lane * 16;
#pragma unroll
    for (int mm = 0; mm < 2; ++mm)
#pragma unroll
      for (int h = 0; h < 2; ++h)
        st_sc1(dst + (mm * 2 + h) * 1024, (u32x4){pk2(sum[mm][2 * h][0], sum[mm][2 * h][1]), pk2(sum[mm][2 * h][2], sum[mm][2 * h][3]),
                                                  pk2(sum[mm][2 * h + 1][0], sum[mm][2 * h + 1][1]), pk2(sum[mm][2 * h + 1][2], sum[mm][2 * h + 1][3])});
    if (b == 0 && wid == 0) {
#pragma unroll
      for (int j = 0; j < 4; ++j) wait_flags(ws + WS_FLG + (j * 64 + lane) * FS, tag);
      if (lane == 0) __hip_atomic_store((gu64*)(ws + WS_EPOCH), tag, __ATOMIC_RELAXED, __HIP_MEMORY_SCOPE_AGENT);
    }
  } else {
    const char* src = xch + (size_t)(b ^ 1) * 16384 + piece * 4096 + lane * 16;
    u32x4 pv[4];
    for (unsigned spins = 0;; ++spins) {
      asm volatile(
          "global_load_dwordx4 %0, %4, off sc1\n\tglobal_load_dwordx4 %1, %4, off offset:1024 sc1\n\t"
          "global_load_dwordx4 %2, %4, off offset:2048 sc1\n\tglobal_load_dwordx4 %3, %4, off offset:3072 sc1\n\ts_waitcnt vmcnt(0)"
          : "=&v"(pv[0]), "=&v"(pv[1]), "=&v"(pv[2]), "=&v"(pv[3]) : "v"(src) : "memory");
      if (__all(fresh4(pv[0]) && fresh4(pv[1]) && fresh4(pv[2]) && fresh4(pv[3])) || spins > (1u << 14)) break;
      __builtin_amdgcn_s_sleep(1);
    }
#pragma unroll
    for (int mm = 0; mm < 2; ++mm)
#pragma unroll
      for (int n = 0; n < 4; ++n) {
        const unsigned q0 = pv[mm * 2 + (n >> 1)][2 * (n & 1)], q1 = pv[mm * 2 + (n >> 1)][2 * (n & 1) + 1];
        const f32x4 v = sum[mm][n];
        u32x2 o;
        o[0] = pk2(v[0] + blo(q0) + blo(xv[mm][n][0]) + blo(bv[n][0]), v[1] + bhi(q0) + bhi(xv[mm][n][0]) + bhi(bv[n][0]));
        o[1] = pk2(v[2] + blo(q1) + blo(xv[mm][n][1]) + blo(bv[n][1]), v[3] + bhi(q1) + bhi(xv[mm][n][1]) + bhi(bv[n][1]));
        *(u32x2*)(out + (size_t)(row0 + mm * 16) * D_ + col0 + n * 16) = o;
      }
  }
}

__global__ __launch_bounds__(512, 2) void k_main(const u16* __restrict__ x, const u16* __restrict__ wqkv, const u16* __restrict__ w1,
                                                 const u16* __restrict__ b1, const u16* __restrict__ wphi,
                                                 const u16* __restrict__ w2, const u16* __restrict__ wo, const u16* __restrict__ b2,
                                                 char* ws, u16* __restrict__ out) {
  extern __shared__ __attribute__((aligned(16))) char smem[];
  LAS char* lds = (LAS char*)smem;
  const int b = blockIdx.x, grp = b & 7, idx = b >> 3;
  const int panel = idx & 7, kind = grp & 1, tcol = (grp >> 1) * 4 + (idx >> 3);
  u64 tag;
  if (kind == 0) {
    tag = gemm1_body<true>(x, wqkv, tcol * 64, 0, panel, tcol, b1, wphi, ws, w2, wo, lds);
  } else {
    const int c0 = tcol * 224, woff = c0 & 63;
    tag = gemm1_body<false>(x, w1, c0 - woff, woff, panel, tcol, b1, wphi, ws, w2, wo, lds);
  }
  gemm2_body(ws, w2, wo, x, b2, out, tag, kind * 16 + tcol,   kind ^ 1, panel, panel * 2 + ((grp >> 1) & 1), (grp >> 2) * 4 + (idx >> 3), lds);
}

extern "C" void kernel_launch(void* const* d_in, const int* in_sizes, int n_in, void* d_out, int out_size, void* d_ws, size_t ws_size,
                              hipStream_t stream) {
  (void)hipFuncSetAttribute((const void*)k_main, hipFuncAttributeMaxDynamicSharedMemorySize, G1_LDS);
  k_main<<<256, 512, G1_LDS, stream>>>((const u16*)d_in[0], (const u16*)d_in[1], (const u16*)d_in[5], (const u16*)d_in[6], (const u16*)d_in[2],
                                       (const u16*)d_in[7], (const u16*)d_in[3], (const u16*)d_in[8], (char*)d_ws, (u16*)d_out);
}
```

```cpp
#include <hip/hip_runtime.h>
#include <hip/hip_bf16.h>
#include <stdint.h>

typedef __bf16 bf16x8 __attribute__((ext_vector_type(8)));
typedef __bf16 bf16x4 __attribute__((ext_vector_type(4)));
typedef __bf16 bf16x2 __attribute__((ext_vector_type(2)));
typedef float f32x4 __attribute__((ext_vector_type(4)));
typedef float f32x2 __attribute__((ext_vector_type(2)));
typedef unsigned u32x4 __attribute__((ext_vector_type(4)));
typedef unsigned u32x2 __attribute__((ext_vector_type(2)));
typedef unsigned short u16;
#define LAS __attribute__((address_space(3)))
#define DI __device__ __forceinline__

constexpr int S_ = 2048, D_ = 1024, F_ = 3584, H_ = 32;
constexpr int KCAT = F_ + D_;
constexpr int NKG1 = D_ / 32;
constexpr int NKG2 = KCAT / 32;

constexpr size_t WS_XN   = 0;
constexpr size_t WS_ACT  = WS_XN + (size_t)S_ * D_ * 2;
constexpr size_t WS_KVX  = WS_ACT + (size_t)S_ * KCAT * 2;
constexpr int KVX_TILE = 8192 + 256;
constexpr size_t KVX_BYTES = (size_t)8 * 16 * KVX_TILE;
constexpr size_t WS_XCH  = WS_KVX + KVX_BYTES;
constexpr size_t WS_FLG  = WS_XCH + (size_t)256 * 16384;
constexpr int FS = 8;
constexpr size_t WS_FLG2 = WS_FLG + 256 * FS, WS_EPOCH = WS_FLG + 512 * FS + 128;

DI float rbf(float x) { return (float)(__bf16)x; }
DI unsigned pk2(float lo, float hi) { bf16x2 v; v[0] = (__bf16)lo; v[1] = (__bf16)hi; return __builtin_bit_cast(unsigned, v); }
DI float blo(unsigned u) { return __uint_as_float(u << 16); }
DI float bhi(unsigned u) { return __uint_as_float(u & 0xffff0000u); }

#define MFMA16(a, b, c) __builtin_amdgcn_mfma_f32_16x16x32_bf16((a), (b), (c), 0, 0, 0)
#define WAIT_V(n) asm volatile("s_waitcnt vmcnt(%0)" ::"n"(n) : "memory")
#define WAIT_L(n) do { static_assert((n) == 0, ""); asm volatile("" ::: "memory"); __builtin_amdgcn_s_waitcnt(0xC07F); asm volatile("" ::: "memory"); } while (0)
#define BAR() do { asm volatile("" ::: "memory"); __builtin_amdgcn_s_barrier(); asm volatile("" ::: "memory"); } while (0)
#define SCHED() __builtin_amdgcn_sched_barrier(0)
template <int N> struct IC { static constexpr int v = N; };

DI void glds16_sc1(unsigned voff, const void* sbase, unsigned ldsaddr) {
  asm volatile("s_mov_b32 m0, %2\n\ts_nop 0\n\tglobal_load_lds_dwordx4 %0, %1 sc1" ::"v"(voff), "s"(sbase), "s"(ldsaddr) : "memory");
}
DI void glds16(unsigned voff, const void* sbase, unsigned ldsaddr) {
  asm volatile("s_mov_b32 m0, %2\n\ts_nop 0\n\tglobal_load_lds_dwordx4 %0, %1" ::"v"(voff), "s"(sbase), "s"(ldsaddr) : "memory");
}
DI const char* uniform_ptr(const char* p) {
  const unsigned long long a = (unsigned long long)p;
  unsigned lo = __builtin_amdgcn_readfirstlane((unsigned)a), hi = __builtin_amdgcn_readfirstlane((unsigned)(a >> 32));
  asm volatile("s_nop 4" : "+s"(lo), "+s"(hi));
  return (const char*)(((unsigned long long)hi << 32) | lo);
}
DI LAS char* opaque(LAS char* p) { asm volatile("" : "+v"(p)); return p; }
DI bf16x8 lds_b128(LAS char* p) { return *(LAS bf16x8*)p; }
DI bf16x4 lds_tr(LAS char* p) { return __builtin_amdgcn_ds_read_tr16_b64_v4bf16((LAS bf16x4*)p); }
DI bf16x8 cat8(bf16x4 a, bf16x4 b) { return __builtin_shufflevector(a, b, 0, 1, 2, 3, 4, 5, 6, 7); }
DI bf16x8 pack8(f32x4 a, f32x4 b) {
  bf16x8 v;
  v[0] = (__bf16)a[0]; v[1] = (__bf16)a[1]; v[2] = (__bf16)a[2]; v[3] = (__bf16)a[3];
  v[4] = (__bf16)b[0]; v[5] = (__bf16)b[1]; v[6] = (__bf16)b[2]; v[7] = (__bf16)b[3];
  return v;
}

typedef unsigned long long u64;
typedef __attribute__((address_space(1))) u64 gu64;
DI void st_sc1(void* p, u32x4 v) { asm volatile("global_store_dwordx4 %0, %1, off sc1\n\ts_nop 1" ::"v"(p), "v"(v) : "memory"); }
DI unsigned umax4(u32x4 v, unsigned m) { return max(max(max(v[0], v[1]), v[2]), max(v[3], m)); }
DI void st8_sc1(void* p, u32x2 v) { asm volatile("global_store_dwordx2 %0, %1, off sc1\n\ts_nop 1" ::"v"(p), "v"(v) : "memory"); }
DI bool fresh4(u32x4 v) { return umax4(v, 0u) != 0xffffffffu; }
#define XRED4(OP, a1, b1, a2, b2) asm volatile( \
    "s_nop 1\n\tv_permlane16_swap_b32 %0, %1\n\tv_permlane16_swap_b32 %2, %3\n\t" \
    OP " %0, %0, %1\n\t" OP " %2, %2, %3\n\tv_mov_b32 %1, %0\n\tv_mov_b32 %3, %2\n\ts_nop 0\n\t" \
    "v_permlane32_swap_b32 %0, %1\n\tv_permlane32_swap_b32 %2, %3\n\t" \
    OP " %0, %0, %1\n\t" OP " %2, %2, %3\n\tv_mov_b32 %1, %0\n\tv_mov_b32 %3, %2\n\ts_nop 0\n\t" \
    "v_permlane16_swap_b32 %0, %1\n\tv_permlane16_swap_b32 %2, %3" \
    : "+v"(a1), "+v"(b1), "+v"(a2), "+v"(b2))
DI float max8(const float* v) {
  const float t1 = fmaxf(fmaxf(v[0], v[1]), v[2]), t2 = fmaxf(fmaxf(v[3], v[4]), v[5]), t3 = fmaxf(fmaxf(v[6], v[7]), t1);
  return fmaxf(t2, t3);
}
DI float min8(const float* v) {
  const float t1 = fminf(fminf(v[0], v[1]), v[2]), t2 = fminf(fminf(v[3], v[4]), v[5]), t3 = fminf(fminf(v[6], v[7]), t1);
  return fminf(t2, t3);
}
DI void dual_softmax2(const f32x4 (&p)[2][2], u32x4 (&pos)[2], u32x4 (&neg)[2]) {
  float v[2][8];
#pragma unroll
  for (int t = 0; t < 2; ++t)
#pragma unroll
    for (int h = 0; h < 2; ++h)
#pragma unroll
      for (int r = 0; r < 2; ++r) {
        unsigned u = pk2(p[t][h][2 * r], p[t][h][2 * r + 1]);
        asm("" : "+v"(u));
        v[t][4 * h + 2 * r] = blo(u); v[t][4 * h + 2 * r + 1] = bhi(u);
      }
  float mx0 = max8(v[0]), nm0 = -min8(v[0]), mx1 = max8(v[1]), nm1 = -min8(v[1]);
  XRED4("v_max_f32", mx0, nm0, mx1, nm1);
  constexpr float L2E = 1.44269504f;
  const f32x2 kp = {L2E, L2E}, kn = {-L2E, -L2E};
  f32x2 ep[2][4], en[2][4];
  float sp[2], sn[2];
#pragma unroll
  for (int t = 0; t < 2; ++t) {
    const float cpv = t ? mx1 : mx0, cnv = t ? nm1 : nm0;
    const f32x2 cp = {cpv, cpv}, cn = {cnv, cnv};
#pragma unroll
    for (int r = 0; r < 4; ++r) {
      const f32x2 x = {v[t][2 * r], v[t][2 * r + 1]};
      const f32x2 ap = (x - cp) * kp, an = (x + cn) * kn;
      ep[t][r] = (f32x2){__builtin_amdgcn_exp2f(ap[0]), __builtin_amdgcn_exp2f(ap[1])};
      en[t][r] = (f32x2){__builtin_amdgcn_exp2f(an[0]), __builtin_amdgcn_exp2f(an[1])};
    }
    const f32x2 s2p = (ep[t][0] + ep[t][1]) + (ep[t][2] + ep[t][3]), s2n = (en[t][0] + en[t][1]) + (en[t][2] + en[t][3]);
    sp[t] = s2p[0] + s2p[1]; sn[t] = s2n[0] + s2n[1];
  }
  XRED4("v_add_f32", sp[0], sn[0], sp[1], sn[1]);
#pragma unroll
  for (int t = 0; t < 2; ++t) {
    const float rp = __builtin_amdgcn_rcpf(sp[t]), rn = __builtin_amdgcn_rcpf(sn[t]);
    const f32x2 rp2 = {rp, rp}, rn2 = {rn, rn};
#pragma unroll
    for (int r = 0; r < 4; ++r) {
      const f32x2 a = ep[t][r] * rp2, b = en[t][r] * rn2;
      pos[t][r] = pk2(a[0], a[1]); neg[t][r] = pk2(b[0], b[1]);
    }
  }
}

DI float sum8lanes(float v) {
  v += __builtin_bit_cast(float, __builtin_amdgcn_update_dpp(0, __builtin_bit_cast(int, v), 0xB1, 0xf, 0xf, true));
  v += __builtin_bit_cast(float, __builtin_amdgcn_update_dpp(0, __builtin_bit_cast(int, v), 0x4E, 0xf, 0xf, true));
  v += __builtin_bit_cast(float, __builtin_amdgcn_update_dpp(0, __builtin_bit_cast(int, v), 0x141, 0xf, 0xf, true));
  return v;
}
DI void wait_flags(const char* p, u64 tag) {
  for (unsigned spins = 0; spins < (1u << 16); ++spins) {
    if (__all(__hip_atomic_load((const gu64*)p, __ATOMIC_RELAXED, __HIP_MEMORY_SCOPE_AGENT) == tag)) break;
    __builtin_amdgcn_s_sleep(1);
  }
}

constexpr int G1_SLOT = 16384;
constexpr int G1_B = 65536, G1_X = 131072;
constexpr int G1_LDS = G1_X + 4096 + 2048;

DI f32x2 gelu2(f32x2 x) {
  const f32x2 c1 = {-0.10294324f, -0.10294324f}, c0 = {-2.3022082f, -2.3022082f}, one = {1.0f, 1.0f};
  const f32x2 a = x * __builtin_elementwise_fma(x * x, c1, c0);
  const f32x2 d = one + (f32x2){__builtin_amdgcn_exp2f(a[0]), __builtin_amdgcn_exp2f(a[1])};
  return x * (f32x2){__builtin_amdgcn_rcpf(d[0]), __builtin_amdgcn_rcpf(d[1])};
}

template <bool QKV>
DI u64 gemm1_body(const u16* __restrict__ x, const u16* __restrict__ W, int wcol0, int woff, int panel, int tcol,
                  const u16* __restrict__ b1, const u16* __restrict__ wphi, char* ws,
                  const u16* __restrict__ w2, const u16* __restrict__ wo, LAS char* lds) {
  char* xn = ws + WS_XN;
  char* act = ws + WS_ACT;
  char* kvx = ws + WS_KVX;
  constexpr int LDW = QKV ? 3 * D_ : F_;
  constexpr int NG = QKV ? 3 : 4;
  constexpr int NT = QKV ? 6 : 7;
  const int tid = threadIdx.x, lane = tid & 63;
  const int wid = __builtin_amdgcn_readfirstlane(tid >> 6);
  const int wr = wid >> 1, wc = wid & 1;
  const int g = lane >> 4, i16 = lane & 15, q4 = i16 >> 2, p4 = lane & 3;
  const unsigned ldsb = (unsigned)(size_t)lds;

  const char* abase = uniform_ptr(xn + (size_t)panel * 16 * NKG1 * 1024);
  const unsigned avoff = (unsigned)(wid * 2) * NKG1 * 1024 + lane * 16;
  const char* bbase = (const char*)W;
  unsigned bvoff[2]; unsigned bdst[2];
#pragma unroll
  for (int j = 0; j < 2; ++j) {
    int pb = QKV ? (wid + 8 * j) : (wid * 2 + j);
    if (QKV && pb >= 12) pb -= 8;
    const int kg = pb / NG, cg = pb % NG;
    const int r = lane >> 3, sw = ((r >> 1) & 1) | ((kg & 1) << 1);
    const int cc = (lane & 7) ^ (sw << 1);
    const int col = QKV ? (cg * D_ + wcol0) : (wcol0 + cg * 64);
    bvoff[j] = (unsigned)(((kg * 8 + r) * LDW + col + cc * 8) * 2);
    bdst[j] = ldsb + G1_B + pb * 1024;
  }
  const unsigned adst = ldsb + wid * 2048;

  LAS char* ard = opaque(lds + wr * 4096 + lane * 16);
  constexpr int NB = QKV ? 2 : 4;
  LAS char* brd[NB];
#pragma unroll
  for (int j = 0; j < NB; ++j) {
    const int n0 = QKV ? (wc * 32 + j * 16) : (woff + wc * 112 + j * 16);
    const int sw = (q4 >> 1) | ((g & 1) << 1);
    brd[j] = opaque(lds + G1_B + (g * NG + (n0 >> 6)) * 1024 + q4 * 128 + (((((n0 & 63) >> 3) ^ (sw << 1)) + (p4 >> 1)) << 4) + (p4 & 1) * 8);
  }

  f32x4 acc[4][NT];
#pragma unroll
  for (int m = 0; m < 4; ++m)
#pragma unroll
    for (int n = 0; n < NT; ++n) acc[m][n] = (f32x4){0.f, 0.f, 0.f, 0.f};

  bf16x8 fa[2][4], fb[2][NT];

  auto issue = [&](int q, int stage, int slot) {
    if (q < 2) glds16_sc1(avoff + q * NKG1 * 1024, abase + (size_t)stage * 1024, adst + q * 1024 + slot * G1_SLOT);
    else       glds16(bvoff[q - 2], bbase + (size_t)stage * 64 * LDW, bdst[q - 2] + slot * G1_SLOT);
  };
  auto rdA = [&](auto P, auto SL, int m) { fa[P.v][m] = lds_b128(ard + SL.v * G1_SLOT + m * 1024); };
  auto rdB = [&](auto P, auto SL, int n) { LAS char* p = brd[n % NB] + (n / NB) * 1024 + SL.v * G1_SLOT; fb[P.v][n] = cat8(lds_tr(p), lds_tr(p + 512)); };
  auto rdBh = [&](auto P, auto SL, int n, int h) {
    const bf16x4 v = lds_tr(brd[n % NB] + (n / NB) * 1024 + SL.v * G1_SLOT + h * 512);
    fb[P.v][n][4 * h] = v[0]; fb[P.v][n][4 * h + 1] = v[1]; fb[P.v][n][4 * h + 2] = v[2]; fb[P.v][n][4 * h + 3] = v[3];
  };
  auto step = [&](auto SC, auto VM, auto GL, auto RD, int s) {
    constexpr int P = SC.v & 1, SNv = (SC.v + 1) & 3;
    const IC<P ^ 1> Pn; const IC<SNv> SN;
    if constexpr (RD.v) { WAIT_V(VM.v); WAIT_L(0); BAR(); }
    SCHED();
#pragma unroll
    for (int i = 0; i < 4 * NT; ++i) {
      const int m = i & 3, n = i >> 2;
      if (QKV && n >= 4) acc[m][n] = MFMA16(fa[P][m], fb[P][n], acc[m][n]);
      else               acc[m][n] = MFMA16(fb[P][n], fa[P][m], acc[m][n]);
      if (i < 4)                { if constexpr (RD.v) rdA(Pn, SN, i); }
      else if (i < 4 + 2 * NT)  { if constexpr (RD.v) rdBh(Pn, SN, (i - 4) >> 1, (i - 4) & 1); }
      if (i % 4 == 3 && i < 16) { if constexpr (GL.v) issue(i / 4, s + 4, SC.v); }
      SCHED();
    }
  };

  const int slot = (QKV ? 0 : 16) + tcol;
  u64 tag;
  {
    const int r8 = lane >> 3, c8 = lane & 7;
    const int row = panel * 256 + slot * 8 + r8;
    const u16* src = x + (size_t)row * D_ + wid * 128 + c8 * 8;
    u32x4 v0, v1; u32x2 ep;
    asm volatile("global_load_dwordx4 %0, %3, off\n\tglobal_load_dwordx4 %1, %3, off offset:128\n\tglobal_load_dwordx2 %2, %4, off sc1"
                 : "=&v"(v0), "=&v"(v1), "=&v"(ep) : "v"(src), "v"(ws + WS_EPOCH) : "memory");
    SCHED();
    asm volatile("s_waitcnt vmcnt(0)" : "+v"(v0), "+v"(v1), "+v"(ep) :: "memory");
    SCHED();
    if (QKV) {
      const u32x4 fill = {~0u, ~0u, ~0u, ~0u};
      char* kp = kvx + (size_t)(panel * 16 + tcol) * KVX_TILE;
      st_sc1(kp + tid * 16, fill);
      st_sc1(kp + 8192 + i16 * 16, fill);
    }
    tag = (((u64)(unsigned)__builtin_amdgcn_readfirstlane(ep[1]) << 32) | (unsigned)__builtin_amdgcn_readfirstlane(ep[0])) + 1;
    f32x2 f[8];
#pragma unroll
    for (int i = 0; i < 4; ++i) { f[i] = (f32x2){blo(v0[i]), bhi(v0[i])}; f[4 + i] = (f32x2){blo(v1[i]), bhi(v1[i])}; }
    const f32x2 s2 = ((f[0] + f[1]) + (f[2] + f[3])) + ((f[4] + f[5]) + (f[6] + f[7]));
    const float mw = sum8lanes(s2[0] + s2[1]) * (1.0f / 128.f);
    f32x2 q2 = {0.f, 0.f};
#pragma unroll
    for (int i = 0; i < 8; ++i) { const f32x2 d = f[i] - (f32x2){mw, mw}; q2 = __builtin_elementwise_fma(d, d, q2); }
    const float qq = sum8lanes(q2[0] + q2[1]);
    LAS f32x2* stt = (LAS f32x2*)(lds + G1_X + 4096);
    if (c8 == 0) stt[r8 * 8 + wid] = (f32x2){mw, qq};
    WAIT_L(0); BAR();
    const f32x2 pw = stt[r8 * 8 + c8];
    const float mean = sum8lanes(pw[0]) * 0.125f;
    const float dm = pw[0] - mean;
    const float m2 = sum8lanes(__builtin_fmaf(128.f * dm, dm, pw[1]));
    const float mb = rbf(mean), vb = rbf(m2 * (1.0f / D_));
    const float rs = 1.0f / rbf(sqrtf(rbf(vb + 1e-5f)));
    u32x4 o0, o1;
#pragma unroll
    for (int i = 0; i < 8; ++i) {
      const f32x2 d = f[i] - (f32x2){mb, mb};
      unsigned u = pk2(d[0], d[1]);
      asm("" : "+v"(u));
      const f32x2 y = (f32x2){blo(u), bhi(u)} * (f32x2){rs, rs};
      if (i < 4) o0[i] = pk2(y[0], y[1]); else o1[i - 4] = pk2(y[0], y[1]);
    }
    char* dst = xn + ((size_t)((row >> 4) * NKG1 + wid * 4 + (c8 >> 2)) * 64 + (c8 & 3) * 16 + (row & 15)) * 16;
    st_sc1(dst, o0); st_sc1(dst + 2048, o1);
    WAIT_V(0); BAR();
    if (tid == 0) __hip_atomic_store((gu64*)(ws + WS_FLG + (panel * 32 + slot) * FS), tag, __ATOMIC_RELAXED, __HIP_MEMORY_SCOPE_AGENT);
    if (QKV && wid >= 4) glds16((unsigned)lane * 16, uniform_ptr((const char*)wphi + (size_t)tcol * 4096 + (wid - 4) * 1024), ldsb + G1_X + (wid - 4) * 1024);
    if (!QKV && wid == 4) glds16((unsigned)(lane < 28 ? lane : 27) * 16, uniform_ptr((const char*)(b1 + tcol * 224)), ldsb + G1_X);
#pragma unroll
    for (int st = 0; st < 4; ++st) { issue(2, st, st); issue(3, st, st); }
    if (wid == 0) wait_flags(ws + WS_FLG + ((QKV && lane >= 32) ? ((lane & 7) * 32 + slot) : (panel * 32 + (lane & 31))) * FS, tag);
    BAR();
  }
#pragma unroll
  for (int st = 0; st < 4; ++st) { issue(0, st, st); issue(1, st, st); }
  WAIT_V(2); BAR();
  {
    const IC<0> P0; const IC<0> S0;
#pragma unroll
    for (int m = 0; m < 4; ++m) rdA(P0, S0, m);
#pragma unroll
    for (int n = 0; n < NT; ++n) rdB(P0, S0, n);
  }
  if (!QKV) {
#pragma unroll
    for (int n = 0; n < NT; ++n) {
      const u32x2 bb = *(LAS u32x2*)(lds + G1_X + (wc * 112 + 4 * g + n * 16) * 2);
#pragma unroll
      for (int m = 0; m < 4; ++m) acc[m][n] = (f32x4){blo(bb[0]), bhi(bb[0]), blo(bb[1]), bhi(bb[1])};
    }
  }
#pragma unroll 1
  for (int s = 0; s < 28; s += 4) {
    if (s == 8) {
      const int ln = __builtin_amdgcn_mbcnt_hi(~0u, __builtin_amdgcn_mbcnt_lo(~0u, 0u));
      char* xp = ws + WS_XCH + (size_t)blockIdx.x * 16384 + (wid * 64 + ln) * 16;
      const u32x4 fill = {~0u, ~0u, ~0u, ~0u};
      st_sc1(xp, fill); st_sc1(xp + 8192, fill);
    }
    step(IC<0>{}, IC<8>{}, IC<1>{}, IC<1>{}, s);
    step(IC<1>{}, IC<8>{}, IC<1>{}, IC<1>{}, s + 1);
    step(IC<2>{}, IC<8>{}, IC<1>{}, IC<1>{}, s + 2);
    step(IC<3>{}, IC<8>{}, IC<1>{}, IC<1>{}, s + 3);
  }
  step(IC<0>{}, IC<8>{}, IC<0>{}, IC<1>{}, 28);
  constexpr int NX = 1;
  {
    int wv = blockIdx.x * 8 + wid;
    if (wv >= 1152) wv -= 1152;
    const char* src = uniform_ptr(wv < 896 ? (const char*)w2 + (size_t)wv * 8192 : (const char*)wo + (size_t)(wv - 896) * 8192);
    asm volatile("s_mov_b32 m0, %2\n\ts_nop 0\n\tglobal_load_lds_dword %0, %1" ::"v"(lane * 128), "s"(src), "s"(ldsb + G1_X + 4096 + wid * 256) : "memory");
  }
  step(IC<1>{}, IC<4 + NX>{}, IC<0>{}, IC<1>{}, 29);
  step(IC<2>{}, IC<0 + NX>{}, IC<0>{}, IC<1>{}, 30);
  step(IC<3>{}, IC<0>{}, IC<0>{}, IC<0>{}, 31);

  if (!QKV) {
    auto act4 = [&](int m, int n) {
      const f32x2 yA = gelu2((f32x2){acc[m][n][0], acc[m][n][1]});
      const f32x2 yB = gelu2((f32x2){acc[m][n][2], acc[m][n][3]});
      return (u32x2){pk2(yA[0], yA[1]), pk2(yB[0], yB[1])};
    };
    const int rt0 = panel * 16 + wr * 4;
    char* obase = (char*)act + ((size_t)rt0 * NKG2 + tcol * 7) * 1024 + lane * 16;
    if (wc == 0) {
#pragma unroll
      for (int m = 0; m < 4; ++m) {
        char* rb = obase + (size_t)m * NKG2 * 1024;
#pragma unroll
        for (int p = 0; p < 3; ++p) { const u32x2 lo = act4(m, 2 * p), hi = act4(m, 2 * p + 1); st_sc1(rb + p * 1024, (u32x4){lo[0], lo[1], hi[0], hi[1]}); }
        st8_sc1(rb + 3 * 1024, act4(m, 6 % NT));
      }
    } else {
#pragma unroll
      for (int m = 0; m < 4; ++m) {
        char* rb = obase + (size_t)m * NKG2 * 1024;
        st8_sc1(rb + 3 * 1024 + 8, act4(m, 0));
#pragma unroll
        for (int p = 0; p < 3; ++p) { const u32x2 lo = act4(m, 2 * p + 1), hi = act4(m, (2 * p + 2) % NT); st_sc1(rb + (4 + p) * 1024, (u32x4){lo[0], lo[1], hi[0], hi[1]}); }
      }
    }
  } else {
    const int head = tcol * 2 + wc;
    LAS char* wp = lds + G1_X + wc * 2048;
    bf16x8 wf[2];
#pragma unroll
    for (int ft = 0; ft < 2; ++ft) {
      LAS char* p = wp + (4 * g + q4) * 64 + (16 * ft + 4 * p4) * 2;
      wf[ft] = cat8(lds_tr(p), lds_tr(p + 16 * 64));
    }
    const f32x4 z4 = {0.f, 0.f, 0.f, 0.f};
    LAS char* sc = lds + (wid >> 2) * G1_B + (wid & 3) * 8704;
#pragma unroll
    for (int mp = 0; mp < 2; ++mp) {
      f32x4 pp[2][2]; u32x4 pos[2], neg[2];
#pragma unroll
      for (int t = 0; t < 2; ++t) {
        const bf16x8 bk = pack8(acc[2 * mp + t][2], acc[2 * mp + t][3]);
        pp[t][0] = MFMA16(wf[0], bk, z4); pp[t][1] = MFMA16(wf[1], bk, z4);
      }
      dual_softmax2(pp, pos, neg);
#pragma unroll
      for (int t = 0; t < 2; ++t) {
        LAS char* rowp = sc + (16 * (2 * mp + t) + i16) * 136 + 8 * g;
        *(LAS u32x2*)(rowp) = (u32x2){pos[t][0], pos[t][1]};      *(LAS u32x2*)(rowp + 32) = (u32x2){pos[t][2], pos[t][3]};
        *(LAS u32x2*)(rowp + 64) = (u32x2){neg[t][0], neg[t][1]}; *(LAS u32x2*)(rowp + 96) = (u32x2){neg[t][2], neg[t][3]};
      }
    }
    WAIT_L(0);
    f32x4 kv[4][3];
#pragma unroll
    for (int ft = 0; ft < 4; ++ft)
#pragma unroll
      for (int d = 0; d < 3; ++d) kv[ft][d] = z4;
    bf16x8 ones;
#pragma unroll
    for (int j = 0; j < 8; ++j) ones[j] = (__bf16)1.0f;
#pragma unroll
    for (int ks = 0; ks < 2; ++ks) {
      const bf16x8 bv0 = pack8(acc[2 * ks][4], acc[2 * ks + 1][4]);
      const bf16x8 bv1 = pack8(acc[2 * ks][5], acc[2 * ks + 1][5]);
#pragma unroll
      for (int ft = 0; ft < 4; ++ft) {
        LAS char* p = sc + (32 * ks + 4 * g + q4) * 136 + (16 * ft + 4 * p4) * 2;
        const bf16x8 a = cat8(lds_tr(p), lds_tr(p + 16 * 136));
        kv[ft][0] = MFMA16(a, bv0, kv[ft][0]);
        kv[ft][1] = MFMA16(a, bv1, kv[ft][1]);
        kv[ft][2] = MFMA16(a, ones, kv[ft][2]);
      }
    }
    WAIT_L(0); BAR();
    {
      LAS char* pp = lds + wid * 8448;
#pragma unroll
      for (int sg = 0; sg < 2; ++sg) {
#pragma unroll
        for (int d = 0; d < 2; ++d) {
          *(LAS f32x4*)(pp + (sg * 2 + d) * 2048 + lane * 16) = kv[2 * sg][d];
          *(LAS f32x4*)(pp + (sg * 2 + d) * 2048 + 1024 + lane * 16) = kv[2 * sg + 1][d];
        }
        if (i16 == 0) {
          *(LAS f32x4*)(pp + 8192 + sg * 128 + g * 16) = kv[2 * sg][2];
          *(LAS f32x4*)(pp + 8192 + sg * 128 + 64 + g * 16) = kv[2 * sg + 1][2];
        }
      }
    }
    WAIT_L(0); BAR();
    char* kvt = kvx + (size_t)tcol * KVX_TILE;
    char* kvmine = kvt + (size_t)panel * 16 * KVX_TILE;
    {
      LAS char* pp = lds + (tid >> 8) * 8448 + ((tid >> 6) & 3) * 2048 + lane * 16;
      f32x4 lo = *(LAS f32x4*)(pp), hi = *(LAS f32x4*)(pp + 1024);
#pragma unroll
      for (int w = 1; w < 4; ++w) { lo += *(LAS f32x4*)(pp + w * 16896); hi += *(LAS f32x4*)(pp + w * 16896 + 1024); }
      st_sc1(kvmine + tid * 16, (u32x4){pk2(lo[0], lo[1]), pk2(lo[2], lo[3]), pk2(hi[0], hi[1]), pk2(hi[2], hi[3])});
    }
    if (tid < 16) {
      LAS char* pp = lds + (tid >> 3) * 8448 + 8192 + ((tid >> 2) & 1) * 128 + (tid & 3) * 16;
      f32x4 lo = *(LAS f32x4*)(pp), hi = *(LAS f32x4*)(pp + 64);
#pragma unroll
      for (int w = 1; w < 4; ++w) { lo += *(LAS f32x4*)(pp + w * 16896); hi += *(LAS f32x4*)(pp + w * 16896 + 64); }
      st_sc1(kvmine + 8192 + tid * 16, (u32x4){pk2(lo[0], lo[1]), pk2(lo[2], lo[3]), pk2(hi[0], hi[1]), pk2(hi[2], hi[3])});
    }
    u32x4 qp[2][2], qn[2][2];
    auto qsm = [&](int mp) {
      f32x4 pp[2][2];
#pragma unroll
      for (int t = 0; t < 2; ++t) {
        const bf16x8 bq = pack8(acc[2 * mp + t][0], acc[2 * mp + t][1]);
        pp[t][0] = MFMA16(wf[0], bq, z4); pp[t][1] = MFMA16(wf[1], bq, z4);
      }
      dual_softmax2(pp, qp[mp], qn[mp]);
    };
    qsm(0);
    const char* kb0 = uniform_ptr(kvt), * kb1 = uniform_ptr(kvt + (size_t)32 * KVX_TILE);
    const char* kb2 = uniform_ptr(kvt + (size_t)64 * KVX_TILE), * kb3 = uniform_ptr(kvt + (size_t)96 * KVX_TILE);
    u32x4 pv[8], pz[8];
#define KV_GATHER(pv, OFF, TAIL) asm volatile( \
          "global_load_dwordx4 %0, %8, %10 sc1\n\tglobal_load_dwordx4 %1, %9, %10 sc1\n\t" \
          "global_load_dwordx4 %2, %8, %11 sc1\n\tglobal_load_dwordx4 %3, %9, %11 sc1\n\t" \
          "global_load_dwordx4 %4, %8, %12 sc1\n\tglobal_load_dwordx4 %5, %9, %12 sc1\n\t" \
          "global_load_dwordx4 %6, %8, %13 sc1\n\tglobal_load_dwordx4 %7, %9, %13 sc1" TAIL \
          : "=&v"(pv[0]), "=&v"(pv[1]), "=&v"(pv[2]), "=&v"(pv[3]), "=&v"(pv[4]), "=&v"(pv[5]), "=&v"(pv[6]), "=&v"(pv[7]) \
          : "v"(OFF), "v"((OFF) + 16 * KVX_TILE), "s"(kb0), "s"(kb1), "s"(kb2), "s"(kb3) : "memory")
#define KV_WAIT(pv) asm volatile("s_waitcnt vmcnt(0)" \
          : "+v"(pv[0]), "+v"(pv[1]), "+v"(pv[2]), "+v"(pv[3]), "+v"(pv[4]), "+v"(pv[5]), "+v"(pv[6]), "+v"(pv[7]) :: "memory")
    LAS char* kvs = lds + 98304;
    auto finish = [&](u32x4 (&pv)[8], unsigned off) {
      for (unsigned spins = 0;; ++spins) {
        unsigned m = 0;
#pragma unroll
        for (int p = 0; p < 8; ++p) m = umax4(pv[p], m);
        if (__all(m != 0xffffffffu) || spins > (1u << 14)) break;
        __builtin_amdgcn_s_sleep(2);
        KV_GATHER(pv, off, "\n\ts_waitcnt vmcnt(0)");
      }
      f32x2 t[4];
#pragma unroll
      for (int r = 0; r < 4; ++r) t[r] = (f32x2){blo(pv[0][r]), bhi(pv[0][r])};
#pragma unroll
      for (int p = 1; p < 8; ++p)
#pragma unroll
        for (int r = 0; r < 4; ++r) t[r] += (f32x2){blo(pv[p][r]), bhi(pv[p][r])};
      *(LAS u32x4*)(kvs + off) = (u32x4){pk2(t[0][0], t[0][1]), pk2(t[1][0], t[1][1]), pk2(t[2][0], t[2][1]), pk2(t[3][0], t[3][1])};
    };
    const unsigned go = (unsigned)tid * 16, gz = 8192u + (unsigned)i16 * 16;
    SCHED();
    KV_GATHER(pv, go, "");
    KV_GATHER(pz, gz, "");
    SCHED();
    qsm(1);
    SCHED();
    KV_WAIT(pv);
    KV_WAIT(pz);
    SCHED();
    finish(pv, go);
    if (wid == 0) finish(pz, gz);
    WAIT_L(0); BAR();
    bf16x8 a[2][3];
#pragma unroll
    for (int sg = 0; sg < 2; ++sg) {
#pragma unroll
      for (int d = 0; d < 2; ++d) a[sg][d] = lds_b128(kvs + wc * 4096 + (sg * 2 + d) * 1024 + lane * 16);
      a[sg][2] = lds_b128(kvs + 8192 + wc * 128 + sg * 64 + g * 16);
    }
#pragma unroll
    for (int m = 0; m < 4; ++m) {
      const bf16x8 bp = __builtin_bit_cast(bf16x8, qp[m >> 1][m & 1]), bn = __builtin_bit_cast(bf16x8, qn[m >> 1][m & 1]);
      f32x4 o0 = MFMA16(a[0][0], bp, z4), o1 = MFMA16(a[0][1], bp, z4), dn = MFMA16(a[0][2], bp, z4);
      o0 = MFMA16(a[1][0], bn, o0); o1 = MFMA16(a[1][1], bn, o1); dn = MFMA16(a[1][2], bn, dn);
      const float rd = __builtin_amdgcn_rcpf(dn[0] + 1e-6f);
      const int rt = panel * 16 + wr * 4 + m;
      u32x4 w;
      w[0] = pk2(o0[0] * rd, o0[1] * rd); w[1] = pk2(o0[2] * rd, o0[3] * rd);
      w[2] = pk2(o1[0] * rd, o1[1] * rd); w[3] = pk2(o1[2] * rd, o1[3] * rd);
      st_sc1((char*)act + ((size_t)rt * NKG2 + 112 + head) * 1024 + lane * 16, w);
    }
  }
  return tag;
}

constexpr int G2_SLOT = 32768;
constexpr int G2_POLL = 4 * G2_SLOT + 4096;
constexpr int KSPLIT = 36;

DI void gemm2_body(char* ws, const u16* __restrict__ w2, const u16* __restrict__ wo,
                   const u16* __restrict__ x, const u16* __restrict__ b2, u16* __restrict__ out,
                   u64 tag, int slot, int split, int panel, int pm, int pn, LAS char* lds) {
  const char* act = ws + WS_ACT;
  char* xch = ws + WS_XCH;
  char* flg2 = ws + WS_FLG2 + panel * 32 * FS;
  const unsigned ldsb = (unsigned)(size_t)lds;
  int lane = __builtin_amdgcn_mbcnt_hi(~0u, __builtin_amdgcn_mbcnt_lo(~0u, 0u));
  asm volatile("" : "+v"(lane));
  const int wid = __builtin_amdgcn_readfirstlane(threadIdx.x >> 6);
  const int kh = wid >> 2, wr = (wid >> 1) & 1, wc = wid & 1;
  const int g = lane >> 4, i16 = lane & 15, q4 = i16 >> 2, p4 = lane & 3;
  const int b = blockIdx.x;
  const int kt0 = split ? KSPLIT : 0, nst = split ? 72 - KSPLIT : KSPLIT;
  const char* abase = uniform_ptr(act + ((size_t)(pm * 8) * NKG2 + kt0 * 2) * 1024);
  const unsigned avoff = (unsigned)wid * NKG2 * 1024 + lane * 16;
  const unsigned adst = ldsb + wid * 1024;
  const int r8 = lane >> 3;
  const unsigned bvoff = (unsigned)((wid * 8 + r8) * D_ + pn * 128 + (((lane & 7) ^ (((r8 >> 1) & 3) << 1)) << 3)) * 2;
  const unsigned bdst = ldsb + 16384 + wid * 2048;
  const char* wb = uniform_ptr((const char*)w2 + (size_t)kt0 * 64 * D_ * 2);
  auto issue = [&](int q, int t, int slot) {
    if (q < 2) glds16_sc1(avoff + q * 1024, abase + (size_t)t * 2048, adst + q * 8192 + slot * G2_SLOT);
    else       glds16(bvoff + (q - 2) * 128, wb + (size_t)t * 64 * D_ * 2, bdst + (q - 2) * 1024 + slot * G2_SLOT);
  };
  WAIT_V(0); WAIT_L(0); BAR();
  if (threadIdx.x == 0) __hip_atomic_store((gu64*)(flg2 + slot * FS), tag, __ATOMIC_RELAXED, __HIP_MEMORY_SCOPE_AGENT);
  if (wid == panel) wait_flags(flg2 + (16 + (split ? 10 + (i16 < 6 ? i16 : 0) : (i16 < 11 ? i16 : 0))) * FS, tag);
#pragma unroll
  for (int st = 0; st < 4; ++st) { issue(2, st, st); issue(3, st, st); }
  BAR();
#pragma unroll
  for (int st = 0; st < 4; ++st) { issue(0, st, st); issue(1, st, st); }

  LAS char* ard[2];
  ard[0] = opaque(lds + kh * 8192 + wr * 4096 + lane * 16); ard[1] = opaque(ard[0] + 65536);
  LAS char* brd[2][4];
#pragma unroll
  for (int j = 0; j < 4; ++j) {
    const int r = 4 * (g & 1) + q4, sw = (r >> 1) & 3;
    brd[0][j] = opaque(lds + 16384 + ((kh * 4 + (g >> 1)) * 2 + wc) * 1024 + r * 128 + ((((2 * j) ^ (sw << 1)) + (p4 >> 1)) << 4) + (p4 & 1) * 8);
    brd[1][j] = opaque(brd[0][j] + 65536);
  }
  f32x4 acc[4][4];
#pragma unroll
  for (int m = 0; m < 4; ++m)
#pragma unroll
    for (int n = 0; n < 4; ++n) acc[m][n] = (f32x4){0.f, 0.f, 0.f, 0.f};
  bf16x8 fa[2][4], fb[2][4];
  auto rdA = [&](auto P, auto SL, int m) { fa[P.v][m] = lds_b128(ard[SL.v >> 1] + (SL.v & 1) * G2_SLOT + m * 1024); };
  auto rdB = [&](auto P, auto SL, int n) { LAS char* p = brd[SL.v >> 1][n] + (SL.v & 1) * G2_SLOT; fb[P.v][n] = cat8(lds_tr(p), lds_tr(p + 4096)); };
  auto rdBh = [&](auto P, auto SL, int n, int h) {
    const bf16x4 v = lds_tr(brd[SL.v >> 1][n] + (SL.v & 1) * G2_SLOT + h * 4096);
    fb[P.v][n][4 * h] = v[0]; fb[P.v][n][4 * h + 1] = v[1]; fb[P.v][n][4 * h + 2] = v[2]; fb[P.v][n][4 * h + 3] = v[3];
  };
  auto step = [&](auto SC, auto VM, auto GL, auto RD, int t) {
    constexpr int P = SC.v & 1, SNv = (SC.v + 1) & 3;
    const IC<P ^ 1> Pn; const IC<SNv> SN;
    if constexpr (RD.v) { WAIT_V(VM.v); WAIT_L(0); BAR(); }
    SCHED();
#pragma unroll
    for (int i = 0; i < 16; ++i) {
      acc[i & 3][i >> 2] = MFMA16(fb[P][i >> 2], fa[P][i & 3], acc[i & 3][i >> 2]);
      if (i < 4)       { if constexpr (RD.v) rdA(Pn, SN, i); }
      else if (i < 12) { if constexpr (RD.v) rdBh(Pn, SN, (i - 4) >> 1, (i - 4) & 1); }
      if (i % 3 == 2 && i < 12) { if constexpr (GL.v) issue(i / 3, t + 4, SC.v); }
      SCHED();
    }
  };
  WAIT_V(2); BAR();
  {
    const IC<0> P0; const IC<0> S0;
#pragma unroll
    for (int m = 0; m < 4; ++m) rdA(P0, S0, m);
#pragma unroll
    for (int n = 0; n < 4; ++n) rdB(P0, S0, n);
  }
  const IC<1> Y; const IC<0> N_; const IC<8> V8;
  const int tp = split ? 8 : 24;
#pragma unroll 1
  for (int t = 0; t < nst - 4; t += 4) {
    if (wid == 0) {
      if (t == tp) {
        asm volatile("s_mov_b32 m0, %2\n\ts_nop 0\n\tglobal_load_lds_dword %0, %1 sc1" ::"v"((lane >> 1) * FS + (lane & 1) * 4), "s"(uniform_ptr(flg2)), "s"(ldsb + G2_POLL) : "memory");
      } else if (t == tp + 4) {
        if (!__all(*(LAS u64*)(lds + G2_POLL + (lane & 31) * 8) == tag)) wait_flags(flg2 + (lane & 31) * FS, tag);
      }
    }
    if (t == 52 - KSPLIT && split == 1) wb = uniform_ptr((const char*)wo - (size_t)(56 - KSPLIT) * 64 * D_ * 2);
    step(IC<0>{}, V8, Y, Y, t); step(IC<1>{}, V8, Y, Y, t + 1); step(IC<2>{}, V8, Y, Y, t + 2); step(IC<3>{}, V8, Y, Y, t + 3);
  }
  step(IC<0>{}, V8, N_, Y, 0);
  step(IC<1>{}, IC<4>{}, N_, Y, 0);
  step(IC<2>{}, IC<0>{}, N_, Y, 0);
  step(IC<3>{}, IC<0>{}, N_, N_, 0);
  const int quad = wid & 3;
  {
    LAS char* pp = lds + (quad * 2 + kh) * 8192 + lane * 16;
#pragma unroll
    for (int mm = 0; mm < 2; ++mm)
#pragma unroll
      for (int n = 0; n < 4; ++n) *(LAS f32x4*)(pp + (mm * 4 + n) * 1024) = kh ? acc[mm][n] : acc[2 + mm][n];
  }
  const bool keep = (wr == split);
  const int row0 = pm * 128 + wr * 64 + kh * 32 + i16, col0 = pn * 128 + wc * 64 + 4 * g;
  u32x2 xv[2][4], bv[4];
  if (keep) {
#pragma unroll
    for (int n = 0; n < 4; ++n) bv[n] = *(const u32x2*)(b2 + col0 + n * 16);
#pragma unroll
    for (int mm = 0; mm < 2; ++mm)
#pragma unroll
      for (int n = 0; n < 4; ++n) xv[mm][n] = *(const u32x2*)(x + (size_t)(row0 + mm * 16) * D_ + col0 + n * 16);
  }
  WAIT_L(0); BAR();
  f32x4 sum[2][4];
  {
    LAS char* pp = lds + (quad * 2 + (kh ^ 1)) * 8192 + lane * 16;
#pragma unroll
    for (int mm = 0; mm < 2; ++mm)
#pragma unroll
      for (int n = 0; n < 4; ++n) sum[mm][n] = (kh ? acc[2 + mm][n] : acc[mm][n]) + *(LAS f32x4*)(pp + (mm * 4 + n) * 1024);
  }
  const int piece = kh * 2 + wc;
  if (!keep) {
    char* dst = xch + (size_t)b * 16384 + piece * 4096 + lane * 16;
#pragma unroll
    for (int mm = 0; mm < 2; ++mm)
#pragma unroll
      for (int h = 0; h < 2; ++h)
        st_sc1(dst + (mm * 2 + h) * 1024, (u32x4){pk2(sum[mm][2 * h][0], sum[mm][2 * h][1]), pk2(sum[mm][2 * h][2], sum[mm][2 * h][3]),
                                                  pk2(sum[mm][2 * h + 1][0], sum[mm][2 * h + 1][1]), pk2(sum[mm][2 * h + 1][2], sum[mm][2 * h + 1][3])});
    if (b == 0 && wid == 0) {
#pragma unroll
      for (int j = 0; j < 4; ++j) wait_flags(ws + WS_FLG + (j * 64 + lane) * FS, tag);
      if (lane == 0) __hip_atomic_store((gu64*)(ws + WS_EPOCH), tag, __ATOMIC_RELAXED, __HIP_MEMORY_SCOPE_AGENT);
    }
  } else {
    const char* src = xch + (size_t)(b ^ 1) * 16384 + piece * 4096 + lane * 16;
    u32x4 pv[4];
    for (unsigned spins = 0;; ++spins) {
      asm volatile(
          "global_load_dwordx4 %0, %4, off sc1\n\tglobal_load_dwordx4 %1, %4, off offset:1024 sc1\n\t"
          "global_load_dwordx4 %2, %4, off offset:2048 sc1\n\tglobal_load_dwordx4 %3, %4, off offset:3072 sc1\n\ts_waitcnt vmcnt(0)"
          : "=&v"(pv[0]), "=&v"(pv[1]), "=&v"(pv[2]), "=&v"(pv[3]) : "v"(src) : "memory");
      if (__all(fresh4(pv[0]) && fresh4(pv[1]) && fresh4(pv[2]) && fresh4(pv[3])) || spins > (1u << 14)) break;
      __builtin_amdgcn_s_sleep(1);
    }
#pragma unroll
    for (int mm = 0; mm < 2; ++mm)
#pragma unroll
      for (int n = 0; n < 4; ++n) {
        const unsigned q0 = pv[mm * 2 + (n >> 1)][2 * (n & 1)], q1 = pv[mm * 2 + (n >> 1)][2 * (n & 1) + 1];
        const f32x4 v = sum[mm][n];
        u32x2 o;
        o[0] = pk2(v[0] + blo(q0) + blo(xv[mm][n][0]) + blo(bv[n][0]), v[1] + bhi(q0) + bhi(xv[mm][n][0]) + bhi(bv[n][0]));
        o[1] = pk2(v[2] + blo(q1) + blo(xv[mm][n][1]) + blo(bv[n][1]), v[3] + bhi(q1) + bhi(xv[mm][n][1]) + bhi(bv[n][1]));
        *(u32x2*)(out + (size_t)(row0 + mm * 16) * D_ + col0 + n * 16) = o;
      }
  }
}

__global__ __launch_bounds__(512, 2) void k_main(const u16* __restrict__ x, const u16* __restrict__ wqkv, const u16* __restrict__ w1,
                                                 const u16* __restrict__ b1, const u16* __restrict__ wphi,
                                                 const u16* __restrict__ w2, const u16* __restrict__ wo, const u16* __restrict__ b2,
                                                 char* ws, u16* __restrict__ out) {
  extern __shared__ __attribute__((aligned(16))) char smem[];
  LAS char* lds = (LAS char*)smem;
  const int b = blockIdx.x, grp = b & 7, idx = b >> 3;
  const int panel = idx & 7, kind = grp & 1, tcol = (grp >> 1) * 4 + (idx >> 3);
  u64 tag;
  if (kind == 0) {
    tag = gemm1_body<true>(x, wqkv, tcol * 64, 0, panel, tcol, b1, wphi, ws, w2, wo, lds);
  } else {
    const int c0 = tcol * 224, woff = c0 & 63;
    tag = gemm1_body<false>(x, w1, c0 - woff, woff, panel, tcol, b1, wphi, ws, w2, wo, lds);
  }
  gemm2_body(ws, w2, wo, x, b2, out, tag, kind * 16 + tcol,   kind ^ 1, panel, panel * 2 + ((grp >> 1) & 1), (grp >> 2) * 4 + (idx >> 3), lds);
}

extern "C" void kernel_launch(void* const* d_in, const int* in_sizes, int n_in, void* d_out, int out_size, void* d_ws, size_t ws_size,
                              hipStream_t stream) {
  (void)hipFuncSetAttribute((const void*)k_main, hipFuncAttributeMaxDynamicSharedMemorySize, G1_LDS);
  k_main<<<256, 512, G1_LDS, stream>>>((const u16*)d_in[0], (const u16*)d_in[1], (const u16*)d_in[5], (const u16*)d_in[6], (const u16*)d_in[2],
                                       (const u16*)d_in[7], (const u16*)d_in[3], (const u16*)d_in[8], (char*)d_ws, (u16*)d_out);
}
```

```cpp
#include <hip/hip_runtime.h>
#include <hip/hip_bf16.h>
#include <stdint.h>

typedef __bf16 bf16x8 __attribute__((ext_vector_type(8)));
typedef __bf16 bf16x4 __attribute__((ext_vector_type(4)));
typedef __bf16 bf16x2 __attribute__((ext_vector_type(2)));
typedef float f32x4 __attribute__((ext_vector_type(4)));
typedef float f32x2 __attribute__((ext_vector_type(2)));
typedef unsigned u32x4 __attribute__((ext_vector_type(4)));
typedef unsigned u32x2 __attribute__((ext_vector_type(2)));
typedef unsigned short u16;
#define LAS __attribute__((address_space(3)))
#define DI __device__ __forceinline__

constexpr int S_ = 2048, D_ = 1024, F_ = 3584, H_ = 32;
constexpr int KCAT = F_ + D_;
constexpr int NKG1 = D_ / 32;
constexpr int NKG2 = KCAT / 32;

constexpr size_t WS_XN   = 0;
constexpr size_t WS_ACT  = WS_XN + (size_t)S_ * D_ * 2;
constexpr size_t WS_KVX  = WS_ACT + (size_t)S_ * KCAT * 2;
constexpr int KVX_TILE = 8192 + 256;
constexpr size_t KVX_BYTES = (size_t)8 * 16 * KVX_TILE;
constexpr size_t WS_XCH  = WS_KVX + KVX_BYTES;
constexpr size_t WS_FLG  = WS_XCH + (size_t)256 * 16384;
constexpr int FS = 8;
constexpr size_t WS_FLG2 = WS_FLG + 256 * FS, WS_EPOCH = WS_FLG + 512 * FS + 128;

DI float rbf(float x) { return (float)(__bf16)x; }
DI unsigned pk2(float lo, float hi) { bf16x2 v; v[0] = (__bf16)lo; v[1] = (__bf16)hi; return __builtin_bit_cast(unsigned, v); }
DI float blo(unsigned u) { return __uint_as_float(u << 16); }
DI float bhi(unsigned u) { return __uint_as_float(u & 0xffff0000u); }

#define MFMA16(a, b, c) __builtin_amdgcn_mfma_f32_16x16x32_bf16((a), (b), (c), 0, 0, 0)
#define WAIT_V(n) asm volatile("s_waitcnt vmcnt(%0)" ::"n"(n) : "memory")
#define WAIT_L(n) do { static_assert((n) == 0, ""); asm volatile("" ::: "memory"); __builtin_amdgcn_s_waitcnt(0xC07F); asm volatile("" ::: "memory"); } while (0)
#define BAR() do { asm volatile("" ::: "memory"); __builtin_amdgcn_s_barrier(); asm volatile("" ::: "memory"); } while (0)
#define SCHED() __builtin_amdgcn_sched_barrier(0)
template <int N> struct IC { static constexpr int v = N; };

DI void glds16_sc1(unsigned voff, const void* sbase, unsigned ldsaddr) {
  asm volatile("s_mov_b32 m0, %2\n\ts_nop 0\n\tglobal_load_lds_dwordx4 %0, %1 sc1" ::"v"(voff), "s"(sbase), "s"(ldsaddr) : "memory");
}
DI void glds16(unsigned voff, const void* sbase, unsigned ldsaddr) {
  asm volatile("s_mov_b32 m0, %2\n\ts_nop 0\n\tglobal_load_lds_dwordx4 %0, %1" ::"v"(voff), "s"(sbase), "s"(ldsaddr) : "memory");
}
DI const char* uniform_ptr(const char* p) {
  const unsigned long long a = (unsigned long long)p;
  unsigned lo = __builtin_amdgcn_readfirstlane((unsigned)a), hi = __builtin_amdgcn_readfirstlane((unsigned)(a >> 32));
  asm volatile("s_nop 4" : "+s"(lo), "+s"(hi));
  return (const char*)(((unsigned long long)hi << 32) | lo);
}
DI LAS char* opaque(LAS char* p) { asm volatile("" : "+v"(p)); return p; }
DI bf16x8 lds_b128(LAS char* p) { return *(LAS bf16x8*)p; }
DI bf16x4 lds_tr(LAS char* p) { return __builtin_amdgcn_ds_read_tr16_b64_v4bf16((LAS bf16x4*)p); }
DI bf16x8 cat8(bf16x4 a, bf16x4 b) { return __builtin_shufflevector(a, b, 0, 1, 2, 3, 4, 5, 6, 7); }
DI bf16x8 pack8(f32x4 a, f32x4 b) {
  bf16x8 v;
  v[0] = (__bf16)a[0]; v[1] = (__bf16)a[1]; v[2] = (__bf16)a[2]; v[3] = (__bf16)a[3];
  v[4] = (__bf16)b[0]; v[5] = (__bf16)b[1]; v[6] = (__bf16)b[2]; v[7] = (__bf16)b[3];
  return v;
}

typedef unsigned long long u64;
typedef __attribute__((address_space(1))) u64 gu64;
DI void st_sc1(void* p, u32x4 v) { asm volatile("global_store_dwordx4 %0, %1, off sc1\n\ts_nop 1" ::"v"(p), "v"(v) : "memory"); }
DI unsigned umax4(u32x4 v, unsigned m) { return max(max(max(v[0], v[1]), v[2]), max(v[3], m)); }
DI void st8_sc1(void* p, u32x2 v) { asm volatile("global_store_dwordx2 %0, %1, off sc1\n\ts_nop 1" ::"v"(p), "v"(v) : "memory"); }
DI bool fresh4(u32x4 v) { return umax4(v, 0u) != 0xffffffffu; }
#define XRED4(OP, a1, b1, a2, b2) asm volatile( \
    "s_nop 1\n\tv_permlane16_swap_b32 %0, %1\n\tv_permlane16_swap_b32 %2, %3\n\t" \
    OP " %0, %0, %1\n\t" OP " %2, %2, %3\n\tv_mov_b32 %1, %0\n\tv_mov_b32 %3, %2\n\ts_nop 0\n\t" \
    "v_permlane32_swap_b32 %0, %1\n\tv_permlane32_swap_b32 %2, %3\n\t" \
    OP " %0, %0, %1\n\t" OP " %2, %2, %3\n\tv_mov_b32 %1, %0\n\tv_mov_b32 %3, %2\n\ts_nop 0\n\t" \
    "v_permlane16_swap_b32 %0, %1\n\tv_permlane16_swap_b32 %2, %3" \
    : "+v"(a1), "+v"(b1), "+v"(a2), "+v"(b2))
DI float max8(const float* v) {
  const float t1 = fmaxf(fmaxf(v[0], v[1]), v[2]), t2 = fmaxf(fmaxf(v[3], v[4]), v[5]), t3 = fmaxf(fmaxf(v[6], v[7]), t1);
  return fmaxf(t2, t3);
}
DI float min8(const float* v) {
  const float t1 = fminf(fminf(v[0], v[1]), v[2]), t2 = fminf(fminf(v[3], v[4]), v[5]), t3 = fminf(fminf(v[6], v[7]), t1);
  return fminf(t2, t3);
}
DI void dual_softmax2(const f32x4 (&p)[2][2], u32x4 (&pos)[2], u32x4 (&neg)[2]) {
  float v[2][8];
#pragma unroll
  for (int t = 0; t < 2; ++t)
#pragma unroll
    for (int h = 0; h < 2; ++h)
#pragma unroll
      for (int r = 0; r < 2; ++r) {
        unsigned u = pk2(p[t][h][2 * r], p[t][h][2 * r + 1]);
        asm("" : "+v"(u));
        v[t][4 * h + 2 * r] = blo(u); v[t][4 * h + 2 * r + 1] = bhi(u);
      }
  float mx0 = max8(v[0]), nm0 = -min8(v[0]), mx1 = max8(v[1]), nm1 = -min8(v[1]);
  XRED4("v_max_f32", mx0, nm0, mx1, nm1);
  constexpr float L2E = 1.44269504f;
  const f32x2 kp = {L2E, L2E}, kn = {-L2E, -L2E};
  f32x2 ep[2][4], en[2][4];
  float sp[2], sn[2];
#pragma unroll
  for (int t = 0; t < 2; ++t) {
    const float cpv = t ? mx1 : mx0, cnv = t ? nm1 : nm0;
    const f32x2 cp = {cpv, cpv}, cn = {cnv, cnv};
#pragma unroll
    for (int r = 0; r < 4; ++r) {
      const f32x2 x = {v[t][2 * r], v[t][2 * r + 1]};
      const f32x2 ap = (x - cp) * kp, an = (x + cn) * kn;
      ep[t][r] = (f32x2){__builtin_amdgcn_exp2f(ap[0]), __builtin_amdgcn_exp2f(ap[1])};
      en[t][r] = (f32x2){__builtin_amdgcn_exp2f(an[0]), __builtin_amdgcn_exp2f(an[1])};
    }
    const f32x2 s2p = (ep[t][0] + ep[t][1]) + (ep[t][2] + ep[t][3]), s2n = (en[t][0] + en[t][1]) + (en[t][2] + en[t][3]);
    sp[t] = s2p[0] + s2p[1]; sn[t] = s2n[0] + s2n[1];
  }
  XRED4("v_add_f32", sp[0], sn[0], sp[1], sn[1]);
#pragma unroll
  for (int t = 0; t < 2; ++t) {
    const float rp = __builtin_amdgcn_rcpf(sp[t]), rn = __builtin_amdgcn_rcpf(sn[t]);
    const f32x2 rp2 = {rp, rp}, rn2 = {rn, rn};
#pragma unroll
    for (int r = 0; r < 4; ++r) {
      const f32x2 a = ep[t][r] * rp2, b = en[t][r] * rn2;
      pos[t][r] = pk2(a[0], a[1]); neg[t][r] = pk2(b[0], b[1]);
    }
  }
}

DI float sum8lanes(float v) {
  v += __builtin_bit_cast(float, __builtin_amdgcn_update_dpp(0, __builtin_bit_cast(int, v), 0xB1, 0xf, 0xf, true));
  v += __builtin_bit_cast(float, __builtin_amdgcn_update_dpp(0, __builtin_bit_cast(int, v), 0x4E, 0xf, 0xf, true));
  v += __builtin_bit_cast(float, __builtin_amdgcn_update_dpp(0, __builtin_bit_cast(int, v), 0x141, 0xf, 0xf, true));
  return v;
}
DI void wait_flags(const char* p, u64 tag) {
  for (unsigned spins = 0; spins < (1u << 16); ++spins) {
    if (__all(__hip_atomic_load((const gu64*)p, __ATOMIC_RELAXED, __HIP_MEMORY_SCOPE_AGENT) == tag)) break;
    __builtin_amdgcn_s_sleep(1);
  }
}

constexpr int G1_SLOT = 16384;
constexpr int G1_B = 65536, G1_X = 131072;
constexpr int G1_LDS = G1_X + 4096 + 2048;

DI f32x2 gelu2(f32x2 x) {
  const f32x2 c1 = {-0.10294324f, -0.10294324f}, c0 = {-2.3022082f, -2.3022082f}, one = {1.0f, 1.0f};
  const f32x2 a = x * __builtin_elementwise_fma(x * x, c1, c0);
  const f32x2 d = one + (f32x2){__builtin_amdgcn_exp2f(a[0]), __builtin_amdgcn_exp2f(a[1])};
  return x * (f32x2){__builtin_amdgcn_rcpf(d[0]), __builtin_amdgcn_rcpf(d[1])};
}

template <bool QKV>
DI u64 gemm1_body(const u16* __restrict__ x, const u16* __restrict__ W, int wcol0, int woff, int panel, int tcol,
                  const u16* __restrict__ b1, const u16* __restrict__ wphi, char* ws,
                  const u16* __restrict__ w2, const u16* __restrict__ wo, LAS char* lds) {
  char* xn = ws + WS_XN;
  char* act = ws + WS_ACT;
  char* kvx = ws + WS_KVX;
  constexpr int LDW = QKV ? 3 * D_ : F_;
  constexpr int NG = QKV ? 3 : 4;
  constexpr int NT = QKV ? 6 : 7;
  const int tid = threadIdx.x, lane = tid & 63;
  const int wid = __builtin_amdgcn_readfirstlane(tid >> 6);
  const int wr = wid >> 1, wc = wid & 1;
  const int g = lane >> 4, i16 = lane & 15, q4 = i16 >> 2, p4 = lane & 3;
  const unsigned ldsb = (unsigned)(size_t)lds;

  const char* abase = uniform_ptr(xn + (size_t)panel * 16 * NKG1 * 1024);
  const unsigned avoff = (unsigned)(wid * 2) * NKG1 * 1024 + lane * 16;
  const char* bbase = (const char*)W;
  unsigned bvoff[2]; unsigned bdst[2];
#pragma unroll
  for (int j = 0; j < 2; ++j) {
    int pb = QKV ? (wid + 8 * j) : (wid * 2 + j);
    if (QKV && pb >= 12) pb -= 8;
    const int kg = pb / NG, cg = pb % NG;
    const int r = lane >> 3, sw = ((r >> 1) & 1) | ((kg & 1) << 1);
    const int cc = (lane & 7) ^ (sw << 1);
    const int col = QKV ? (cg * D_ + wcol0) : (wcol0 + cg * 64);
    bvoff[j] = (unsigned)(((kg * 8 + r) * LDW + col + cc * 8) * 2);
    bdst[j] = ldsb + G1_B + pb * 1024;
  }
  const unsigned adst = ldsb + wid * 2048;

  LAS char* ard = opaque(lds + wr * 4096 + lane * 16);
  constexpr int NB = QKV ? 2 : 4;
  LAS char* brd[NB];
#pragma unroll
  for (int j = 0; j < NB; ++j) {
    const int n0 = QKV ? (wc * 32 + j * 16) : (woff + wc * 112 + j * 16);
    const int sw = (q4 >> 1) | ((g & 1) << 1);
    brd[j] = opaque(lds + G1_B + (g * NG + (n0 >> 6)) * 1024 + q4 * 128 + (((((n0 & 63) >> 3) ^ (sw << 1)) + (p4 >> 1)) << 4) + (p4 & 1) * 8);
  }

  f32x4 acc[4][NT];
#pragma unroll
  for (int m = 0; m < 4; ++m)
#pragma unroll
    for (int n = 0; n < NT; ++n) acc[m][n] = (f32x4){0.f, 0.f, 0.f, 0.f};

  bf16x8 fa[2][4], fb[2][NT];

  auto issue = [&](int q, int stage, int slot) {
    if (q < 2) glds16_sc1(avoff + q * NKG1 * 1024, abase + (size_t)stage * 1024, adst + q * 1024 + slot * G1_SLOT);
    else       glds16(bvoff[q - 2], bbase + (size_t)stage * 64 * LDW, bdst[q - 2] + slot * G1_SLOT);
  };
  auto rdA = [&](auto P, auto SL, int m) { fa[P.v][m] = lds_b128(ard + SL.v * G1_SLOT + m * 1024); };
  auto rdB = [&](auto P, auto SL, int n) { LAS char* p = brd[n % NB] + (n / NB) * 1024 + SL.v * G1_SLOT; fb[P.v][n] = cat8(lds_tr(p), lds_tr(p + 512)); };
  auto rdBh = [&](auto P, auto SL, int n, int h) {
    const bf16x4 v = lds_tr(brd[n % NB] + (n / NB) * 1024 + SL.v * G1_SLOT + h * 512);
    fb[P.v][n][4 * h] = v[0]; fb[P.v][n][4 * h + 1] = v[1]; fb[P.v][n][4 * h + 2] = v[2]; fb[P.v][n][4 * h + 3] = v[3];
  };
  auto step = [&](auto SC, auto VM, auto GL, auto RD, int s) {
    constexpr int P = SC.v & 1, SNv = (SC.v + 1) & 3;
    const IC<P ^ 1> Pn; const IC<SNv> SN;
    if constexpr (RD.v) { WAIT_V(VM.v); WAIT_L(0); BAR(); }
    SCHED();
#pragma unroll
    for (int i = 0; i < 4 * NT; ++i) {
      const int m = i & 3, n = i >> 2;
      if (QKV && n >= 4) acc[m][n] = MFMA16(fa[P][m], fb[P][n], acc[m][n]);
      else               acc[m][n] = MFMA16(fb[P][n], fa[P][m], acc[m][n]);
      if (i < 4)                { if constexpr (RD.v) rdA(Pn, SN, i); }
      else if (i < 4 + 2 * NT)  { if constexpr (RD.v) rdBh(Pn, SN, (i - 4) >> 1, (i - 4) & 1); }
      if (i % 4 == 3 && i < 16) { if constexpr (GL.v) issue(i / 4, s + 4, SC.v); }
      SCHED();
    }
  };

  const int slot = (QKV ? 0 : 16) + tcol;
  u64 tag;
  {
    const int r8 = lane >> 3, c8 = lane & 7;
    const int row = panel * 256 + slot * 8 + r8;
    const u16* src = x + (size_t)row * D_ + wid * 128 + c8 * 8;
    u32x4 v0, v1; u32x2 ep;
    asm volatile("global_load_dwordx4 %0, %3, off\n\tglobal_load_dwordx4 %1, %3, off offset:128\n\tglobal_load_dwordx2 %2, %4, off sc1"
                 : "=&v"(v0), "=&v"(v1), "=&v"(ep) : "v"(src), "v"(ws + WS_EPOCH) : "memory");
    SCHED();
    asm volatile("s_waitcnt vmcnt(0)" : "+v"(v0), "+v"(v1), "+v"(ep) :: "memory");
    SCHED();
    if (QKV) {
      const u32x4 fill = {~0u, ~0u, ~0u, ~0u};
      char* kp = kvx + (size_t)(panel * 16 + tcol) * KVX_TILE;
      st_sc1(kp + tid * 16, fill);
      st_sc1(kp + 8192 + i16 * 16, fill);
    }
    tag = (((u64)(unsigned)__builtin_amdgcn_readfirstlane(ep[1]) << 32) | (unsigned)__builtin_amdgcn_readfirstlane(ep[0])) + 1;
    f32x2 f[8];
#pragma unroll
    for (int i = 0; i < 4; ++i) { f[i] = (f32x2){blo(v0[i]), bhi(v0[i])}; f[4 + i] = (f32x2){blo(v1[i]), bhi(v1[i])}; }
    const f32x2 s2 = ((f[0] + f[1]) + (f[2] + f[3])) + ((f[4] + f[5]) + (f[6] + f[7]));
    const float mw = sum8lanes(s2[0] + s2[1]) * (1.0f / 128.f);
    f32x2 q2 = {0.f, 0.f};
#pragma unroll
    for (int i = 0; i < 8; ++i) { const f32x2 d = f[i] - (f32x2){mw, mw}; q2 = __builtin_elementwise_fma(d, d, q2); }
    const float qq = sum8lanes(q2[0] + q2[1]);
    LAS f32x2* stt = (LAS f32x2*)(lds + G1_X + 4096);
    if (c8 == 0) stt[r8 * 8 + wid] = (f32x2){mw, qq};
    WAIT_L(0); BAR();
    const f32x2 pw = stt[r8 * 8 + c8];
    const float mean = sum8lanes(pw[0]) * 0.125f;
    const float dm = pw[0] - mean;
    const float m2 = sum8lanes(__builtin_fmaf(128.f * dm, dm, pw[1]));
    const float mb = rbf(mean), vb = rbf(m2 * (1.0f / D_));
    const float rs = 1.0f / rbf(sqrtf(rbf(vb + 1e-5f)));
    u32x4 o0, o1;
#pragma unroll
    for (int i = 0; i < 8; ++i) {
      const f32x2 d = f[i] - (f32x2){mb, mb};
      unsigned u = pk2(d[0], d[1]);
      asm("" : "+v"(u));
      const f32x2 y = (f32x2){blo(u), bhi(u)} * (f32x2){rs, rs};
      if (i < 4) o0[i] = pk2(y[0], y[1]); else o1[i - 4] = pk2(y[0], y[1]);
    }
    char* dst = xn + ((size_t)((row >> 4) * NKG1 + wid * 4 + (c8 >> 2)) * 64 + (c8 & 3) * 16 + (row & 15)) * 16;
    st_sc1(dst, o0); st_sc1(dst + 2048, o1);
    WAIT_V(0); BAR();
    if (tid == 0) __hip_atomic_store((gu64*)(ws + WS_FLG + (panel * 32 + slot) * FS), tag, __ATOMIC_RELAXED, __HIP_MEMORY_SCOPE_AGENT);
    if (QKV && wid >= 4) glds16((unsigned)lane * 16, uniform_ptr((const char*)wphi + (size_t)tcol * 4096 + (wid - 4) * 1024), ldsb + G1_X + (wid - 4) * 1024);
    if (!QKV && wid == 4) glds16((unsigned)(lane < 28 ? lane : 27) * 16, uniform_ptr((const char*)(b1 + tcol * 224)), ldsb + G1_X);
#pragma unroll
    for (int st = 0; st < 4; ++st) { issue(2, st, st); issue(3, st, st); }
    if (wid == 0) wait_flags(ws + WS_FLG + ((QKV && lane >= 32) ? ((lane & 7) * 32 + slot) : (panel * 32 + (lane & 31))) * FS, tag);
    BAR();
  }
#pragma unroll
  for (int st = 0; st < 4; ++st) { issue(0, st, st); issue(1, st, st); }
  WAIT_V(2); BAR();
  {
    const IC<0> P0; const IC<0> S0;
#pragma unroll
    for (int m = 0; m < 4; ++m) rdA(P0, S0, m);
#pragma unroll
    for (int n = 0; n < NT; ++n) rdB(P0, S0, n);
  }
#pragma unroll 1
  for (int s = 0; s < 28; s += 4) {
    if (s == 8) {
      const int ln = __builtin_amdgcn_mbcnt_hi(~0u, __builtin_amdgcn_mbcnt_lo(~0u, 0u));
      char* xp = ws + WS_XCH + (size_t)blockIdx.x * 16384 + (wid * 64 + ln) * 16;
      const u32x4 fill = {~0u, ~0u, ~0u, ~0u};
      st_sc1(xp, fill); st_sc1(xp + 8192, fill);
    }
    step(IC<0>{}, IC<8>{}, IC<1>{}, IC<1>{}, s);
    step(IC<1>{}, IC<8>{}, IC<1>{}, IC<1>{}, s + 1);
    step(IC<2>{}, IC<8>{}, IC<1>{}, IC<1>{}, s + 2);
    step(IC<3>{}, IC<8>{}, IC<1>{}, IC<1>{}, s + 3);
  }
  step(IC<0>{}, IC<8>{}, IC<0>{}, IC<1>{}, 28);
  constexpr int NX = 1;
  {
    int wv = blockIdx.x * 8 + wid;
    if (wv >= 1152) wv -= 1152;
    const char* src = uniform_ptr(wv < 896 ? (const char*)w2 + (size_t)wv * 8192 : (const char*)wo + (size_t)(wv - 896) * 8192);
    asm volatile("s_mov_b32 m0, %2\n\ts_nop 0\n\tglobal_load_lds_dword %0, %1" ::"v"(lane * 128), "s"(src), "s"(ldsb + G1_X + 4096 + wid * 256) : "memory");
  }
  step(IC<1>{}, IC<4 + NX>{}, IC<0>{}, IC<1>{}, 29);
  step(IC<2>{}, IC<0 + NX>{}, IC<0>{}, IC<1>{}, 30);
  step(IC<3>{}, IC<0>{}, IC<0>{}, IC<0>{}, 31);

  if (!QKV) {
    u32x2 bb[NT];
#pragma unroll
    for (int n = 0; n < NT; ++n) bb[n] = *(LAS u32x2*)(lds + G1_X + (wc * 112 + 4 * g + n * 16) * 2);
    auto act4 = [&](int m, int n) {
      const f32x2 yA = gelu2((f32x2){acc[m][n][0] + blo(bb[n][0]), acc[m][n][1] + bhi(bb[n][0])});
      const f32x2 yB = gelu2((f32x2){acc[m][n][2] + blo(bb[n][1]), acc[m][n][3] + bhi(bb[n][1])});
      return (u32x2){pk2(yA[0], yA[1]), pk2(yB[0], yB[1])};
    };
    const int rt0 = panel * 16 + wr * 4;
    char* obase = (char*)act + ((size_t)rt0 * NKG2 + tcol * 7) * 1024 + lane * 16;
    if (wc == 0) {
#pragma unroll
      for (int m = 0; m < 4; ++m) {
        char* rb = obase + (size_t)m * NKG2 * 1024;
#pragma unroll
        for (int p = 0; p < 3; ++p) { const u32x2 lo = act4(m, 2 * p), hi = act4(m, 2 * p + 1); st_sc1(rb + p * 1024, (u32x4){lo[0], lo[1], hi[0], hi[1]}); }
        st8_sc1(rb + 3 * 1024, act4(m, 6 % NT));
      }
    } else {
#pragma unroll
      for (int m = 0; m < 4; ++m) {
        char* rb = obase + (size_t)m * NKG2 * 1024;
        st8_sc1(rb + 3 * 1024 + 8, act4(m, 0));
#pragma unroll
        for (int p = 0; p < 3; ++p) { const u32x2 lo = act4(m, 2 * p + 1), hi = act4(m, (2 * p + 2) % NT); st_sc1(rb + (4 + p) * 1024, (u32x4){lo[0], lo[1], hi[0], hi[1]}); }
      }
    }
  } else {
    const int head = tcol * 2 + wc;
    LAS char* wp = lds + G1_X + wc * 2048;
    bf16x8 wf[2];
#pragma unroll
    for (int ft = 0; ft < 2; ++ft) {
      LAS char* p = wp + (4 * g + q4) * 64 + (16 * ft + 4 * p4) * 2;
      wf[ft] = cat8(lds_tr(p), lds_tr(p + 16 * 64));
    }
    const f32x4 z4 = {0.f, 0.f, 0.f, 0.f};
    LAS char* sc = lds + (wid >> 2) * G1_B + (wid & 3) * 8704;
#pragma unroll
    for (int mp = 0; mp < 2; ++mp) {
      f32x4 pp[2][2]; u32x4 pos[2], neg[2];
#pragma unroll
      for (int t = 0; t < 2; ++t) {
        const bf16x8 bk = pack8(acc[2 * mp + t][2], acc[2 * mp + t][3]);
        pp[t][0] = MFMA16(wf[0], bk, z4); pp[t][1] = MFMA16(wf[1], bk, z4);
      }
      dual_softmax2(pp, pos, neg);
#pragma unroll
      for (int t = 0; t < 2; ++t) {
        LAS char* rowp = sc + (16 * (2 * mp + t) + i16) * 136 + 8 * g;
        *(LAS u32x2*)(rowp) = (u32x2){pos[t][0], pos[t][1]};      *(LAS u32x2*)(rowp + 32) = (u32x2){pos[t][2], pos[t][3]};
        *(LAS u32x2*)(rowp + 64) = (u32x2){neg[t][0], neg[t][1]}; *(LAS u32x2*)(rowp + 96) = (u32x2){neg[t][2], neg[t][3]};
      }
    }
    WAIT_L(0);
    f32x4 kv[4][3];
#pragma unroll
    for (int ft = 0; ft < 4; ++ft)
#pragma unroll
      for (int d = 0; d < 3; ++d) kv[ft][d] = z4;
    bf16x8 ones;
#pragma unroll
    for (int j = 0; j < 8; ++j) ones[j] = (__bf16)1.0f;
#pragma unroll
    for (int ks = 0; ks < 2; ++ks) {
      const bf16x8 bv0 = pack8(acc[2 * ks][4], acc[2 * ks + 1][4]);
      const bf16x8 bv1 = pack8(acc[2 * ks][5], acc[2 * ks + 1][5]);
#pragma unroll
      for (int ft = 0; ft < 4; ++ft) {
        LAS char* p = sc + (32 * ks + 4 * g + q4) * 136 + (16 * ft + 4 * p4) * 2;
        const bf16x8 a = cat8(lds_tr(p), lds_tr(p + 16 * 136));
        kv[ft][0] = MFMA16(a, bv0, kv[ft][0]);
        kv[ft][1] = MFMA16(a, bv1, kv[ft][1]);
        kv[ft][2] = MFMA16(a, ones, kv[ft][2]);
      }
    }
    WAIT_L(0); BAR();
    {
      LAS char* pp = lds + wid * 8448;
#pragma unroll
      for (int sg = 0; sg < 2; ++sg) {
#pragma unroll
        for (int d = 0; d < 2; ++d) {
          *(LAS f32x4*)(pp + (sg * 2 + d) * 2048 + lane * 16) = kv[2 * sg][d];
          *(LAS f32x4*)(pp + (sg * 2 + d) * 2048 + 1024 + lane * 16) = kv[2 * sg + 1][d];
        }
        if (i16 == 0) {
          *(LAS f32x4*)(pp + 8192 + sg * 128 + g * 16) = kv[2 * sg][2];
          *(LAS f32x4*)(pp + 8192 + sg * 128 + 64 + g * 16) = kv[2 * sg + 1][2];
        }
      }
    }
    WAIT_L(0); BAR();
    char* kvt = kvx + (size_t)tcol * KVX_TILE;
    char* kvmine = kvt + (size_t)panel * 16 * KVX_TILE;
    {
      LAS char* pp = lds + (tid >> 8) * 8448 + ((tid >> 6) & 3) * 2048 + lane * 16;
      f32x4 lo = *(LAS f32x4*)(pp), hi = *(LAS f32x4*)(pp + 1024);
#pragma unroll
      for (int w = 1; w < 4; ++w) { lo += *(LAS f32x4*)(pp + w * 16896); hi += *(LAS f32x4*)(pp + w * 16896 + 1024); }
      st_sc1(kvmine + tid * 16, (u32x4){pk2(lo[0], lo[1]), pk2(lo[2], lo[3]), pk2(hi[0], hi[1]), pk2(hi[2], hi[3])});
    }
    if (tid < 16) {
      LAS char* pp = lds + (tid >> 3) * 8448 + 8192 + ((tid >> 2) & 1) * 128 + (tid & 3) * 16;
      f32x4 lo = *(LAS f32x4*)(pp), hi = *(LAS f32x4*)(pp + 64);
#pragma unroll
      for (int w = 1; w < 4; ++w) { lo += *(LAS f32x4*)(pp + w * 16896); hi += *(LAS f32x4*)(pp + w * 16896 + 64); }
      st_sc1(kvmine + 8192 + tid * 16, (u32x4){pk2(lo[0], lo[1]), pk2(lo[2], lo[3]), pk2(hi[0], hi[1]), pk2(hi[2], hi[3])});
    }
    u32x4 qp[2][2], qn[2][2];
    auto qsm = [&](int mp) {
      f32x4 pp[2][2];
#pragma unroll
      for (int t = 0; t < 2; ++t) {
        const bf16x8 bq = pack8(acc[2 * mp + t][0], acc[2 * mp + t][1]);
        pp[t][0] = MFMA16(wf[0], bq, z4); pp[t][1] = MFMA16(wf[1], bq, z4);
      }
      dual_softmax2(pp, qp[mp], qn[mp]);
    };
    qsm(0);
    const char* kb0 = uniform_ptr(kvt), * kb1 = uniform_ptr(kvt + (size_t)32 * KVX_TILE);
    const char* kb2 = uniform_ptr(kvt + (size_t)64 * KVX_TILE), * kb3 = uniform_ptr(kvt + (size_t)96 * KVX_TILE);
    u32x4 pv[8], pz[8];
#define KV_GATHER(pv, OFF, TAIL) asm volatile( \
          "global_load_dwordx4 %0, %8, %10 sc1\n\tglobal_load_dwordx4 %1, %9, %10 sc1\n\t" \
          "global_load_dwordx4 %2, %8, %11 sc1\n\tglobal_load_dwordx4 %3, %9, %11 sc1\n\t" \
          "global_load_dwordx4 %4, %8, %12 sc1\n\tglobal_load_dwordx4 %5, %9, %12 sc1\n\t" \
          "global_load_dwordx4 %6, %8, %13 sc1\n\tglobal_load_dwordx4 %7, %9, %13 sc1" TAIL \
          : "=&v"(pv[0]), "=&v"(pv[1]), "=&v"(pv[2]), "=&v"(pv[3]), "=&v"(pv[4]), "=&v"(pv[5]), "=&v"(pv[6]), "=&v"(pv[7]) \
          : "v"(OFF), "v"((OFF) + 16 * KVX_TILE), "s"(kb0), "s"(kb1), "s"(kb2), "s"(kb3) : "memory")
#define KV_WAIT(pv) asm volatile("s_waitcnt vmcnt(0)" \
          : "+v"(pv[0]), "+v"(pv[1]), "+v"(pv[2]), "+v"(pv[3]), "+v"(pv[4]), "+v"(pv[5]), "+v"(pv[6]), "+v"(pv[7]) :: "memory")
    LAS char* kvs = lds + 98304;
    auto finish = [&](u32x4 (&pv)[8], unsigned off) {
      for (unsigned spins = 0;; ++spins) {
        unsigned m = 0;
#pragma unroll
        for (int p = 0; p < 8; ++p) m = umax4(pv[p], m);
        if (__all(m != 0xffffffffu) || spins > (1u << 14)) break;
        __builtin_amdgcn_s_sleep(2);
        KV_GATHER(pv, off, "\n\ts_waitcnt vmcnt(0)");
      }
      f32x2 t[4];
#pragma unroll
      for (int r = 0; r < 4; ++r) t[r] = (f32x2){blo(pv[0][r]), bhi(pv[0][r])};
#pragma unroll
      for (int p = 1; p < 8; ++p)
#pragma unroll
        for (int r = 0; r < 4; ++r) t[r] += (f32x2){blo(pv[p][r]), bhi(pv[p][r])};
      *(LAS u32x4*)(kvs + off) = (u32x4){pk2(t[0][0], t[0][1]), pk2(t[1][0], t[1][1]), pk2(t[2][0], t[2][1]), pk2(t[3][0], t[3][1])};
    };
    const unsigned go = (unsigned)tid * 16, gz = 8192u + (unsigned)i16 * 16;
    SCHED();
    KV_GATHER(pv, go, "");
    KV_GATHER(pz, gz, "");
    SCHED();
    qsm(1);
    SCHED();
    KV_WAIT(pv);
    KV_WAIT(pz);
    SCHED();
    finish(pv, go);
    if (wid == 0) finish(pz, gz);
    WAIT_L(0); BAR();
    bf16x8 a[2][3];
#pragma unroll
    for (int sg = 0; sg < 2; ++sg) {
#pragma unroll
      for (int d = 0; d < 2; ++d) a[sg][d] = lds_b128(kvs + wc * 4096 + (sg * 2 + d) * 1024 + lane * 16);
      a[sg][2] = lds_b128(kvs + 8192 + wc * 128 + sg * 64 + g * 16);
    }
#pragma unroll
    for (int m = 0; m < 4; ++m) {
      const bf16x8 bp = __builtin_bit_cast(bf16x8, qp[m >> 1][m & 1]), bn = __builtin_bit_cast(bf16x8, qn[m >> 1][m & 1]);
      f32x4 o0 = MFMA16(a[0][0], bp, z4), o1 = MFMA16(a[0][1], bp, z4), dn = MFMA16(a[0][2], bp, z4);
      o0 = MFMA16(a[1][0], bn, o0); o1 = MFMA16(a[1][1], bn, o1); dn = MFMA16(a[1][2], bn, dn);
      const float rd = __builtin_amdgcn_rcpf(dn[0] + 1e-6f);
      const int rt = panel * 16 + wr * 4 + m;
      u32x4 w;
      w[0] = pk2(o0[0] * rd, o0[1] * rd); w[1] = pk2(o0[2] * rd, o0[3] * rd);
      w[2] = pk2(o1[0] * rd, o1[1] * rd); w[3] = pk2(o1[2] * rd, o1[3] * rd);
      st_sc1((char*)act + ((size_t)rt * NKG2 + 112 + head) * 1024 + lane * 16, w);
    }
  }
  return tag;
}

constexpr int G2_SLOT = 32768;
constexpr int G2_POLL = 4 * G2_SLOT + 4096;
constexpr int KSPLIT = 36;

DI void gemm2_body(char* ws, const u16* __restrict__ w2, const u16* __restrict__ wo,
                   const u16* __restrict__ x, const u16* __restrict__ b2, u16* __restrict__ out,
                   u64 tag, int slot, int split, int panel, int pm, int pn, LAS char* lds) {
  const char* act = ws + WS_ACT;
  char* xch = ws + WS_XCH;
  char* flg2 = ws + WS_FLG2 + panel * 32 * FS;
  const unsigned ldsb = (unsigned)(size_t)lds;
  int lane = __builtin_amdgcn_mbcnt_hi(~0u, __builtin_amdgcn_mbcnt_lo(~0u, 0u));
  asm volatile("" : "+v"(lane));
  const int wid = __builtin_amdgcn_readfirstlane(threadIdx.x >> 6);
  const int kh = wid >> 2, wr = (wid >> 1) & 1, wc = wid & 1;
  const int g = lane >> 4, i16 = lane & 15, q4 = i16 >> 2, p4 = lane & 3;
  const int b = blockIdx.x;
  const int kt0 = split ? KSPLIT : 0, nst = split ? 72 - KSPLIT : KSPLIT;
  const char* abase = uniform_ptr(act + ((size_t)(pm * 8) * NKG2 + kt0 * 2) * 1024);
  const unsigned avoff = (unsigned)wid * NKG2 * 1024 + lane * 16;
  const unsigned adst = ldsb + wid * 1024;
  const int r8 = lane >> 3;
  const unsigned bvoff = (unsigned)((wid * 8 + r8) * D_ + pn * 128 + (((lane & 7) ^ (((r8 >> 1) & 3) << 1)) << 3)) * 2;
  const unsigned bdst = ldsb + 16384 + wid * 2048;
  const char* wb = uniform_ptr((const char*)w2 + (size_t)kt0 * 64 * D_ * 2);
  auto issue = [&](int q, int t, int slot) {
    if (q < 2) glds16_sc1(avoff + q * 1024, abase + (size_t)t * 2048, adst + q * 8192 + slot * G2_SLOT);
    else       glds16(bvoff + (q - 2) * 128, wb + (size_t)t * 64 * D_ * 2, bdst + (q - 2) * 1024 + slot * G2_SLOT);
  };
  WAIT_V(0); WAIT_L(0); BAR();
  if (threadIdx.x == 0) __hip_atomic_store((gu64*)(flg2 + slot * FS), tag, __ATOMIC_RELAXED, __HIP_MEMORY_SCOPE_AGENT);
  if (wid == panel) wait_flags(flg2 + (16 + (split ? 10 + (i16 < 6 ? i16 : 0) : (i16 < 11 ? i16 : 0))) * FS, tag);
#pragma unroll
  for (int st = 0; st < 4; ++st) { issue(2, st, st); issue(3, st, st); }
  BAR();
#pragma unroll
  for (int st = 0; st < 4; ++st) { issue(0, st, st); issue(1, st, st); }

  LAS char* ard[2];
  ard[0] = opaque(lds + kh * 8192 + wr * 4096 + lane * 16); ard[1] = opaque(ard[0] + 65536);
  LAS char* brd[2][4];
#pragma unroll
  for (int j = 0; j < 4; ++j) {
    const int r = 4 * (g & 1) + q4, sw = (r >> 1) & 3;
    brd[0][j] = opaque(lds + 16384 + ((kh * 4 + (g >> 1)) * 2 + wc) * 1024 + r * 128 + ((((2 * j) ^ (sw << 1)) + (p4 >> 1)) << 4) + (p4 & 1) * 8);
    brd[1][j] = opaque(brd[0][j] + 65536);
  }
  f32x4 acc[4][4];
#pragma unroll
  for (int m = 0; m < 4; ++m)
#pragma unroll
    for (int n = 0; n < 4; ++n) acc[m][n] = (f32x4){0.f, 0.f, 0.f, 0.f};
  bf16x8 fa[2][4], fb[2][4];
  auto rdA = [&](auto P, auto SL, int m) { fa[P.v][m] = lds_b128(ard[SL.v >> 1] + (SL.v & 1) * G2_SLOT + m * 1024); };
  auto rdB = [&](auto P, auto SL, int n) { LAS char* p = brd[SL.v >> 1][n] + (SL.v & 1) * G2_SLOT; fb[P.v][n] = cat8(lds_tr(p), lds_tr(p + 4096)); };
  auto rdBh = [&](auto P, auto SL, int n, int h) {
    const bf16x4 v = lds_tr(brd[SL.v >> 1][n] + (SL.v & 1) * G2_SLOT + h * 4096);
    fb[P.v][n][4 * h] = v[0]; fb[P.v][n][4 * h + 1] = v[1]; fb[P.v][n][4 * h + 2] = v[2]; fb[P.v][n][4 * h + 3] = v[3];
  };
  auto step = [&](auto SC, auto VM, auto GL, auto RD, int t) {
    constexpr int P = SC.v & 1, SNv = (SC.v + 1) & 3;
    const IC<P ^ 1> Pn; const IC<SNv> SN;
    if constexpr (RD.v) { WAIT_V(VM.v); WAIT_L(0); BAR(); }
    SCHED();
#pragma unroll
    for (int i = 0; i < 16; ++i) {
      acc[i & 3][i >> 2] = MFMA16(fb[P][i >> 2], fa[P][i & 3], acc[i & 3][i >> 2]);
      if (i < 4)       { if constexpr (RD.v) rdA(Pn, SN, i); }
      else if (i < 12) { if constexpr (RD.v) rdBh(Pn, SN, (i - 4) >> 1, (i - 4) & 1); }
      if (i % 3 == 2 && i < 12) { if constexpr (GL.v) issue(i / 3, t + 4, SC.v); }
      SCHED();
    }
  };
  WAIT_V(2); BAR();
  {
    const IC<0> P0; const IC<0> S0;
#pragma unroll
    for (int m = 0; m < 4; ++m) rdA(P0, S0, m);
#pragma unroll
    for (int n = 0; n < 4; ++n) rdB(P0, S0, n);
  }
  const IC<1> Y; const IC<0> N_; const IC<8> V8;
  const int tp = split ? 8 : 24;
#pragma unroll 1
  for (int t = 0; t < nst - 4; t += 4) {
    if (wid == 0) {
      if (t == tp) {
        asm volatile("s_mov_b32 m0, %2\n\ts_nop 0\n\tglobal_load_lds_dword %0, %1 sc1" ::"v"((lane >> 1) * FS + (lane & 1) * 4), "s"(uniform_ptr(flg2)), "s"(ldsb + G2_POLL) : "memory");
      } else if (t == tp + 4) {
        if (!__all(*(LAS u64*)(lds + G2_POLL + (lane & 31) * 8) == tag)) wait_flags(flg2 + (lane & 31) * FS, tag);
      }
    }
    if (t == 52 - KSPLIT && split == 1) wb = uniform_ptr((const char*)wo - (size_t)(56 - KSPLIT) * 64 * D_ * 2);
    step(IC<0>{}, V8, Y, Y, t); step(IC<1>{}, V8, Y, Y, t + 1); step(IC<2>{}, V8, Y, Y, t + 2); step(IC<3>{}, V8, Y, Y, t + 3);
  }
  step(IC<0>{}, V8, N_, Y, 0);
  step(IC<1>{}, IC<4>{}, N_, Y, 0);
  step(IC<2>{}, IC<0>{}, N_, Y, 0);
  step(IC<3>{}, IC<0>{}, N_, N_, 0);
  const int quad = wid & 3;
  {
    LAS char* pp = lds + (quad * 2 + kh) * 8192 + lane * 16;
#pragma unroll
    for (int mm = 0; mm < 2; ++mm)
#pragma unroll
      for (int n = 0; n < 4; ++n) *(LAS f32x4*)(pp + (mm * 4 + n) * 1024) = kh ? acc[mm][n] : acc[2 + mm][n];
  }
  const bool keep = (wr == split);
  const int row0 = pm * 128 + wr * 64 + kh * 32 + i16, col0 = pn * 128 + wc * 64 + 4 * g;
  u32x2 xv[2][4], bv[4];
  if (keep) {
#pragma unroll
    for (int n = 0; n < 4; ++n) bv[n] = *(const u32x2*)(b2 + col0 + n * 16);
#pragma unroll
    for (int mm = 0; mm < 2; ++mm)
#pragma unroll
      for (int n = 0; n < 4; ++n) xv[mm][n] = *(const u32x2*)(x + (size_t)(row0 + mm * 16) * D_ + col0 + n * 16);
  }
  WAIT_L(0); BAR();
  f32x4 sum[2][4];
  {
    LAS char* pp = lds + (quad * 2 + (kh ^ 1)) * 8192 + lane * 16;
#pragma unroll
    for (int mm = 0; mm < 2; ++mm)
#pragma unroll
      for (int n = 0; n < 4; ++n) sum[mm][n] = (kh ? acc[2 + mm][n] : acc[mm][n]) + *(LAS f32x4*)(pp + (mm * 4 + n) * 1024);
  }
  const int piece = kh * 2 + wc;
  if (!keep) {
    char* dst = xch + (size_t)b * 16384 + piece * 4096 + lane * 16;
#pragma unroll
    for (int mm = 0; mm < 2; ++mm)
#pragma unroll
      for (int h = 0; h < 2; ++h)
        st_sc1(dst + (mm * 2 + h) * 1024, (u32x4){pk2(sum[mm][2 * h][0], sum[mm][2 * h][1]), pk2(sum[mm][2 * h][2], sum[mm][2 * h][3]),
                                                  pk2(sum[mm][2 * h + 1][0], sum[mm][2 * h + 1][1]), pk2(sum[mm][2 * h + 1][2], sum[mm][2 * h + 1][3])});
    if (b == 0 && wid == 0) {
#pragma unroll
      for (int j = 0; j < 4; ++j) wait_flags(ws + WS_FLG + (j * 64 + lane) * FS, tag);
      if (lane == 0) __hip_atomic_store((gu64*)(ws + WS_EPOCH), tag, __ATOMIC_RELAXED, __HIP_MEMORY_SCOPE_AGENT);
    }
  } else {
    const char* src = xch + (size_t)(b ^ 1) * 16384 + piece * 4096 + lane * 16;
    u32x4 pv[4];
    for (unsigned spins = 0;; ++spins) {
      asm volatile(
          "global_load_dwordx4 %0, %4, off sc1\n\tglobal_load_dwordx4 %1, %4, off offset:1024 sc1\n\t"
          "global_load_dwordx4 %2, %4, off offset:2048 sc1\n\tglobal_load_dwordx4 %3, %4, off offset:3072 sc1\n\ts_waitcnt vmcnt(0)"
          : "=&v"(pv[0]), "=&v"(pv[1]), "=&v"(pv[2]), "=&v"(pv[3]) : "v"(src) : "memory");
      if (__all(fresh4(pv[0]) && fresh4(pv[1]) && fresh4(pv[2]) && fresh4(pv[3])) || spins > (1u << 14)) break;
      __builtin_amdgcn_s_sleep(1);
    }
#pragma unroll
    for (int mm = 0; mm < 2; ++mm)
#pragma unroll
      for (int n = 0; n < 4; ++n) {
        const unsigned q0 = pv[mm * 2 + (n >> 1)][2 * (n & 1)], q1 = pv[mm * 2 + (n >> 1)][2 * (n & 1) + 1];
        const f32x4 v = sum[mm][n];
        u32x2 o;
        o[0] = pk2(v[0] + blo(q0) + blo(xv[mm][n][0]) + blo(bv[n][0]), v[1] + bhi(q0) + bhi(xv[mm][n][0]) + bhi(bv[n][0]));
        o[1] = pk2(v[2] + blo(q1) + blo(xv[mm][n][1]) + blo(bv[n][1]), v[3] + bhi(q1) + bhi(xv[mm][n][1]) + bhi(bv[n][1]));
        *(u32x2*)(out + (size_t)(row0 + mm * 16) * D_ + col0 + n * 16) = o;
      }
  }
}

__global__ __launch_bounds__(512, 2) void k_main(const u16* __restrict__ x, const u16* __restrict__ wqkv, const u16* __restrict__ w1,
                                                 const u16* __restrict__ b1, const u16* __restrict__ wphi,
                                                 const u16* __restrict__ w2, const u16* __restrict__ wo, const u16* __restrict__ b2,
                                                 char* ws, u16* __restrict__ out) {
  extern __shared__ __attribute__((aligned(16))) char smem[];
  LAS char* lds = (LAS char*)smem;
  const int b = blockIdx.x, grp = b & 7, idx = b >> 3;
  const int panel = idx & 7, kind = grp & 1, tcol = (grp >> 1) * 4 + (idx >> 3);
  u64 tag;
  if (kind == 0) {
    tag = gemm1_body<true>(x, wqkv, tcol * 64, 0, panel, tcol, b1, wphi, ws, w2, wo, lds);
  } else {
    const int c0 = tcol * 224, woff = c0 & 63;
    tag = gemm1_body<false>(x, w1, c0 - woff, woff, panel, tcol, b1, wphi, ws, w2, wo, lds);
  }
  gemm2_body(ws, w2, wo, x, b2, out, tag, kind * 16 + tcol,   kind ^ 1, panel, panel * 2 + ((grp >> 1) & 1), (grp >> 2) * 4 + (idx >> 3), lds);
}

extern "C" void kernel_launch(void* const* d_in, const int* in_sizes, int n_in, void* d_out, int out_size, void* d_ws, size_t ws_size,
                              hipStream_t stream) {
  (void)hipFuncSetAttribute((const void*)k_main, hipFuncAttributeMaxDynamicSharedMemorySize, G1_LDS);
  k_main<<<256, 512, G1_LDS, stream>>>((const u16*)d_in[0], (const u16*)d_in[1], (const u16*)d_in[5], (const u16*)d_in[6], (const u16*)d_in[2],
                                       (const u16*)d_in[7], (const u16*)d_in[3], (const u16*)d_in[8], (char*)d_ws, (u16*)d_out);
}
```
